```python
import torch

def _softmax(x, axis=-1):
    return torch.softmax(x, dim=axis)
def _gelu(x):
    return torch.nn.functional.gelu(x, approximate="tanh")
def _silu(x):
    return torch.nn.functional.silu(x)
def _sigmoid(x):
    return torch.sigmoid(x)
def _layernorm(x, axis=-1, eps=1e-5):
    m = x.mean(dim=axis, keepdim=True)
    v = x.var(dim=axis, keepdim=True, unbiased=False)
    return (x - m) / torch.sqrt(v + eps)
def _scatter_add(tgt, idx, src, axis=0):
    return tgt.clone().index_add_(axis, idx, src)
def _segment_sum(src, seg, n):
    out = torch.zeros((n,) + src.shape[1:], dtype=src.dtype)
    return out.index_add_(0, seg, src)
def _pad(x, pw):
    flat = tuple(v for pair in reversed(pw) for v in pair)
    return torch.nn.functional.pad(x, flat)

kernel_name = 'sequential_preLN__ssm_conv_s4__mixer_token_mlp__prompt'

def setup_inputs():

    _w = (lambda a: (lambda t: t.to(torch.bfloat16) if t.is_floating_point() else t)(torch.as_tensor(a)))
    def _A_setup():
        B, L, D, N = 1, 2048, 512, 128
        A_re = -(lambda _x: torch.exp(torch.as_tensor(_x)))((lambda *s: torch.randn(*s, dtype=torch.bfloat16) * 6.4)(D, N))
        A_im = torch.pi * (lambda *s: torch.randn(*s, dtype=torch.bfloat16) * 0.05)(D, N)
        B_re = (lambda *s: torch.randn(*s, dtype=torch.bfloat16) * 0.1)(D, N)
        B_im = (lambda *s: torch.randn(*s, dtype=torch.bfloat16) * 0.1)(D, N)
        C_re = (lambda *s: torch.randn(*s, dtype=torch.bfloat16) * 0.1)(D, N)
        C_im = (lambda *s: torch.randn(*s, dtype=torch.bfloat16) * 0.1)(D, N)
        log_dt = (lambda *s: torch.randn(*s, dtype=torch.bfloat16) * 0.05)(D) * 0.5 - 4.0
        Dskip = (lambda *s: torch.randn(*s, dtype=torch.bfloat16) * 0.05)(D)
        return {
            "u": _w((lambda *s: torch.randn(*s, dtype=torch.bfloat16) * 0.05)(B, L, D)),
            "A_re": _w(A_re),
            "A_im": _w(A_im),
            "B_re": _w(B_re),
            "B_im": _w(B_im),
            "C_re": _w(C_re),
            "C_im": _w(C_im),
            "log_dt": _w(log_dt),
            "Dskip": _w(Dskip),
        }
    _A_in = _A_setup()

    def _B_setup():
        B, S, D, F = 1, 2048, 512, 1792
        return {
            "x": _w((lambda *s: torch.randn(*s, dtype=torch.bfloat16) * 0.05)(B, S, D)),
            "w1": _w((lambda *s: torch.randn(*s, dtype=torch.bfloat16) * 0.4)(S, F) * (S ** -0.5)),
            "b1": _w((lambda *s: torch.randn(*s, dtype=torch.bfloat16) * 0.1)(F) * 0.02),
            "w2": _w((lambda *s: torch.randn(*s, dtype=torch.bfloat16) * 0.1)(F, S) * (F ** -0.5)),
            "b2": _w((lambda *s: torch.randn(*s, dtype=torch.bfloat16) * 0.1)(S) * 0.02),
        }
    _B_in = _B_setup()

    return {
        "A_u": _A_in["u"],
        "A_A_re": _A_in["A_re"],
        "A_A_im": _A_in["A_im"],
        "A_B_re": _A_in["B_re"],
        "A_B_im": _A_in["B_im"],
        "A_C_re": _A_in["C_re"],
        "A_C_im": _A_in["C_im"],
        "A_log_dt": _A_in["log_dt"],
        "A_Dskip": _A_in["Dskip"],
        "B_x": _B_in["x"],
        "B_w1": _B_in["w1"],
        "B_b1": _B_in["b1"],
        "B_w2": _B_in["w2"],
        "B_b2": _B_in["b2"],
    }

def reference(A_u, A_A_re, A_A_im, A_B_re, A_B_im, A_C_re, A_C_im, A_log_dt, A_Dskip, B_x, B_w1, B_b1, B_w2, B_b2):
    def _A_ref(u, A_re, A_im, B_re, B_im, C_re, C_im, log_dt, Dskip):
        Bsz, L, D = u.shape
        dt = torch.exp(log_dt)[:, None]
        A = A_re + 1j * A_im
        Bc = B_re + 1j * B_im
        Cc = C_re + 1j * C_im
        logdA = dt * A
        dB = dt * Bc
        l = (lambda x, dt: x.to(dt))(torch.arange(L), A_re.dtype)
        P = torch.exp(logdA[:, :, None] * l[None, None, :])
        K = torch.einsum("dn,dnl,dn->dl", Cc, P, dB).real
        L2 = 2 * L
        Kp = _pad(K, ((0, 0), (0, L)))
        up = (lambda x, *d: x.permute(*d))(u, 0, 2, 1)
        up = _pad(up, ((0, 0), (0, 0), (0, L)))
        Kf = (lambda x, axes=(-2,-1): torch.fft.rfft2(x.float(), dim=axes))(Kp[:, None, :], axes=(-2, -1))
        Uf = (lambda x, axes=(-2,-1): torch.fft.rfft2(x.float(), dim=axes))(up[:, :, None, :], axes=(-2, -1))
        Yf = Uf * Kf[None, :, :, :]
        y = (lambda x, s, axes=(-2,-1): torch.fft.irfft2(x, s=s, dim=axes).to(torch.bfloat16))(Yf, (1, L2), axes=(-2, -1))[:, :, 0, :L]
        y = (lambda x, *d: x.permute(*d))(y, 0, 2, 1) + u * Dskip[None, None, :]
        return y

    def _B_ref(x, w1, b1, w2, b2):
        B, S, D = x.shape
        h = _layernorm(x, axis=-1)
        u = torch.einsum("bsd,sf->bdf", h, w1) + b1
        u = _gelu(u)
        y = torch.einsum("bdf,fs->bds", u, w2) + b2
        return (lambda x, *d: x.permute(*d))(y, 0, 2, 1)

    x = A_u
    h = x + _A_ref(_layernorm(x, axis=-1), A_A_re, A_A_im, A_B_re, A_B_im, A_C_re, A_C_im, A_log_dt, A_Dskip)
    h2 = h + _B_ref(_layernorm(h, axis=-1), B_w1, B_b1, B_w2, B_b2)
    return h2
```

```cpp
#include <hip/hip_runtime.h>
#include <hip/hip_bf16.h>
#define ROUND_L 1
#define ROUND_DT 0
typedef unsigned short u16;
__device__ __forceinline__ float b2f(u16 v){ return __uint_as_float(((unsigned)v)<<16); }
__device__ __forceinline__ u16 f2b(float f){ unsigned u=__float_as_uint(f); return (u16)((u + 0x7FFF + ((u>>16)&1))>>16); }
constexpr int L=2048, D=512, N=128, F=1792;

__global__ void k_ln(const u16* x, float* u){
  int s=blockIdx.x, t=threadIdx.x; float v[8]; float sum=0;
  for(int i=0;i<8;i++){ v[i]=b2f(x[s*D+t*8+i]); sum+=v[i]; }
  for(int o=32;o;o>>=1) sum+=__shfl_xor(sum,o);
  float m=sum/D, q=0; for(int i=0;i<8;i++){ q+=(v[i]-m)*(v[i]-m);}
  for(int o=32;o;o>>=1) q+=__shfl_xor(q,o);
  float r=rsqrtf(q/D+1e-5f);
  for(int i=0;i<8;i++) u[s*D+t*8+i]=(v[i]-m)*r;
}
__global__ void k_K(const u16* Are,const u16* Aim,const u16* Bre,const u16* Bim,const u16* Cre,const u16* Cim,const u16* logdt,float* K){
  int d=blockIdx.y; int l=blockIdx.x*256+threadIdx.x;
  float dt=expf(b2f(logdt[d])); if(ROUND_DT) dt=b2f(f2b(dt));
  float lf=(float)l; if(ROUND_L) lf=b2f(f2b(lf));
  float acc=0;
  for(int n=0;n<N;n++){ int i=d*N+n; float a=dt*b2f(Are[i]), b=dt*b2f(Aim[i]);
    float br=dt*b2f(Bre[i]), bi=dt*b2f(Bim[i]), cr=b2f(Cre[i]), ci=b2f(Cim[i]);
    float wr=cr*br-ci*bi, wi=cr*bi+ci*br; float e=expf(a*lf); float s,c; sincosf(b*lf,&s,&c);
    acc+=e*(wr*c-wi*s);}
  K[d*L+l]=acc;
}
__global__ void k_conv(const u16* x,const float* u,const float* K,const u16* Dk,float* h){
  int t=blockIdx.x; int d=threadIdx.x; float acc=0;
  for(int s=0;s<=t;s++) acc+=K[d*L+t-s]*u[s*D+d];
  h[t*D+d]=b2f(x[t*D+d])+acc+u[t*D+d]*b2f(Dk[d]);
}
__global__ void k_ln2(const float* h, float* hn){
  int s=blockIdx.x,t=threadIdx.x; float v[8];
  for(int i=0;i<8;i++) v[i]=h[s*D+t*8+i];
  for(int rep=0;rep<2;rep++){ float sum=0; for(int i=0;i<8;i++) sum+=v[i];
    for(int o=32;o;o>>=1) sum+=__shfl_xor(sum,o);
    float m=sum/D,q=0; for(int i=0;i<8;i++) q+=(v[i]-m)*(v[i]-m);
    for(int o=32;o;o>>=1) q+=__shfl_xor(q,o);
    float r=rsqrtf(q/D+1e-5f); for(int i=0;i<8;i++) v[i]=(v[i]-m)*r; }
  for(int i=0;i<8;i++) hn[s*D+t*8+i]=v[i];
}
__global__ void k_g1(const float* hn,const u16* w1,const u16* b1,float* G){
  int d=blockIdx.y; int f=blockIdx.x*256+threadIdx.x; float acc=0;
  for(int s=0;s<L;s++) acc+=hn[s*D+d]*b2f(w1[s*F+f]);
  acc+=b2f(b1[f]);
  float z=0.7978845608028654f*(acc+0.044715f*acc*acc*acc);
  G[d*F+f]=0.5f*acc*(1.f+tanhf(z));
}
__global__ void k_g2(const float* G,const u16* w2,const u16* b2,const float* h,u16* out){
  int d=blockIdx.y; int s=blockIdx.x*256+threadIdx.x; float acc=0;
  for(int f=0;f<F;f++) acc+=G[d*F+f]*b2f(w2[f*L+s]);
  acc+=b2f(b2[s]);
  out[s*D+d]=f2b(h[s*D+d]+acc);
}
extern "C" void kernel_launch(void* const* d_in, const int* in_sizes, int n_in, void* d_out, int out_size, void* d_ws, size_t ws_size, hipStream_t stream){
  const u16* x=(const u16*)d_in[0];
  float* ws=(float*)d_ws; float* u=ws; float* K=u+L*D; float* h=K+D*L; float* hn=h+L*D; float* G=hn+L*D;
  k_ln<<<L,64,0,stream>>>(x,u);
  k_K<<<dim3(L/256,D),256,0,stream>>>((const u16*)d_in[1],(const u16*)d_in[2],(const u16*)d_in[3],(const u16*)d_in[4],(const u16*)d_in[5],(const u16*)d_in[6],(const u16*)d_in[7],K);
  k_conv<<<L,D,0,stream>>>(x,u,K,(const u16*)d_in[8],h);
  k_ln2<<<L,64,0,stream>>>(h,hn);
  k_g1<<<dim3(F/256,D),256,0,stream>>>(hn,(const u16*)d_in[10],(const u16*)d_in[11],G);
  k_g2<<<dim3(L/256,D),256,0,stream>>>(G,(const u16*)d_in[12],(const u16*)d_in[13],h,(u16*)d_out);
}
```

```cpp
#include <hip/hip_runtime.h>
#include <hip/hip_bf16.h>

#define STAMPS 0

typedef unsigned short u16;
typedef short s16x4 __attribute__((ext_vector_type(4)));
typedef short s16x8 __attribute__((ext_vector_type(8)));
typedef __bf16 bf16x2 __attribute__((ext_vector_type(2)));
typedef __bf16 bf16x8 __attribute__((ext_vector_type(8)));
typedef _Float16 f16x2 __attribute__((ext_vector_type(2)));
typedef _Float16 f16x8 __attribute__((ext_vector_type(8)));
typedef float f32x2 __attribute__((ext_vector_type(2)));
typedef float f32x4 __attribute__((ext_vector_type(4)));
typedef float f32x16 __attribute__((ext_vector_type(16)));
typedef unsigned v2u __attribute__((ext_vector_type(2)));
typedef unsigned v4u __attribute__((ext_vector_type(4)));
#define LAS __attribute__((address_space(3)))
#define DEVI __device__ __forceinline__
typedef __amdgpu_buffer_rsrc_t rsrc_t;

constexpr int SEQ = 2048, DM = 512, NS = 128, FF = 1792;
constexpr unsigned WS_EP = 0;
constexpr unsigned WS_U = 0x10000;
constexpr unsigned WS_Y = WS_U + 0x200000;
constexpr unsigned WS_HN = WS_Y + 0x200000;
constexpr unsigned WS_HR = WS_HN + 0x200000;
constexpr unsigned WS_G = WS_HR + 0x200000;
constexpr unsigned WS_END = WS_G + 0x200000;
constexpr unsigned WS_DBG = WS_END;
constexpr int SC1 = 16;
constexpr unsigned TAGM = 0x00070007u;
constexpr unsigned SPIN_CAP = 1u << 16;

constexpr int TROW = 544;
constexpr int TCH = 53 * TROW;
constexpr int WK_CT = 2 * TCH;
constexpr int WK_RED = WK_CT + 2 * 1536;
constexpr int KCP = 4160;
constexpr int WK_KC = 0;
constexpr int WK_UL = 8 * KCP;
constexpr int ULC = 4608;
constexpr int WK_SIZE = 65536;
static_assert(WK_RED + 512 <= WK_SIZE && WK_UL + 2 * ULC <= WK_CT, "lds map");
constexpr int STG = 24576;

DEVI rsrc_t mk(const void* p, unsigned bytes) { return __builtin_amdgcn_make_buffer_rsrc((void*)p, 0, bytes, 0x00020000); }
DEVI float bflo(unsigned v) { return __uint_as_float(v << 16); }
DEVI float bfhi(unsigned v) { return __uint_as_float(v & 0xffff0000u); }
DEVI unsigned pkbf(float a, float b) { bf16x2 r = __builtin_convertvector(((f32x2){a, b}), bf16x2); return __builtin_bit_cast(unsigned, r); }
DEVI unsigned pkh(float a, float b) { f16x2 r = __builtin_convertvector(((f32x2){a, b}), f16x2); return __builtin_bit_cast(unsigned, r); }
DEVI void sched0() { __builtin_amdgcn_sched_barrier(0); }
DEVI void bar() { asm volatile("s_waitcnt lgkmcnt(0)" ::: "memory"); __builtin_amdgcn_s_barrier(); asm volatile("" ::: "memory"); }
template <int C> DEVI float ror_add(float s) { return s + __uint_as_float(__builtin_amdgcn_update_dpp(0u, __float_as_uint(s), 0x120 + C, 0xf, 0xf, true)); }
template <int C> DEVI float ror_max(float s) { return fmaxf(s, __uint_as_float(__builtin_amdgcn_update_dpp(0u, __float_as_uint(s), 0x120 + C, 0xf, 0xf, true))); }
DEVI unsigned max4(v4u q) { return max(max(q.x, q.y), max(q.z, q.w)); }
DEVI unsigned max16(v4u a, v4u b, v4u c, v4u d) {
  unsigned m = max(max(a.x, a.y), a.z); m = max(max(m, a.w), b.x); m = max(max(m, b.y), b.z); m = max(max(m, b.w), c.x);
  unsigned n = max(max(c.y, c.z), c.w); n = max(max(n, d.x), d.y); n = max(max(n, d.z), d.w); return max(m, n);
}
DEVI bool anybad(unsigned m) { return __builtin_amdgcn_ballot_w64(m == ~0u) != 0ull; }
struct cx { f32x2 v, r; };
DEVI cx mkcx(f32x2 v) { return cx{v, (f32x2){-v.y, v.x}}; }
DEVI f32x2 cmul(f32x2 a, cx b) { f32x2 t = (f32x2){a.x, a.x} * b.v; return __builtin_elementwise_fma((f32x2){a.y, a.y}, b.r, t); }
DEVI f32x2 cexp(float a2, float br, float l) {
  float m = __builtin_amdgcn_exp2f(a2 * l), f = __builtin_amdgcn_fractf(br * l);
  return (f32x2){m * __builtin_amdgcn_cosf(f), m * __builtin_amdgcn_sinf(f)};
}
DEVI s16x8 trfrag(LAS char* p0, LAS char* p1) {
  s16x4 lo = __builtin_amdgcn_ds_read_tr16_b64_v4i16((LAS s16x4*)p0), hi = __builtin_amdgcn_ds_read_tr16_b64_v4i16((LAS s16x4*)p1);
  return __builtin_shufflevector(lo, hi, 0, 1, 2, 3, 4, 5, 6, 7);
}

DEVI void row_stats(const float (&a)[8], const float (&b)[8], LAS float* red, int w, int lane, float& mean, float& var) {
  float h[8];
#pragma unroll
  for (int e = 0; e < 8; ++e) {
    float s1 = a[e] + b[e], s2 = fmaf(a[e], a[e], b[e] * b[e]);
    auto r = __builtin_amdgcn_permlane32_swap(__float_as_uint(s1), __float_as_uint(s2), false, false);
    h[e] = __uint_as_float(r[0]) + __uint_as_float(r[1]);
  }
  f32x4 q;
#pragma unroll
  for (int e = 0; e < 4; ++e) {
    auto r = __builtin_amdgcn_permlane16_swap(__float_as_uint(h[e]), __float_as_uint(h[e + 4]), false, false);
    float s = __uint_as_float(r[0]) + __uint_as_float(r[1]);
    s = ror_add<8>(s); s = ror_add<4>(s); s = ror_add<2>(s); s = ror_add<1>(s);
    q[e] = s;
  }
  if ((lane & 15) == 0) *(LAS f32x4*)(red + w * 16 + (lane >> 4) * 4) = q;
  bar();
  const int e = lane & 7;
  float s1 = (red[e] + red[16 + e]) + (red[32 + e] + red[48 + e]);
  float s2 = (red[8 + e] + red[24 + e]) + (red[40 + e] + red[56 + e]);
  mean = s1 * (1.f / DM);
  var = fmaxf(fmaf(-mean, mean, s2 * (1.f / DM)), 0.f);
}
DEVI float rdl(float v, int l) { return __uint_as_float(__builtin_amdgcn_readlane(__float_as_uint(v), l)); }

#define STAMP(i) do {} while (0)

__global__ void __launch_bounds__(256, 1)
s4_mixer_kernel(const u16* __restrict__ x, const u16* __restrict__ Are, const u16* __restrict__ Aim, const u16* __restrict__ Bre,
                const u16* __restrict__ Bim, const u16* __restrict__ Cre, const u16* __restrict__ Cim, const u16* __restrict__ logdt,
                const u16* __restrict__ Dsk, const u16* __restrict__ w1, const u16* __restrict__ b1, const u16* __restrict__ w2,
                const u16* __restrict__ b2, u16* __restrict__ out, char* ws) {
  __shared__ __attribute__((aligned(16))) char stage_s[4 * STG];
  __shared__ __attribute__((aligned(16))) char work_s[WK_SIZE];
  LAS char* const work = (LAS char*)work_s;
  const int t = threadIdx.x, lane = t & 63;
  const int w = __builtin_amdgcn_readfirstlane(t >> 6);
  const int bx = blockIdx.x & 7, bj = blockIdx.x >> 3;
  const int p = bx * 32 + bj;
  const int dt = 4 * (bx & 1) + (bj & 3);
  const int ft = 7 * (bx >> 1) + (bj >> 2);
  const int st = 8 * (bx >> 1) + (bj >> 2);
  const bool g1 = bj < 28;
  LAS char* const stg = (LAS char*)stage_s + w * STG;
  STAMP(0);

  const rsrc_t rws = mk(ws, WS_END);
  const rsrc_t rws1 = mk(ws, g1 ? WS_END : 0u);
  const rsrc_t rw1 = mk(w1, g1 ? SEQ * FF * 2 : 0u);
  const rsrc_t rw2 = mk(w2, FF * SEQ * 2);
  unsigned spins = 0;

  const int cw = w >> 1, hw = w & 1;
  const int lg = p >> 2, i0 = 2 * (p & 3);
  const int dch = 16 * (lg >> 1) + 2 * (i0 + cw) + (lg & 1);
  unsigned xr[8];
  {
    const rsrc_t rx = mk(x, SEQ * DM * 2);
#pragma unroll
    for (int e = 0; e < 8; ++e) xr[e] = __builtin_amdgcn_raw_buffer_load_b32(rx, t * 4 + (e & 3) * 1024, p * 8192 + (e >> 2) * 4096, 0);
  }
  const v4u ep = __builtin_amdgcn_raw_buffer_load_b128(rws, WS_EP + (t & 1) * 16, 0, SC1);
  const unsigned dsk = ((const unsigned*)Dsk)[t];
  const unsigned pAr = ((const unsigned*)Are)[dch * 64 + lane], pAi = ((const unsigned*)Aim)[dch * 64 + lane];
  const unsigned pBr = ((const unsigned*)Bre)[dch * 64 + lane], pBi = ((const unsigned*)Bim)[dch * 64 + lane];
  const unsigned pCr = ((const unsigned*)Cre)[dch * 64 + lane], pCi = ((const unsigned*)Cim)[dch * 64 + lane];
  const unsigned ldt2 = ((const unsigned*)logdt)[dch >> 1];
  const v4u b2v = ((const v4u*)b2)[p];
  sched0();
  const v4u POI = {~0u, ~0u, ~0u, ~0u};
  {
    const unsigned o = WS_Y + (t >> 3) * 32768 + p * 128 + (t & 7) * 16;
    __builtin_amdgcn_raw_buffer_store_b128(POI, rws, o, 0, SC1);
    __builtin_amdgcn_raw_buffer_store_b128(POI, rws, o, 32 * 32768, SC1);
  }
  sched0();
  const unsigned dch16 = ((lane & 7) ^ (((lane >> 4) & 1) << 2)) * 16;
  const unsigned vw1 = (lane >> 3) * (FF * 2) + dch16, vw2 = (lane >> 3) * (SEQ * 2) + dch16;
  const unsigned sw1 = w * 512 * (FF * 2) + ft * 128, sw2 = w * 448 * (SEQ * 2) + st * 128;
#define DMA1(slot, hs) __builtin_amdgcn_raw_ptr_buffer_load_lds(rw1, (LAS void*)(stg + (slot) * 1024), 16, vw1, sw1 + (hs) * 8 * (FF * 2), 0, 0)
#define DMA2(slot, hs) __builtin_amdgcn_raw_ptr_buffer_load_lds(rw2, (LAS void*)(stg + (slot) * 1024), 16, vw2, sw2 + (hs) * 8 * (SEQ * 2), 0, 0)
#pragma unroll
  for (int i = 0; i < 24; ++i) DMA1(i, i);
  sched0();
  STAMP(1);

  float v0[8], v1[8];
  v4u tag;
  {
    float a[8], b[8];
#pragma unroll
    for (int e = 0; e < 8; ++e) { a[e] = bflo(xr[e]); b[e] = bfhi(xr[e]); }
    float mean, var;
    asm volatile("s_waitcnt vmcnt(24)" ::: "memory");
    row_stats(a, b, (LAS float*)(work + WK_RED), w, lane, mean, var);
    const float rs = __builtin_amdgcn_rsqf(var + 1e-5f);
    const float d0 = bflo(dsk), d1 = bfhi(dsk);
    float u0[8], u1[8];
#pragma unroll
    for (int e = 0; e < 8; ++e) {
      const float m = rdl(mean, e), r = rdl(rs, e);
      u0[e] = (a[e] - m) * r; u1[e] = (b[e] - m) * r;
      v0[e] = fmaf(u0[e], d0, a[e]); v1[e] = fmaf(u1[e], d1, b[e]);
    }
    tag = (ep + 0x00010001u) & TAGM;
    v4u c0, c1;
#pragma unroll
    for (int k = 0; k < 4; ++k) {
      c0[k] = ((__builtin_bit_cast(unsigned, __builtin_amdgcn_cvt_pkrtz(u0[2 * k], u0[2 * k + 1])) + 0x00040004u) & ~TAGM) | tag[k];
      c1[k] = ((__builtin_bit_cast(unsigned, __builtin_amdgcn_cvt_pkrtz(u1[2 * k], u1[2 * k + 1])) + 0x00040004u) & ~TAGM) | tag[k];
    }
    const unsigned o = WS_U + (t >> 3) * 65536 + p * 128 + (t & 7) * 16;
    __builtin_amdgcn_raw_buffer_store_b128(c0, rws, o, 0, SC1);
    __builtin_amdgcn_raw_buffer_store_b128(c1, rws, o, 32768, SC1);
  }
  sched0();
  const unsigned ohn = (((t >> 5) * 128 + (p >> 1)) * 2) * 1024 + (p & 1) * 512 + (t & 31) * 16;
  const unsigned og = WS_G + ((dt * 112 + 4 * ft + 2 * (w >> 1)) * 2 + (w & 1)) * 1024 + lane * 16;
  __builtin_amdgcn_raw_buffer_store_b128(POI, rws, ohn, WS_HN, SC1);
  __builtin_amdgcn_raw_buffer_store_b128(POI, rws, ohn + 1024, WS_HN, SC1);
  __builtin_amdgcn_raw_buffer_store_b128(POI, rws, ohn, WS_HR, SC1);
  __builtin_amdgcn_raw_buffer_store_b128(POI, rws, ohn + 1024, WS_HR, SC1);
  __builtin_amdgcn_raw_buffer_store_b128(POI, rws1, og, 0, SC1);
  __builtin_amdgcn_raw_buffer_store_b128(POI, rws1, og + 2048, 0, SC1);
  sched0();
  STAMP(2);

  float kinv;
  {
    const float ld = (dch & 1) ? bfhi(ldt2) : bflo(ldt2);
    const float dtv = bflo(pkbf(__builtin_amdgcn_exp2f(ld * 1.44269504088896341f), 0.f));
    float wr[2], wi[2];
#pragma unroll
    for (int k = 0; k < 2; ++k) {
      const float br = dtv * (k ? bfhi(pBr) : bflo(pBr)), bi = dtv * (k ? bfhi(pBi) : bflo(pBi));
      const float cr = k ? bfhi(pCr) : bflo(pCr), ci = k ? bfhi(pCi) : bflo(pCi);
      wr[k] = cr * br - ci * bi; wi[k] = cr * bi + ci * br;
    }
    float mx = fmaxf(fmaxf(fabsf(wr[0]), fabsf(wi[0])), fmaxf(fabsf(wr[1]), fabsf(wi[1])));
    mx = ror_max<8>(mx); mx = ror_max<4>(mx); mx = ror_max<2>(mx); mx = ror_max<1>(mx);
    mx = fmaxf(fmaxf(rdl(mx, 0), rdl(mx, 16)), fmaxf(rdl(mx, 32), rdl(mx, 48)));
    int ex = (int)(__float_as_uint(mx) >> 23);
    ex = ex < 16 ? 16 : (ex > 240 ? 240 : ex);
    const float sc = __uint_as_float((unsigned)(261 - ex) << 23);
    kinv = __uint_as_float((unsigned)(ex - 7) << 23);
    const float a2 = dtv * (hw ? bfhi(pAr) : bflo(pAr)) * 1.44269504088896341f;
    const float bq = dtv * (hw ? bfhi(pAi) : bflo(pAi)) * 0.15915494309189535f;
    const f32x2 W0 = {(hw ? wr[1] : wr[0]) * sc, -(hw ? wi[1] : wi[0]) * sc};
    const cx e1 = mkcx(cexp(a2, -bq, 1.f)), e256 = mkcx(cexp(a2, -bq, 256.f)), e8 = mkcx(cexp(a2, bq, 8.f));
    f32x2 la = {1.f, 0.f}, lb = cexp(a2, bq, 128.f);
    LAS unsigned* tb = (LAS unsigned*)(work + cw * TCH + hw * 256 + lane * 4);
#define TPUT(row, z) tb[(row) * (TROW / 4)] = pkh((z).x, (z).y)
#pragma unroll
    for (int i = 0; i < 16; ++i) {
      TPUT(i, la); TPUT(16 + i, lb);
      if (i < 15) { la = cmul(la, e8); lb = cmul(lb, e8); }
    }
    f32x2 h[8]; h[0] = W0;
#pragma unroll
    for (int i = 1; i < 8; ++i) h[i] = cmul(h[i - 1], e1);
#pragma unroll
    for (int i = 0; i < 8; ++i) TPUT(32 + i, h[i]);
    f32x2 g = cmul(h[0], e256), g4 = cmul(h[4], e256);
    TPUT(40, g); TPUT(41, cmul(h[2], e256)); TPUT(42, g4); TPUT(43, cmul(h[6], e256));
    g = cmul(g, e256); g4 = cmul(g4, e256); TPUT(44, g); TPUT(45, g4);
    g = cmul(g, e256); g4 = cmul(g4, e256); TPUT(46, g); TPUT(47, g4);
#pragma unroll
    for (int i = 0; i < 5; ++i) { g = cmul(g, e256); TPUT(48 + i, g); }
    bar();
    const int r = lane & 15, g16 = lane >> 4;
    LAS char* pa0 = work + cw * TCH + (32 + r) * TROW + g16 * 16;
    LAS char* pa1 = work + cw * TCH + ((r & 3) ? 52 : 48 + (r >> 2)) * TROW + g16 * 16;
    LAS char* pb = work + cw * TCH + (16 * hw + r) * TROW + g16 * 16;
    f32x4 k0 = {0.f, 0.f, 0.f, 0.f}, k1 = {0.f, 0.f, 0.f, 0.f};
#pragma unroll
    for (int ks = 0; ks < 8; ++ks) {
      const f16x8 fb = *(LAS f16x8*)(pb + ks * 64);
      k0 = __builtin_amdgcn_mfma_f32_16x16x32_f16(*(LAS f16x8*)(pa0 + ks * 64), fb, k0, 0, 0, 0);
      k1 = __builtin_amdgcn_mfma_f32_16x16x32_f16(*(LAS f16x8*)(pa1 + ks * 64), fb, k1, 0, 0, 0);
    }
    const int bb = 16 * hw + r;
    LAS u16* ct = (LAS u16*)(work + WK_CT + cw * 1536);
    const int ia = g16 < 2 ? 8 * bb + 4 * g16 : (g16 == 2 ? 256 + 4 * bb : 384 + 2 * bb);
    const int ib = g16 < 3 ? ia + 2 : 448 + 2 * bb;
    *(LAS unsigned*)(ct + ia) = pkh(k0[0], k0[1]);
    *(LAS unsigned*)(ct + ib) = pkh(k0[2], k0[3]);
    const unsigned k1p = pkh(k1[0], k1[1]);
    ct[512 + 32 * g16 + bb] = (u16)k1p;
    ct[640 + 32 * g16 + bb] = (u16)(k1p >> 16);
    bar();
    const int tau = t & 127;
    unsigned E[10];
#pragma unroll
    for (int k = 0; k < 10; ++k) {
      const int l0 = 16 * tau - 4 + 2 * k;
      const unsigned bits = pkbf((float)l0, (float)(l0 + 1));
      const int j0 = l0 < 128 ? (l0 < 0 ? 0 : l0) : (int)(bits & 0xffffu) - 0x4300 + 128;
      const int j1 = l0 < 128 ? (l0 < 0 ? 0 : l0 + 1) : (int)(bits >> 16) - 0x4300 + 128;
      const unsigned val = (unsigned)ct[j0] | ((unsigned)ct[j1] << 16);
      E[k] = l0 < 0 ? 0u : val;
    }
    unsigned O[9];
#pragma unroll
    for (int k = 0; k < 9; ++k) O[k] = __builtin_amdgcn_alignbit(E[k + 1], E[k], 16);
    LAS char* kc = work + WK_KC + cw * 4 * KCP + 32 + tau * 32;
    *(LAS v4u*)(kc) = (v4u){E[2], E[3], E[4], E[5]};             *(LAS v4u*)(kc + 16) = (v4u){E[6], E[7], E[8], E[9]};
    *(LAS v4u*)(kc + KCP) = (v4u){O[1], O[2], O[3], O[4]};       *(LAS v4u*)(kc + KCP + 16) = (v4u){O[5], O[6], O[7], O[8]};
    *(LAS v4u*)(kc + 2 * KCP) = (v4u){E[1], E[2], E[3], E[4]};   *(LAS v4u*)(kc + 2 * KCP + 16) = (v4u){E[5], E[6], E[7], E[8]};
    *(LAS v4u*)(kc + 3 * KCP) = (v4u){O[0], O[1], O[2], O[3]};   *(LAS v4u*)(kc + 3 * KCP + 16) = (v4u){O[4], O[5], O[6], O[7]};
    const v4u Z = {0u, 0u, 0u, 0u};
    if (t < 16) *(LAS v4u*)(work + WK_KC + (t >> 1) * KCP + (t & 1) * 16) = Z;
    if (t >= 64 && t < 128) *(LAS v4u*)(work + WK_UL + ((t >> 5) & 1) * ULC + (t & 31) * 16) = Z;
    bar();
  }
  STAMP(3);
  f16x8 TA[4][8];
  {
    LAS char* pa = work + WK_KC + cw * 4 * KCP + ((lane & 15) >> 2) * KCP + (28 - 8 * (lane >> 4) + 4 * (lane & 3)) * 2 + hw * 256;
#pragma unroll
    for (int i = 0; i < 4; ++i)
#pragma unroll
      for (int mu = 0; mu < 8; ++mu) TA[i][mu] = __builtin_bit_cast(f16x8, trfrag(pa + (8 * mu + i) * 64 + 8, pa + (8 * mu + i) * 64));
  }
  const int trq = (lane & 15) >> 2;
  const int trc = (((2 * ((lane >> 4) & 1) + ((lane & 3) >> 1)) ^ (((trq >> 1) & 1) << 2) ^ ((w >> 1) << 2)) * 16) + 8 * (lane & 1);
  LAS char* const t1a = stg + (8 * (lane >> 5) + trq) * 128 + trc;
  LAS char* const t1b = stg + (8 * (lane >> 5) + trq) * 128 + (trc ^ 64);
  LAS char* const t2a = stg + (4 * (lane >> 5) + trq) * 128 + trc;
  LAS char* const t2b = stg + (4 * (lane >> 5) + trq) * 128 + (trc ^ 64);
  bf16x8 W1[32][2], W2[16][2];
#define GET1(ks, slot) do { W1[ks][0] = __builtin_bit_cast(bf16x8, trfrag(t1a + (slot) * 2048, t1a + (slot) * 2048 + 512)); \
                            W1[ks][1] = __builtin_bit_cast(bf16x8, trfrag(t1b + (slot) * 2048, t1b + (slot) * 2048 + 512)); } while (0)
#define FR2(sub, slot) __builtin_bit_cast(bf16x8, trfrag(((sub) ? t2b : t2a) + (slot) * 2048, ((sub) ? t2b : t2a) + (slot) * 2048 + 1024))
#define GET2(ks, slot) do { W2[ks][0] = FR2(0, slot); W2[ks][1] = FR2(1, slot); } while (0)
#pragma unroll
  for (int i = 0; i < 12; ++i) GET1(i, i);
  sched0();
  STAMP(4);

  {
    const unsigned o = WS_U + lg * 32768 + (t >> 1) * 128 + (i0 + (t & 1)) * 16;
    v4u q0, q1;
    for (;;) {
      q0 = __builtin_amdgcn_raw_buffer_load_b128(rws, o, 0, SC1) ^ tag;
      q1 = __builtin_amdgcn_raw_buffer_load_b128(rws, o, 16384, SC1) ^ tag;
      const v4u z = q0 | q1;
      if (__all((((z.x | z.y) | (z.z | z.w)) & TAGM) == 0u) || ++spins > SPIN_CAP) break;
      asm volatile("s_sleep 1" ::: "memory");
    }
    LAS char* ul = work + WK_UL + (t & 1) * ULC + 512 + (t >> 1) * 16;
    *(LAS v4u*)ul = q0; *(LAS v4u*)(ul + 2048) = q1;
  }
  bar();
  if (blockIdx.x == 0 && t < 2) __builtin_amdgcn_raw_buffer_store_b128(tag, rws, WS_EP + t * 16, 0, SC1);
  STAMP(5);

  {
    f32x4 acc[8];
#pragma unroll
    for (int c = 0; c < 8; ++c) acc[c] = (f32x4){0.f, 0.f, 0.f, 0.f};
    LAS char* pb = work + WK_UL + cw * ULC + (16 * (lane & 15) + 8 * (lane >> 4) - 128 * hw + 144) * 2;
    f16x8 UB[2][8];
#pragma unroll
    for (int g = 0; g < 8; ++g) UB[0][g] = *(LAS f16x8*)(pb + (256 * g + 96) * 2);
#pragma unroll
    for (int i = 0; i < 4; ++i) {
      int n = 0;
#pragma unroll
      for (int g = 0; g < 8; ++g) {
#pragma unroll
        for (int mu = 0; mu + g < 8; ++mu) {
          acc[g + mu] = __builtin_amdgcn_mfma_f32_16x16x32_f16(TA[i][mu], UB[i & 1][g], acc[g + mu], 0, 0, 0);
          if (n % 6 == 2) { DMA1(i * 6 + n / 6, 24 + i * 6 + n / 6); sched0(); }
          if (i < 3 && n % 4 == 0 && n < 32) { UB[(i + 1) & 1][n / 4] = *(LAS f16x8*)(pb + (256 * (n / 4) + 96 - 32 * (i + 1)) * 2); sched0(); }
          ++n;
        }
      }
    }
    sched0();
    STAMP(6);
    LAS char* ex = work + lane * 16;
    f32x4 keep[4];
    if (hw) {
#pragma unroll
      for (int k = 0; k < 4; ++k) { *(LAS f32x4*)(ex + (w * 4 + k) * 1024) = acc[k]; keep[k] = acc[4 + k] * kinv; }
      asm volatile("" ::: "memory");
    } else {
#pragma unroll
      for (int k = 0; k < 4; ++k) { *(LAS f32x4*)(ex + (w * 4 + k) * 1024) = acc[4 + k]; keep[k] = acc[k] * kinv; }
    }
    bar();
    unsigned pk[4][2];
#pragma unroll
    for (int k = 0; k < 4; ++k) {
      const f32x4 y = *(LAS f32x4*)(ex + ((w ^ 1) * 4 + k) * 1024) * kinv + keep[k];
      pk[k][0] = pkbf(y[0], y[1]); pk[k][1] = pkbf(y[2], y[3]);
    }
    const int col = lane & 15, rho = lane >> 4;
    const unsigned oy = WS_Y + (8 * (4 * hw + (rho & 1)) + 2 * (col >> 2) + (rho >> 1)) * 32768 + p * 128 + cw * 64 + (col & 3) * 16;
#pragma unroll
    for (int k = 0; k < 4; k += 2) {
      auto r0 = __builtin_amdgcn_permlane16_swap(pk[k][0], pk[k + 1][0], false, false);
      auto r1 = __builtin_amdgcn_permlane16_swap(pk[k][1], pk[k + 1][1], false, false);
      __builtin_amdgcn_raw_buffer_store_b128((v4u){r0[0], r1[0], r0[1], r1[1]}, rws, oy, k * 8 * 32768, SC1);
    }
  }
  sched0();
  STAMP(7);
#pragma unroll
  for (int i = 0; i < 12; ++i) GET1(12 + i, i);
  asm volatile("s_waitcnt lgkmcnt(0)" ::: "memory");
  sched0();
#pragma unroll
  for (int i = 0; i < 16; ++i) DMA1(i, 48 + i);
#pragma unroll
  for (int i = 0; i < 8; ++i) DMA2(16 + i, i);
  sched0();
  STAMP(8);

  {
    const unsigned o = WS_Y + ((p >> 3) * 2 + (p & 1)) * 32768 + ((t >> 3) * 8 + ((t & 7) >> 1)) * 128 + (t & 1) * 64 + ((p >> 1) & 3) * 16;
    v4u q0, q1;
    for (;;) {
      q0 = __builtin_amdgcn_raw_buffer_load_b128(rws, o, 0, SC1);
      q1 = __builtin_amdgcn_raw_buffer_load_b128(rws, o + 512, 0, SC1);
      if (__all(max(max4(q0), max4(q1)) != ~0u) || ++spins > SPIN_CAP) break;
      asm volatile("s_sleep 1" ::: "memory");
    }
    STAMP(9);
#pragma unroll
    for (int k = 0; k < 4; ++k) {
      v0[2 * k] += bflo(q0[k]); v0[2 * k + 1] += bfhi(q0[k]);
      v1[2 * k] += bflo(q1[k]); v1[2 * k + 1] += bfhi(q1[k]);
    }
    float mean, var;
    row_stats(v0, v1, (LAS float*)(work + WK_RED + 256), w, lane, mean, var);
    const float r1 = __builtin_amdgcn_rsqf(var + 1e-5f);
    const float rs = r1 * __builtin_amdgcn_rsqf(fmaf(var * r1, r1, 1e-5f));
    v4u n0, n1, h0, h1;
#pragma unroll
    for (int k = 0; k < 4; ++k) {
      const float ma = rdl(mean, 2 * k), ra = rdl(rs, 2 * k), mb = rdl(mean, 2 * k + 1), rb = rdl(rs, 2 * k + 1);
      n0[k] = pkbf((v0[2 * k] - ma) * ra, (v0[2 * k + 1] - mb) * rb);
      n1[k] = pkbf((v1[2 * k] - ma) * ra, (v1[2 * k + 1] - mb) * rb);
    }
    __builtin_amdgcn_raw_buffer_store_b128(n0, rws, ohn, WS_HN, SC1);
    __builtin_amdgcn_raw_buffer_store_b128(n1, rws, ohn + 1024, WS_HN, SC1);
    sched0();
#pragma unroll
    for (int k = 0; k < 4; ++k) {
      const float ba = bflo(b2v[k]), bb = bfhi(b2v[k]);
      h0[k] = pkbf(v0[2 * k] + ba, v0[2 * k + 1] + bb);
      h1[k] = pkbf(v1[2 * k] + ba, v1[2 * k + 1] + bb);
    }
    __builtin_amdgcn_raw_buffer_store_b128(h0, rws, ohn, WS_HR, SC1);
    __builtin_amdgcn_raw_buffer_store_b128(h1, rws, ohn + 1024, WS_HR, SC1);
  }
  sched0();
  STAMP(10);
#pragma unroll
  for (int i = 0; i < 8; ++i) GET1(24 + i, i);
#pragma unroll
  for (int i = 0; i < 4; ++i) GET2(i, 8 + i);
  asm volatile("s_waitcnt lgkmcnt(0)" ::: "memory");
  sched0();
#pragma unroll
  for (int i = 0; i < 24; ++i) DMA2(i, 8 + i);
  v2u b1v[4];
  {
    const rsrc_t rb1 = mk(b1, g1 ? FF * 2 : 0u);
#pragma unroll
    for (int k = 0; k < 4; ++k) b1v[k] = __builtin_amdgcn_raw_buffer_load_b64(rb1, (lane >> 5) * 8 + k * 16, ft * 128 + (w >> 1) * 64, 0);
  }
  sched0();
  STAMP(11);

  {
    f32x16 acc[2][2];
#pragma unroll
    for (int a = 0; a < 2; ++a)
#pragma unroll
      for (int b = 0; b < 2; ++b)
#pragma unroll
        for (int r = 0; r < 16; ++r) acc[a][b][r] = 0.f;
    const unsigned va0 = lane * 16 + (w & 1) * 1024, va1 = va0 ^ 1024;
    const unsigned sa = WS_HN + (dt * 128 + w * 32) * 2048;
#define LDA1(i) __builtin_amdgcn_raw_buffer_load_b128(rws1, (((i) & 1) ? va1 : va0) + (((i) >> 1) & 1) * 2048, sa + ((i) >> 2) * 4096, SC1)
    v4u q[16];
    for (;;) {
      const unsigned pr = __builtin_amdgcn_raw_buffer_load_b32(rws1, (lane >> 1) * 2048 + 1024 + (lane & 1) * 512 + 508, sa, SC1);
      sched0();
#pragma unroll
      for (int i = 0; i < 16; ++i) { q[i] = LDA1(i); sched0(); }
      if (!(anybad(pr) & (++spins <= SPIN_CAP))) break;
      asm volatile("s_sleep 1" ::: "memory");
    }
    STAMP(12);
#pragma unroll
    for (int bt = 0; bt < 16; ++bt) {
      const int s0 = (bt & 3) * 4;
      if (__builtin_expect(anybad(max16(q[s0], q[s0 + 1], q[s0 + 2], q[s0 + 3])), 0)) {
        bool again;
        do {
          asm volatile("s_sleep 1" ::: "memory");
#pragma unroll
          for (int i = 0; i < 4; ++i) q[s0 + i] = LDA1(bt * 4 + i);
          again = anybad(max16(q[s0], q[s0 + 1], q[s0 + 2], q[s0 + 3])) & (++spins <= SPIN_CAP);
        } while (again);
      }
#pragma unroll
      for (int i = 0; i < 4; ++i) {
        const int ks = bt * 2 + (i >> 1);
        const bf16x8 a = __builtin_bit_cast(bf16x8, q[s0 + i]);
        acc[i & 1][0] = __builtin_amdgcn_mfma_f32_32x32x16_bf16(W1[ks][0], a, acc[i & 1][0], 0, 0, 0);
        acc[i & 1][1] = __builtin_amdgcn_mfma_f32_32x32x16_bf16(W1[ks][1], a, acc[i & 1][1], 0, 0, 0);
        if (bt < 12) q[s0 + i] = LDA1(bt * 4 + 16 + i);
        sched0();
      }
    }
    STAMP(13);
    LAS char* ex = work + lane * 16;
#pragma unroll
    for (int lam = 1; lam < 4; ++lam)
#pragma unroll
      for (int k = 0; k < 4; ++k) {
        const f32x16& s = acc[lam & 1][lam >> 1];
        *(LAS f32x4*)(ex + (((w ^ lam) * 3 + lam - 1) * 4 + k) * 1024) = (f32x4){s[4 * k], s[4 * k + 1], s[4 * k + 2], s[4 * k + 3]};
      }
    bar();
    float gv[16];
#pragma unroll
    for (int k = 0; k < 4; ++k) {
      const f32x4 a = *(LAS f32x4*)(ex + ((w * 3 + 0) * 4 + k) * 1024), b = *(LAS f32x4*)(ex + ((w * 3 + 1) * 4 + k) * 1024),
                  c = *(LAS f32x4*)(ex + ((w * 3 + 2) * 4 + k) * 1024);
#pragma unroll
      for (int j = 0; j < 4; ++j) {
        const float bias = (j & 1) ? bfhi(b1v[k][j >> 1]) : bflo(b1v[k][j >> 1]);
        const float u = ((acc[0][0][4 * k + j] + a[j]) + (b[j] + c[j])) + bias;
        const float z = u * fmaf(u * u, -0.10294324f, -2.30220820f);
        gv[4 * k + j] = u * __builtin_amdgcn_rcpf(1.f + __builtin_amdgcn_exp2f(z));
      }
    }
    __builtin_amdgcn_raw_buffer_store_b128((v4u){pkbf(gv[0], gv[1]), pkbf(gv[2], gv[3]), pkbf(gv[4], gv[5]), pkbf(gv[6], gv[7])}, rws1, og, 0, SC1);
    __builtin_amdgcn_raw_buffer_store_b128((v4u){pkbf(gv[8], gv[9]), pkbf(gv[10], gv[11]), pkbf(gv[12], gv[13]), pkbf(gv[14], gv[15])}, rws1, og + 2048, 0, SC1);
  }
  sched0();
  STAMP(14);
#pragma unroll
  for (int i = 0; i < 12; ++i) GET2(4 + i, i);
  asm volatile("s_waitcnt lgkmcnt(0)" ::: "memory");
  sched0();
#pragma unroll
  for (int i = 0; i < 24; ++i) DMA2(i, 32 + i);
  sched0();
  STAMP(15);

  {
    f32x16 acc[2][2];
    const unsigned va = (lane ^ ((w & 1) << 4)) * 16;
    {
      const unsigned sh = WS_HR + (dt * 128 + 4 * st + w) * 2048;
      v4u r0, r1;
      for (;;) {
        r0 = __builtin_amdgcn_raw_buffer_load_b128(rws, va, sh, SC1);
        r1 = __builtin_amdgcn_raw_buffer_load_b128(rws, va + 1024, sh, SC1);
        if (__all(max(max4(r0), max4(r1)) != ~0u) || ++spins > SPIN_CAP) break;
        asm volatile("s_sleep 1" ::: "memory");
      }
      const int jj = (lane & 31) - 16 * (w & 1) - 8 * (lane >> 5);
      v4u id;
#pragma unroll
      for (int k = 0; k < 4; ++k) id[k] = jj == 2 * k ? 0x3f80u : (jj == 2 * k + 1 ? 0x3f800000u : 0u);
      f32x16 z;
#pragma unroll
      for (int r = 0; r < 16; ++r) z[r] = 0.f;
      acc[0][0] = __builtin_amdgcn_mfma_f32_32x32x16_bf16(__builtin_bit_cast(bf16x8, r0), __builtin_bit_cast(bf16x8, id), z, 0, 0, 0);
      acc[1][0] = __builtin_amdgcn_mfma_f32_32x32x16_bf16(__builtin_bit_cast(bf16x8, r1), __builtin_bit_cast(bf16x8, id), z, 0, 0, 0);
      acc[0][1] = z; acc[1][1] = z;
    }
    const unsigned sa = WS_G + (dt * 112 + w * 28) * 2048;
#define LDA2(i) __builtin_amdgcn_raw_buffer_load_b128(rws, va + ((i) & 3) * 1024, sa + ((i) >> 2) * 4096, SC1)
    v4u q[16];
    for (;;) {
      const unsigned pr = __builtin_amdgcn_raw_buffer_load_b32(rws, (lane < 56 ? lane : 55) * 1024 + 1020, sa, SC1);
      sched0();
#pragma unroll
      for (int i = 0; i < 16; ++i) { q[i] = LDA2(i); sched0(); }
      if (!(anybad(pr) & (++spins <= SPIN_CAP))) break;
      asm volatile("s_sleep 1" ::: "memory");
    }
    STAMP(16);
#pragma unroll
    for (int bt = 0; bt < 14; ++bt) {
      const int s0 = (bt & 3) * 4;
      bf16x8 wa[2][2];
#pragma unroll
      for (int k = 0; k < 2; ++k) {
        const int ks = bt * 2 + k;
        if (ks < 16) { wa[k][0] = W2[ks][0]; wa[k][1] = W2[ks][1]; }
        else { wa[k][0] = FR2(0, ks - 16); wa[k][1] = FR2(1, ks - 16); }
      }
      if (__builtin_expect(anybad(max16(q[s0], q[s0 + 1], q[s0 + 2], q[s0 + 3])), 0)) {
        bool again;
        do {
          asm volatile("s_sleep 1" ::: "memory");
#pragma unroll
          for (int i = 0; i < 4; ++i) q[s0 + i] = LDA2(bt * 4 + i);
          again = anybad(max16(q[s0], q[s0 + 1], q[s0 + 2], q[s0 + 3])) & (++spins <= SPIN_CAP);
        } while (again);
      }
#pragma unroll
      for (int i = 0; i < 4; ++i) {
        const bf16x8 a = __builtin_bit_cast(bf16x8, q[s0 + i]);
        acc[i & 1][0] = __builtin_amdgcn_mfma_f32_32x32x16_bf16(a, wa[i >> 1][0], acc[i & 1][0], 0, 0, 0);
        acc[i & 1][1] = __builtin_amdgcn_mfma_f32_32x32x16_bf16(a, wa[i >> 1][1], acc[i & 1][1], 0, 0, 0);
        if (bt < 10) q[s0 + i] = LDA2(bt * 4 + 16 + i);
        sched0();
      }
    }
    STAMP(17);
    bar();
    LAS char* ex = work + lane * 16;
#pragma unroll
    for (int lam = 1; lam < 4; ++lam)
#pragma unroll
      for (int k = 0; k < 4; ++k) {
        const f32x16& s = acc[k >> 1][lam >> 1];
        const int r = 8 * (lam & 1) + 4 * (k & 1);
        *(LAS f32x4*)(ex + (((w ^ lam) * 3 + lam - 1) * 4 + k) * 1024) = (f32x4){s[r], s[r + 1], s[r + 2], s[r + 3]};
      }
    bar();
    f32x4 o[4];
#pragma unroll
    for (int k = 0; k < 4; ++k) {
      const f32x4 a = *(LAS f32x4*)(ex + ((w * 3 + 0) * 4 + k) * 1024), b = *(LAS f32x4*)(ex + ((w * 3 + 1) * 4 + k) * 1024),
                  c = *(LAS f32x4*)(ex + ((w * 3 + 2) * 4 + k) * 1024);
      const f32x16& s = acc[k >> 1][0];
      const int r = 4 * (k & 1);
      o[k] = ((f32x4){s[r], s[r + 1], s[r + 2], s[r + 3]} + a) + (b + c);
    }
    const rsrc_t ro = mk(out, SEQ * DM * 2);
    const unsigned vo = (lane & 31) * 1024 + (lane >> 5) * 16, so = (64 * st + 32 * (w >> 1)) * 1024 + dt * 128 + (w & 1) * 64;
#pragma unroll
    for (int k = 0; k < 2; ++k)
      __builtin_amdgcn_raw_buffer_store_b128((v4u){pkbf(o[k][0], o[2 + k][0]), pkbf(o[k][1], o[2 + k][1]), pkbf(o[k][2], o[2 + k][2]), pkbf(o[k][3], o[2 + k][3])},
                                             ro, vo + k * 32, so, 0);
  }
  STAMP(18);
}

extern "C" void kernel_launch(void* const* d_in, const int* in_sizes, int n_in, void* d_out, int out_size, void* d_ws, size_t ws_size,
                              hipStream_t stream) {
  s4_mixer_kernel<<<256, 256, 0, stream>>>((const u16*)d_in[0], (const u16*)d_in[1], (const u16*)d_in[2], (const u16*)d_in[3],
                                           (const u16*)d_in[4], (const u16*)d_in[5], (const u16*)d_in[6], (const u16*)d_in[7],
                                           (const u16*)d_in[8], (const u16*)d_in[10], (const u16*)d_in[11], (const u16*)d_in[12],
                                           (const u16*)d_in[13], (u16*)d_out, (char*)d_ws);
}
```

```cpp
#define CODEPF 1
#define MAP8 1
#include <hip/hip_runtime.h>
#include <hip/hip_bf16.h>

#define NTW 0
#define NTX 0
#define WSEND 0
#define STAMPS 0

typedef unsigned short u16;
typedef short s16x4 __attribute__((ext_vector_type(4)));
typedef short s16x8 __attribute__((ext_vector_type(8)));
typedef __bf16 bf16x2 __attribute__((ext_vector_type(2)));
typedef __bf16 bf16x8 __attribute__((ext_vector_type(8)));
typedef _Float16 f16x2 __attribute__((ext_vector_type(2)));
typedef _Float16 f16x8 __attribute__((ext_vector_type(8)));
typedef float f32x2 __attribute__((ext_vector_type(2)));
typedef float f32x4 __attribute__((ext_vector_type(4)));
typedef float f32x16 __attribute__((ext_vector_type(16)));
typedef unsigned v2u __attribute__((ext_vector_type(2)));
typedef unsigned v4u __attribute__((ext_vector_type(4)));
#define LAS __attribute__((address_space(3)))
#define DEVI __device__ __forceinline__
typedef __amdgpu_buffer_rsrc_t rsrc_t;

constexpr int SEQ = 2048, DM = 512, NS = 128, FF = 1792;
constexpr unsigned WS_EP = 0;
constexpr unsigned WS_U = 0x10000;
constexpr unsigned WS_Y = WS_U + 0x200000;
constexpr unsigned WS_HN = WS_Y + 0x200000;
constexpr unsigned WS_HR = WS_HN + 0x200000;
constexpr unsigned WS_G = WS_HR + 0x200000;
constexpr unsigned WS_END = WS_G + 0x200000;
constexpr unsigned WS_DBG = WS_END;
constexpr int SC1 = 16;
constexpr unsigned TAGM = 0x00070007u;
constexpr unsigned SPIN_CAP = 1u << 16;

constexpr int TROW = 544;
constexpr int TCH = 53 * TROW;
constexpr int WK_CT = 2 * TCH;
constexpr int WK_RED = WK_CT + 2 * 1536;
constexpr int KCP = 4160;
constexpr int WK_KC = 0;
constexpr int WK_UL = 8 * KCP;
constexpr int ULC = 4608;
constexpr int WK_SIZE = 65536;
static_assert(WK_RED + 512 <= WK_SIZE && WK_UL + 2 * ULC <= WK_CT, "lds map");
constexpr int STG = 24576;

DEVI rsrc_t mk(const void* p, unsigned bytes) { return __builtin_amdgcn_make_buffer_rsrc((void*)p, 0, bytes, 0x00020000); }
DEVI float bflo(unsigned v) { return __uint_as_float(v << 16); }
DEVI float bfhi(unsigned v) { return __uint_as_float(v & 0xffff0000u); }
DEVI unsigned pkbf(float a, float b) { bf16x2 r = __builtin_convertvector(((f32x2){a, b}), bf16x2); return __builtin_bit_cast(unsigned, r); }
DEVI unsigned pkh(float a, float b) { f16x2 r = __builtin_convertvector(((f32x2){a, b}), f16x2); return __builtin_bit_cast(unsigned, r); }
DEVI void sched0() { __builtin_amdgcn_sched_barrier(0); }
DEVI void bar() { asm volatile("s_waitcnt lgkmcnt(0)" ::: "memory"); __builtin_amdgcn_s_barrier(); asm volatile("" ::: "memory"); }
template <int C> DEVI float ror_add(float s) { return s + __uint_as_float(__builtin_amdgcn_update_dpp(0u, __float_as_uint(s), 0x120 + C, 0xf, 0xf, true)); }
template <int C> DEVI float ror_max(float s) { return fmaxf(s, __uint_as_float(__builtin_amdgcn_update_dpp(0u, __float_as_uint(s), 0x120 + C, 0xf, 0xf, true))); }
DEVI unsigned max4(v4u q) { return max(max(q.x, q.y), max(q.z, q.w)); }
DEVI unsigned max16(v4u a, v4u b, v4u c, v4u d) {
  unsigned m = max(max(a.x, a.y), a.z); m = max(max(m, a.w), b.x); m = max(max(m, b.y), b.z); m = max(max(m, b.w), c.x);
  unsigned n = max(max(c.y, c.z), c.w); n = max(max(n, d.x), d.y); n = max(max(n, d.z), d.w); return max(m, n);
}
DEVI bool anybad(unsigned m) { return __builtin_amdgcn_ballot_w64(m == ~0u) != 0ull; }
struct cx { f32x2 v, r; };
DEVI cx mkcx(f32x2 v) { return cx{v, (f32x2){-v.y, v.x}}; }
DEVI f32x2 cmul(f32x2 a, cx b) { f32x2 t = (f32x2){a.x, a.x} * b.v; return __builtin_elementwise_fma((f32x2){a.y, a.y}, b.r, t); }
DEVI f32x2 cexp(float a2, float br, float l) {
  float m = __builtin_amdgcn_exp2f(a2 * l), f = __builtin_amdgcn_fractf(br * l);
  return (f32x2){m * __builtin_amdgcn_cosf(f), m * __builtin_amdgcn_sinf(f)};
}
DEVI s16x8 trfrag(LAS char* p0, LAS char* p1) {
  s16x4 lo = __builtin_amdgcn_ds_read_tr16_b64_v4i16((LAS s16x4*)p0), hi = __builtin_amdgcn_ds_read_tr16_b64_v4i16((LAS s16x4*)p1);
  return __builtin_shufflevector(lo, hi, 0, 1, 2, 3, 4, 5, 6, 7);
}

DEVI void row_stats(const float (&a)[8], const float (&b)[8], LAS float* red, int w, int lane, float& mean, float& var) {
  float h[8];
#pragma unroll
  for (int e = 0; e < 8; ++e) {
    float s1 = a[e] + b[e], s2 = fmaf(a[e], a[e], b[e] * b[e]);
    auto r = __builtin_amdgcn_permlane32_swap(__float_as_uint(s1), __float_as_uint(s2), false, false);
    h[e] = __uint_as_float(r[0]) + __uint_as_float(r[1]);
  }
  f32x4 q;
#pragma unroll
  for (int e = 0; e < 4; ++e) {
    auto r = __builtin_amdgcn_permlane16_swap(__float_as_uint(h[e]), __float_as_uint(h[e + 4]), false, false);
    float s = __uint_as_float(r[0]) + __uint_as_float(r[1]);
    s = ror_add<8>(s); s = ror_add<4>(s); s = ror_add<2>(s); s = ror_add<1>(s);
    q[e] = s;
  }
  if ((lane & 15) == 0) *(LAS f32x4*)(red + w * 16 + (lane >> 4) * 4) = q;
  bar();
  const int e = lane & 7;
  float s1 = (red[e] + red[16 + e]) + (red[32 + e] + red[48 + e]);
  float s2 = (red[8 + e] + red[24 + e]) + (red[40 + e] + red[56 + e]);
  mean = s1 * (1.f / DM);
  var = fmaxf(fmaf(-mean, mean, s2 * (1.f / DM)), 0.f);
}
DEVI float rdl(float v, int l) { return __uint_as_float(__builtin_amdgcn_readlane(__float_as_uint(v), l)); }

#define STAMP(i) do {} while (0)

__global__ void __launch_bounds__(256, 1)
s4_mixer_kernel(const u16* __restrict__ x, const u16* __restrict__ Are, const u16* __restrict__ Aim, const u16* __restrict__ Bre,
                const u16* __restrict__ Bim, const u16* __restrict__ Cre, const u16* __restrict__ Cim, const u16* __restrict__ logdt,
                const u16* __restrict__ Dsk, const u16* __restrict__ w1, const u16* __restrict__ b1, const u16* __restrict__ w2,
                const u16* __restrict__ b2, u16* __restrict__ out, char* ws) {
  __shared__ __attribute__((aligned(16))) char stage_s[4 * STG];
  __shared__ __attribute__((aligned(16))) char work_s[WK_SIZE];
  LAS char* const work = (LAS char*)work_s;
  const int t = threadIdx.x, lane = t & 63;
  const int w = __builtin_amdgcn_readfirstlane(t >> 6);
  const int bx = blockIdx.x & 7, bj = blockIdx.x >> 3;
  const int p = bx * 32 + bj;
  const int dt = bj & 7;
  const int q1 = bx * 28 + bj;
  const int dt1 = q1 & 7, ft = q1 >> 3;
  const int st = 4 * bx + (bj >> 3);
  const bool g1 = bj < 28;
  LAS char* const stg = (LAS char*)stage_s + w * STG;
  STAMP(0);

  const rsrc_t rws = mk(ws, WS_END);
  const rsrc_t rws1 = mk(ws, g1 ? WS_END : 0u);
  const rsrc_t rw1 = mk(w1, g1 ? SEQ * FF * 2 : 0u);
  const rsrc_t rw2 = mk(w2, FF * SEQ * 2);
  unsigned spins = 0;

  const int cw = w >> 1, hw = w & 1;
  const int lg = p >> 2, i0 = 2 * (p & 3);
  const int dch = 16 * (lg >> 1) + 2 * (i0 + cw) + (lg & 1);
  unsigned xr[8];
  {
    const rsrc_t rx = mk(x, SEQ * DM * 2);
#pragma unroll
    for (int e = 0; e < 8; ++e) xr[e] = __builtin_amdgcn_raw_buffer_load_b32(rx, t * 4 + (e & 3) * 1024, p * 8192 + (e >> 2) * 4096, NTX);
  }
  const v4u ep = __builtin_amdgcn_raw_buffer_load_b128(rws, WS_EP + (t & 1) * 16, 0, SC1);
  const unsigned dsk = ((const unsigned*)Dsk)[t];
  const unsigned pAr = ((const unsigned*)Are)[dch * 64 + lane], pAi = ((const unsigned*)Aim)[dch * 64 + lane];
  const unsigned pBr = ((const unsigned*)Bre)[dch * 64 + lane], pBi = ((const unsigned*)Bim)[dch * 64 + lane];
  const unsigned pCr = ((const unsigned*)Cre)[dch * 64 + lane], pCi = ((const unsigned*)Cim)[dch * 64 + lane];
  const unsigned ldt2 = ((const unsigned*)logdt)[dch >> 1];
  const v4u b2v = ((const v4u*)b2)[p];
  unsigned cpf;
  {
    unsigned long long pc; asm volatile("s_getpc_b64 %0" : "=s"(pc));
    const rsrc_t rc = mk((const void*)(pc & ~127ull), 24576);
    cpf = __builtin_amdgcn_raw_buffer_load_b32(rc, t * 128, 0, 0);
  }
  sched0();
  const v4u POI = {~0u, ~0u, ~0u, ~0u};
  {
    const unsigned o = WS_Y + (t >> 3) * 32768 + p * 128 + (t & 7) * 16;
    __builtin_amdgcn_raw_buffer_store_b128(POI, rws, o, 0, SC1);
    __builtin_amdgcn_raw_buffer_store_b128(POI, rws, o, 32 * 32768, SC1);
  }
  sched0();
  const unsigned dch16 = ((lane & 7) ^ (((lane >> 4) & 1) << 2)) * 16;
  const unsigned vw1 = (lane >> 3) * (FF * 2) + dch16, vw2 = (lane >> 3) * (SEQ * 2) + dch16;
  const unsigned sw1 = w * 512 * (FF * 2) + ft * 128, sw2 = w * 448 * (SEQ * 2) + st * 128;
#define DMA1(slot, hs) __builtin_amdgcn_raw_ptr_buffer_load_lds(rw1, (LAS void*)(stg + (slot) * 1024), 16, vw1, sw1 + (hs) * 8 * (FF * 2), 0, NTW)
#define DMA2(slot, hs) __builtin_amdgcn_raw_ptr_buffer_load_lds(rw2, (LAS void*)(stg + (slot) * 1024), 16, vw2, sw2 + (hs) * 8 * (SEQ * 2), 0, NTW)
#pragma unroll
  for (int i = 0; i < 24; ++i) DMA1(i, i);
  sched0();
  STAMP(1);

  float v0[8], v1[8];
  v4u tag;
  {
    float a[8], b[8];
#pragma unroll
    for (int e = 0; e < 8; ++e) { a[e] = bflo(xr[e]); b[e] = bfhi(xr[e]); }
    float mean, var;
    asm volatile("s_waitcnt vmcnt(24)" ::: "memory");
    asm volatile("" :: "v"(cpf));
    row_stats(a, b, (LAS float*)(work + WK_RED), w, lane, mean, var);
    const float rs = __builtin_amdgcn_rsqf(var + 1e-5f);
    const float d0 = bflo(dsk), d1 = bfhi(dsk);
    float u0[8], u1[8];
#pragma unroll
    for (int e = 0; e < 8; ++e) {
      const float m = rdl(mean, e), r = rdl(rs, e);
      u0[e] = (a[e] - m) * r; u1[e] = (b[e] - m) * r;
      v0[e] = fmaf(u0[e], d0, a[e]); v1[e] = fmaf(u1[e], d1, b[e]);
    }
    tag = (ep + 0x00010001u) & TAGM;
    v4u c0, c1;
#pragma unroll
    for (int k = 0; k < 4; ++k) {
      c0[k] = ((__builtin_bit_cast(unsigned, __builtin_amdgcn_cvt_pkrtz(u0[2 * k], u0[2 * k + 1])) + 0x00040004u) & ~TAGM) | tag[k];
      c1[k] = ((__builtin_bit_cast(unsigned, __builtin_amdgcn_cvt_pkrtz(u1[2 * k], u1[2 * k + 1])) + 0x00040004u) & ~TAGM) | tag[k];
    }
    const unsigned o = WS_U + (t >> 3) * 65536 + p * 128 + (t & 7) * 16;
    __builtin_amdgcn_raw_buffer_store_b128(c0, rws, o, 0, SC1);
    __builtin_amdgcn_raw_buffer_store_b128(c1, rws, o, 32768, SC1);
  }
  sched0();
  const unsigned ohn = (((t >> 5) * 128 + (p >> 1)) * 2) * 1024 + (p & 1) * 512 + (t & 31) * 16;
  const unsigned og = WS_G + ((dt1 * 112 + 4 * ft + 2 * (w >> 1)) * 2 + (w & 1)) * 1024 + lane * 16;
  __builtin_amdgcn_raw_buffer_store_b128(POI, rws, ohn, WS_HN, SC1);
  __builtin_amdgcn_raw_buffer_store_b128(POI, rws, ohn + 1024, WS_HN, SC1);
  __builtin_amdgcn_raw_buffer_store_b128(POI, rws, ohn, WS_HR, SC1);
  __builtin_amdgcn_raw_buffer_store_b128(POI, rws, ohn + 1024, WS_HR, SC1);
  __builtin_amdgcn_raw_buffer_store_b128(POI, rws1, og, 0, SC1);
  __builtin_amdgcn_raw_buffer_store_b128(POI, rws1, og + 2048, 0, SC1);
  sched0();
  STAMP(2);

  float kinv;
  {
    const float ld = (dch & 1) ? bfhi(ldt2) : bflo(ldt2);
    const float dtv = bflo(pkbf(__builtin_amdgcn_exp2f(ld * 1.44269504088896341f), 0.f));
    float wr[2], wi[2];
#pragma unroll
    for (int k = 0; k < 2; ++k) {
      const float br = dtv * (k ? bfhi(pBr) : bflo(pBr)), bi = dtv * (k ? bfhi(pBi) : bflo(pBi));
      const float cr = k ? bfhi(pCr) : bflo(pCr), ci = k ? bfhi(pCi) : bflo(pCi);
      wr[k] = cr * br - ci * bi; wi[k] = cr * bi + ci * br;
    }
    float mx = fmaxf(fmaxf(fabsf(wr[0]), fabsf(wi[0])), fmaxf(fabsf(wr[1]), fabsf(wi[1])));
    mx = ror_max<8>(mx); mx = ror_max<4>(mx); mx = ror_max<2>(mx); mx = ror_max<1>(mx);
    mx = fmaxf(fmaxf(rdl(mx, 0), rdl(mx, 16)), fmaxf(rdl(mx, 32), rdl(mx, 48)));
    int ex = (int)(__float_as_uint(mx) >> 23);
    ex = ex < 16 ? 16 : (ex > 240 ? 240 : ex);
    const float sc = __uint_as_float((unsigned)(261 - ex) << 23);
    kinv = __uint_as_float((unsigned)(ex - 7) << 23);
    const float a2 = dtv * (hw ? bfhi(pAr) : bflo(pAr)) * 1.44269504088896341f;
    const float bq = dtv * (hw ? bfhi(pAi) : bflo(pAi)) * 0.15915494309189535f;
    const f32x2 W0 = {(hw ? wr[1] : wr[0]) * sc, -(hw ? wi[1] : wi[0]) * sc};
    const cx e1 = mkcx(cexp(a2, -bq, 1.f)), e256 = mkcx(cexp(a2, -bq, 256.f)), e8 = mkcx(cexp(a2, bq, 8.f));
    f32x2 la = {1.f, 0.f}, lb = cexp(a2, bq, 128.f);
    LAS unsigned* tb = (LAS unsigned*)(work + cw * TCH + hw * 256 + lane * 4);
#define TPUT(row, z) tb[(row) * (TROW / 4)] = pkh((z).x, (z).y)
#pragma unroll
    for (int i = 0; i < 16; ++i) {
      TPUT(i, la); TPUT(16 + i, lb);
      if (i < 15) { la = cmul(la, e8); lb = cmul(lb, e8); }
    }
    f32x2 h[8]; h[0] = W0;
#pragma unroll
    for (int i = 1; i < 8; ++i) h[i] = cmul(h[i - 1], e1);
#pragma unroll
    for (int i = 0; i < 8; ++i) TPUT(32 + i, h[i]);
    f32x2 g = cmul(h[0], e256), g4 = cmul(h[4], e256);
    TPUT(40, g); TPUT(41, cmul(h[2], e256)); TPUT(42, g4); TPUT(43, cmul(h[6], e256));
    g = cmul(g, e256); g4 = cmul(g4, e256); TPUT(44, g); TPUT(45, g4);
    g = cmul(g, e256); g4 = cmul(g4, e256); TPUT(46, g); TPUT(47, g4);
#pragma unroll
    for (int i = 0; i < 5; ++i) { g = cmul(g, e256); TPUT(48 + i, g); }
    bar();
    const int r = lane & 15, g16 = lane >> 4;
    LAS char* pa0 = work + cw * TCH + (32 + r) * TROW + g16 * 16;
    LAS char* pa1 = work + cw * TCH + ((r & 3) ? 52 : 48 + (r >> 2)) * TROW + g16 * 16;
    LAS char* pb = work + cw * TCH + (16 * hw + r) * TROW + g16 * 16;
    f32x4 k0 = {0.f, 0.f, 0.f, 0.f}, k1 = {0.f, 0.f, 0.f, 0.f};
#pragma unroll
    for (int ks = 0; ks < 8; ++ks) {
      const f16x8 fb = *(LAS f16x8*)(pb + ks * 64);
      k0 = __builtin_amdgcn_mfma_f32_16x16x32_f16(*(LAS f16x8*)(pa0 + ks * 64), fb, k0, 0, 0, 0);
      k1 = __builtin_amdgcn_mfma_f32_16x16x32_f16(*(LAS f16x8*)(pa1 + ks * 64), fb, k1, 0, 0, 0);
    }
    const int bb = 16 * hw + r;
    LAS u16* ct = (LAS u16*)(work + WK_CT + cw * 1536);
    const int ia = g16 < 2 ? 8 * bb + 4 * g16 : (g16 == 2 ? 256 + 4 * bb : 384 + 2 * bb);
    const int ib = g16 < 3 ? ia + 2 : 448 + 2 * bb;
    *(LAS unsigned*)(ct + ia) = pkh(k0[0], k0[1]);
    *(LAS unsigned*)(ct + ib) = pkh(k0[2], k0[3]);
    const unsigned k1p = pkh(k1[0], k1[1]);
    ct[512 + 32 * g16 + bb] = (u16)k1p;
    ct[640 + 32 * g16 + bb] = (u16)(k1p >> 16);
    bar();
    const int tau = t & 127;
    unsigned E[10];
#pragma unroll
    for (int k = 0; k < 10; ++k) {
      const int l0 = 16 * tau - 4 + 2 * k;
      const unsigned bits = pkbf((float)l0, (float)(l0 + 1));
      const int j0 = l0 < 128 ? (l0 < 0 ? 0 : l0) : (int)(bits & 0xffffu) - 0x4300 + 128;
      const int j1 = l0 < 128 ? (l0 < 0 ? 0 : l0 + 1) : (int)(bits >> 16) - 0x4300 + 128;
      const unsigned val = (unsigned)ct[j0] | ((unsigned)ct[j1] << 16);
      E[k] = l0 < 0 ? 0u : val;
    }
    unsigned O[9];
#pragma unroll
    for (int k = 0; k < 9; ++k) O[k] = __builtin_amdgcn_alignbit(E[k + 1], E[k], 16);
    LAS char* kc = work + WK_KC + cw * 4 * KCP + 32 + tau * 32;
    *(LAS v4u*)(kc) = (v4u){E[2], E[3], E[4], E[5]};             *(LAS v4u*)(kc + 16) = (v4u){E[6], E[7], E[8], E[9]};
    *(LAS v4u*)(kc + KCP) = (v4u){O[1], O[2], O[3], O[4]};       *(LAS v4u*)(kc + KCP + 16) = (v4u){O[5], O[6], O[7], O[8]};
    *(LAS v4u*)(kc + 2 * KCP) = (v4u){E[1], E[2], E[3], E[4]};   *(LAS v4u*)(kc + 2 * KCP + 16) = (v4u){E[5], E[6], E[7], E[8]};
    *(LAS v4u*)(kc + 3 * KCP) = (v4u){O[0], O[1], O[2], O[3]};   *(LAS v4u*)(kc + 3 * KCP + 16) = (v4u){O[4], O[5], O[6], O[7]};
    const v4u Z = {0u, 0u, 0u, 0u};
    if (t < 16) *(LAS v4u*)(work + WK_KC + (t >> 1) * KCP + (t & 1) * 16) = Z;
    if (t >= 64 && t < 128) *(LAS v4u*)(work + WK_UL + ((t >> 5) & 1) * ULC + (t & 31) * 16) = Z;
    bar();
  }
  STAMP(3);
  f16x8 TA[4][8];
  {
    LAS char* pa = work + WK_KC + cw * 4 * KCP + ((lane & 15) >> 2) * KCP + (28 - 8 * (lane >> 4) + 4 * (lane & 3)) * 2 + hw * 256;
#pragma unroll
    for (int i = 0; i < 4; ++i)
#pragma unroll
      for (int mu = 0; mu < 8; ++mu) TA[i][mu] = __builtin_bit_cast(f16x8, trfrag(pa + (8 * mu + i) * 64 + 8, pa + (8 * mu + i) * 64));
  }
  const int trq = (lane & 15) >> 2;
  const int trc = (((2 * ((lane >> 4) & 1) + ((lane & 3) >> 1)) ^ (((trq >> 1) & 1) << 2) ^ ((w >> 1) << 2)) * 16) + 8 * (lane & 1);
  LAS char* const t1a = stg + (8 * (lane >> 5) + trq) * 128 + trc;
  LAS char* const t1b = stg + (8 * (lane >> 5) + trq) * 128 + (trc ^ 64);
  LAS char* const t2a = stg + (4 * (lane >> 5) + trq) * 128 + trc;
  LAS char* const t2b = stg + (4 * (lane >> 5) + trq) * 128 + (trc ^ 64);
  bf16x8 W1[32][2], W2[16][2];
#define GET1(ks, slot) do { W1[ks][0] = __builtin_bit_cast(bf16x8, trfrag(t1a + (slot) * 2048, t1a + (slot) * 2048 + 512)); \
                            W1[ks][1] = __builtin_bit_cast(bf16x8, trfrag(t1b + (slot) * 2048, t1b + (slot) * 2048 + 512)); } while (0)
#define FR2(sub, slot) __builtin_bit_cast(bf16x8, trfrag(((sub) ? t2b : t2a) + (slot) * 2048, ((sub) ? t2b : t2a) + (slot) * 2048 + 1024))
#define GET2(ks, slot) do { W2[ks][0] = FR2(0, slot); W2[ks][1] = FR2(1, slot); } while (0)
#pragma unroll
  for (int i = 0; i < 12; ++i) GET1(i, i);
  sched0();
  STAMP(4);

  {
    const unsigned o = WS_U + lg * 32768 + (t >> 1) * 128 + (i0 + (t & 1)) * 16;
    v4u q0, q1;
    for (;;) {
      q0 = __builtin_amdgcn_raw_buffer_load_b128(rws, o, 0, SC1) ^ tag;
      q1 = __builtin_amdgcn_raw_buffer_load_b128(rws, o, 16384, SC1) ^ tag;
      const v4u z = q0 | q1;
      if (__all((((z.x | z.y) | (z.z | z.w)) & TAGM) == 0u) || ++spins > SPIN_CAP) break;
      asm volatile("s_sleep 1" ::: "memory");
    }
    LAS char* ul = work + WK_UL + (t & 1) * ULC + 512 + (t >> 1) * 16;
    *(LAS v4u*)ul = q0; *(LAS v4u*)(ul + 2048) = q1;
  }
  bar();
  if (blockIdx.x == 0 && t < 2) __builtin_amdgcn_raw_buffer_store_b128(tag, rws, WS_EP + t * 16, 0, SC1);
  STAMP(5);

  {
    f32x4 acc[8];
#pragma unroll
    for (int c = 0; c < 8; ++c) acc[c] = (f32x4){0.f, 0.f, 0.f, 0.f};
    LAS char* pb = work + WK_UL + cw * ULC + (16 * (lane & 15) + 8 * (lane >> 4) - 128 * hw + 144) * 2;
    f16x8 UB[2][8];
#pragma unroll
    for (int g = 0; g < 8; ++g) UB[0][g] = *(LAS f16x8*)(pb + (256 * g + 96) * 2);
#pragma unroll
    for (int i = 0; i < 4; ++i) {
      int n = 0;
#pragma unroll
      for (int g = 0; g < 8; ++g) {
#pragma unroll
        for (int mu = 0; mu + g < 8; ++mu) {
          acc[g + mu] = __builtin_amdgcn_mfma_f32_16x16x32_f16(TA[i][mu], UB[i & 1][g], acc[g + mu], 0, 0, 0);
          if (n % 6 == 2) { DMA1(i * 6 + n / 6, 24 + i * 6 + n / 6); sched0(); }
          if (i < 3 && n % 4 == 0 && n < 32) { UB[(i + 1) & 1][n / 4] = *(LAS f16x8*)(pb + (256 * (n / 4) + 96 - 32 * (i + 1)) * 2); sched0(); }
          ++n;
        }
      }
    }
    sched0();
    STAMP(6);
    LAS char* ex = work + lane * 16;
    f32x4 keep[4];
    if (hw) {
#pragma unroll
      for (int k = 0; k < 4; ++k) { *(LAS f32x4*)(ex + (w * 4 + k) * 1024) = acc[k]; keep[k] = acc[4 + k] * kinv; }
      asm volatile("" ::: "memory");
    } else {
#pragma unroll
      for (int k = 0; k < 4; ++k) { *(LAS f32x4*)(ex + (w * 4 + k) * 1024) = acc[4 + k]; keep[k] = acc[k] * kinv; }
    }
    bar();
    unsigned pk[4][2];
#pragma unroll
    for (int k = 0; k < 4; ++k) {
      const f32x4 y = *(LAS f32x4*)(ex + ((w ^ 1) * 4 + k) * 1024) * kinv + keep[k];
      pk[k][0] = pkbf(y[0], y[1]); pk[k][1] = pkbf(y[2], y[3]);
    }
    const int col = lane & 15, rho = lane >> 4;
    const unsigned oy = WS_Y + (8 * (4 * hw + (rho & 1)) + 2 * (col >> 2) + (rho >> 1)) * 32768 + p * 128 + cw * 64 + (col & 3) * 16;
#pragma unroll
    for (int k = 0; k < 4; k += 2) {
      auto r0 = __builtin_amdgcn_permlane16_swap(pk[k][0], pk[k + 1][0], false, false);
      auto r1 = __builtin_amdgcn_permlane16_swap(pk[k][1], pk[k + 1][1], false, false);
      __builtin_amdgcn_raw_buffer_store_b128((v4u){r0[0], r1[0], r0[1], r1[1]}, rws, oy, k * 8 * 32768, SC1);
    }
  }
  sched0();
  STAMP(7);
#pragma unroll
  for (int i = 0; i < 12; ++i) GET1(12 + i, i);
  asm volatile("s_waitcnt lgkmcnt(0)" ::: "memory");
  sched0();
#pragma unroll
  for (int i = 0; i < 16; ++i) DMA1(i, 48 + i);
#pragma unroll
  for (int i = 0; i < 8; ++i) DMA2(16 + i, i);
  sched0();
  STAMP(8);

  {
    const unsigned o = WS_Y + ((p >> 3) * 2 + (p & 1)) * 32768 + ((t >> 3) * 8 + ((t & 7) >> 1)) * 128 + (t & 1) * 64 + ((p >> 1) & 3) * 16;
    v4u q0, q1;
    for (;;) {
      q0 = __builtin_amdgcn_raw_buffer_load_b128(rws, o, 0, SC1);
      q1 = __builtin_amdgcn_raw_buffer_load_b128(rws, o + 512, 0, SC1);
      if (__all(max(max4(q0), max4(q1)) != ~0u) || ++spins > SPIN_CAP) break;
      asm volatile("s_sleep 1" ::: "memory");
    }
    STAMP(9);
#pragma unroll
    for (int k = 0; k < 4; ++k) {
      v0[2 * k] += bflo(q0[k]); v0[2 * k + 1] += bfhi(q0[k]);
      v1[2 * k] += bflo(q1[k]); v1[2 * k + 1] += bfhi(q1[k]);
    }
    float mean, var;
    row_stats(v0, v1, (LAS float*)(work + WK_RED + 256), w, lane, mean, var);
    const float r1 = __builtin_amdgcn_rsqf(var + 1e-5f);
    const float rs = r1 * __builtin_amdgcn_rsqf(fmaf(var * r1, r1, 1e-5f));
    v4u n0, n1, h0, h1;
#pragma unroll
    for (int k = 0; k < 4; ++k) {
      const float ma = rdl(mean, 2 * k), ra = rdl(rs, 2 * k), mb = rdl(mean, 2 * k + 1), rb = rdl(rs, 2 * k + 1);
      n0[k] = pkbf((v0[2 * k] - ma) * ra, (v0[2 * k + 1] - mb) * rb);
      n1[k] = pkbf((v1[2 * k] - ma) * ra, (v1[2 * k + 1] - mb) * rb);
    }
    __builtin_amdgcn_raw_buffer_store_b128(n0, rws, ohn, WS_HN, SC1);
    __builtin_amdgcn_raw_buffer_store_b128(n1, rws, ohn + 1024, WS_HN, SC1);
    sched0();
#pragma unroll
    for (int k = 0; k < 4; ++k) {
      const float ba = bflo(b2v[k]), bb = bfhi(b2v[k]);
      h0[k] = pkbf(v0[2 * k] + ba, v0[2 * k + 1] + bb);
      h1[k] = pkbf(v1[2 * k] + ba, v1[2 * k + 1] + bb);
    }
    __builtin_amdgcn_raw_buffer_store_b128(h0, rws, ohn, WS_HR, SC1);
    __builtin_amdgcn_raw_buffer_store_b128(h1, rws, ohn + 1024, WS_HR, SC1);
  }
  sched0();
  STAMP(10);
#pragma unroll
  for (int i = 0; i < 8; ++i) GET1(24 + i, i);
#pragma unroll
  for (int i = 0; i < 4; ++i) GET2(i, 8 + i);
  asm volatile("s_waitcnt lgkmcnt(0)" ::: "memory");
  sched0();
#pragma unroll
  for (int i = 0; i < 24; ++i) DMA2(i, 8 + i);
  v2u b1v[4];
  {
    const rsrc_t rb1 = mk(b1, g1 ? FF * 2 : 0u);
#pragma unroll
    for (int k = 0; k < 4; ++k) b1v[k] = __builtin_amdgcn_raw_buffer_load_b64(rb1, (lane >> 5) * 8 + k * 16, ft * 128 + (w >> 1) * 64, 0);
  }
  sched0();
  STAMP(11);

  {
    f32x16 acc[2][2];
#pragma unroll
    for (int a = 0; a < 2; ++a)
#pragma unroll
      for (int b = 0; b < 2; ++b)
#pragma unroll
        for (int r = 0; r < 16; ++r) acc[a][b][r] = 0.f;
    const unsigned va0 = lane * 16 + (w & 1) * 1024, va1 = va0 ^ 1024;
    const unsigned sa = WS_HN + (dt1 * 128 + w * 32) * 2048;
#define LDA1(i) __builtin_amdgcn_raw_buffer_load_b128(rws1, (((i) & 1) ? va1 : va0) + (((i) >> 1) & 1) * 2048, sa + ((i) >> 2) * 4096, SC1)
    v4u q[16];
    for (;;) {
      const unsigned pr = __builtin_amdgcn_raw_buffer_load_b32(rws1, (lane >> 1) * 2048 + 1024 + (lane & 1) * 512 + 508, sa, SC1);
      sched0();
#pragma unroll
      for (int i = 0; i < 16; ++i) { q[i] = LDA1(i); sched0(); }
      if (!(anybad(pr) & (++spins <= SPIN_CAP))) break;
      asm volatile("s_sleep 1" ::: "memory");
    }
    STAMP(12);
#pragma unroll
    for (int bt = 0; bt < 16; ++bt) {
      const int s0 = (bt & 3) * 4;
      if (__builtin_expect(anybad(max16(q[s0], q[s0 + 1], q[s0 + 2], q[s0 + 3])), 0)) {
        bool again;
        do {
          asm volatile("s_sleep 1" ::: "memory");
#pragma unroll
          for (int i = 0; i < 4; ++i) q[s0 + i] = LDA1(bt * 4 + i);
          again = anybad(max16(q[s0], q[s0 + 1], q[s0 + 2], q[s0 + 3])) & (++spins <= SPIN_CAP);
        } while (again);
      }
#pragma unroll
      for (int i = 0; i < 4; ++i) {
        const int ks = bt * 2 + (i >> 1);
        const bf16x8 a = __builtin_bit_cast(bf16x8, q[s0 + i]);
        acc[i & 1][0] = __builtin_amdgcn_mfma_f32_32x32x16_bf16(W1[ks][0], a, acc[i & 1][0], 0, 0, 0);
        acc[i & 1][1] = __builtin_amdgcn_mfma_f32_32x32x16_bf16(W1[ks][1], a, acc[i & 1][1], 0, 0, 0);
        if (bt < 12) q[s0 + i] = LDA1(bt * 4 + 16 + i);
        sched0();
      }
    }
    STAMP(13);
    LAS char* ex = work + lane * 16;
#pragma unroll
    for (int lam = 1; lam < 4; ++lam)
#pragma unroll
      for (int k = 0; k < 4; ++k) {
        const f32x16& s = acc[lam & 1][lam >> 1];
        *(LAS f32x4*)(ex + (((w ^ lam) * 3 + lam - 1) * 4 + k) * 1024) = (f32x4){s[4 * k], s[4 * k + 1], s[4 * k + 2], s[4 * k + 3]};
      }
    bar();
    float gv[16];
#pragma unroll
    for (int k = 0; k < 4; ++k) {
      const f32x4 a = *(LAS f32x4*)(ex + ((w * 3 + 0) * 4 + k) * 1024), b = *(LAS f32x4*)(ex + ((w * 3 + 1) * 4 + k) * 1024),
                  c = *(LAS f32x4*)(ex + ((w * 3 + 2) * 4 + k) * 1024);
#pragma unroll
      for (int j = 0; j < 4; ++j) {
        const float bias = (j & 1) ? bfhi(b1v[k][j >> 1]) : bflo(b1v[k][j >> 1]);
        const float u = ((acc[0][0][4 * k + j] + a[j]) + (b[j] + c[j])) + bias;
        const float z = u * fmaf(u * u, -0.10294324f, -2.30220820f);
        gv[4 * k + j] = u * __builtin_amdgcn_rcpf(1.f + __builtin_amdgcn_exp2f(z));
      }
    }
    __builtin_amdgcn_raw_buffer_store_b128((v4u){pkbf(gv[0], gv[1]), pkbf(gv[2], gv[3]), pkbf(gv[4], gv[5]), pkbf(gv[6], gv[7])}, rws1, og, 0, SC1);
    __builtin_amdgcn_raw_buffer_store_b128((v4u){pkbf(gv[8], gv[9]), pkbf(gv[10], gv[11]), pkbf(gv[12], gv[13]), pkbf(gv[14], gv[15])}, rws1, og + 2048, 0, SC1);
  }
  sched0();
  STAMP(14);
#pragma unroll
  for (int i = 0; i < 12; ++i) GET2(4 + i, i);
  asm volatile("s_waitcnt lgkmcnt(0)" ::: "memory");
  sched0();
#pragma unroll
  for (int i = 0; i < 24; ++i) DMA2(i, 32 + i);
  sched0();
  STAMP(15);

  {
    f32x16 acc[2][2];
    const unsigned va = (lane ^ ((w & 1) << 4)) * 16;
    {
      const unsigned sh = WS_HR + (dt * 128 + 4 * st + w) * 2048;
      v4u r0, r1;
      for (;;) {
        r0 = __builtin_amdgcn_raw_buffer_load_b128(rws, va, sh, SC1);
        r1 = __builtin_amdgcn_raw_buffer_load_b128(rws, va + 1024, sh, SC1);
        if (__all(max(max4(r0), max4(r1)) != ~0u) || ++spins > SPIN_CAP) break;
        asm volatile("s_sleep 1" ::: "memory");
      }
      const int jj = (lane & 31) - 16 * (w & 1) - 8 * (lane >> 5);
      v4u id;
#pragma unroll
      for (int k = 0; k < 4; ++k) id[k] = jj == 2 * k ? 0x3f80u : (jj == 2 * k + 1 ? 0x3f800000u : 0u);
      f32x16 z;
#pragma unroll
      for (int r = 0; r < 16; ++r) z[r] = 0.f;
      acc[0][0] = __builtin_amdgcn_mfma_f32_32x32x16_bf16(__builtin_bit_cast(bf16x8, r0), __builtin_bit_cast(bf16x8, id), z, 0, 0, 0);
      acc[1][0] = __builtin_amdgcn_mfma_f32_32x32x16_bf16(__builtin_bit_cast(bf16x8, r1), __builtin_bit_cast(bf16x8, id), z, 0, 0, 0);
      acc[0][1] = z; acc[1][1] = z;
    }
    const unsigned sa = WS_G + (dt * 112 + w * 28) * 2048;
#define LDA2(i) __builtin_amdgcn_raw_buffer_load_b128(rws, va + ((i) & 3) * 1024, sa + ((i) >> 2) * 4096, SC1)
    v4u q[16];
    for (;;) {
      const unsigned pr = __builtin_amdgcn_raw_buffer_load_b32(rws, (lane < 56 ? lane : 55) * 1024 + 1020, sa, SC1);
      sched0();
#pragma unroll
      for (int i = 0; i < 16; ++i) { q[i] = LDA2(i); sched0(); }
      if (!(anybad(pr) & (++spins <= SPIN_CAP))) break;
      asm volatile("s_sleep 1" ::: "memory");
    }
    STAMP(16);
#pragma unroll
    for (int bt = 0; bt < 14; ++bt) {
      const int s0 = (bt & 3) * 4;
      bf16x8 wa[2][2];
#pragma unroll
      for (int k = 0; k < 2; ++k) {
        const int ks = bt * 2 + k;
        if (ks < 16) { wa[k][0] = W2[ks][0]; wa[k][1] = W2[ks][1]; }
        else { wa[k][0] = FR2(0, ks - 16); wa[k][1] = FR2(1, ks - 16); }
      }
      if (__builtin_expect(anybad(max16(q[s0], q[s0 + 1], q[s0 + 2], q[s0 + 3])), 0)) {
        bool again;
        do {
          asm volatile("s_sleep 1" ::: "memory");
#pragma unroll
          for (int i = 0; i < 4; ++i) q[s0 + i] = LDA2(bt * 4 + i);
          again = anybad(max16(q[s0], q[s0 + 1], q[s0 + 2], q[s0 + 3])) & (++spins <= SPIN_CAP);
        } while (again);
      }
#pragma unroll
      for (int i = 0; i < 4; ++i) {
        const bf16x8 a = __builtin_bit_cast(bf16x8, q[s0 + i]);
        acc[i & 1][0] = __builtin_amdgcn_mfma_f32_32x32x16_bf16(a, wa[i >> 1][0], acc[i & 1][0], 0, 0, 0);
        acc[i & 1][1] = __builtin_amdgcn_mfma_f32_32x32x16_bf16(a, wa[i >> 1][1], acc[i & 1][1], 0, 0, 0);
        if (bt < 10) q[s0 + i] = LDA2(bt * 4 + 16 + i);
        sched0();
      }
    }
    STAMP(17);
    bar();
    LAS char* ex = work + lane * 16;
#pragma unroll
    for (int lam = 1; lam < 4; ++lam)
#pragma unroll
      for (int k = 0; k < 4; ++k) {
        const f32x16& s = acc[k >> 1][lam >> 1];
        const int r = 8 * (lam & 1) + 4 * (k & 1);
        *(LAS f32x4*)(ex + (((w ^ lam) * 3 + lam - 1) * 4 + k) * 1024) = (f32x4){s[r], s[r + 1], s[r + 2], s[r + 3]};
      }
    bar();
    f32x4 o[4];
#pragma unroll
    for (int k = 0; k < 4; ++k) {
      const f32x4 a = *(LAS f32x4*)(ex + ((w * 3 + 0) * 4 + k) * 1024), b = *(LAS f32x4*)(ex + ((w * 3 + 1) * 4 + k) * 1024),
                  c = *(LAS f32x4*)(ex + ((w * 3 + 2) * 4 + k) * 1024);
      const f32x16& s = acc[k >> 1][0];
      const int r = 4 * (k & 1);
      o[k] = ((f32x4){s[r], s[r + 1], s[r + 2], s[r + 3]} + a) + (b + c);
    }
    const rsrc_t ro = mk(out, SEQ * DM * 2);
    const unsigned vo = (lane & 31) * 1024 + (lane >> 5) * 16, so = (64 * st + 32 * (w >> 1)) * 1024 + dt * 128 + (w & 1) * 64;
#pragma unroll
    for (int k = 0; k < 2; ++k)
      __builtin_amdgcn_raw_buffer_store_b128((v4u){pkbf(o[k][0], o[2 + k][0]), pkbf(o[k][1], o[2 + k][1]), pkbf(o[k][2], o[2 + k][2]), pkbf(o[k][3], o[2 + k][3])},
                                             ro, vo + k * 32, so, 0);
  }
  STAMP(18);
}

extern "C" void kernel_launch(void* const* d_in, const int* in_sizes, int n_in, void* d_out, int out_size, void* d_ws, size_t ws_size,
                              hipStream_t stream) {
  s4_mixer_kernel<<<256, 256, 0, stream>>>((const u16*)d_in[0], (const u16*)d_in[1], (const u16*)d_in[2], (const u16*)d_in[3],
                                           (const u16*)d_in[4], (const u16*)d_in[5], (const u16*)d_in[6], (const u16*)d_in[7],
                                           (const u16*)d_in[8], (const u16*)d_in[10], (const u16*)d_in[11], (const u16*)d_in[12],
                                           (const u16*)d_in[13], (u16*)d_out, (char*)d_ws + (WSEND ? ws_size - (16u << 20) : 0));
}
```

```cpp
#define CODEPF 1
#define MAP8 1
#define PROBE 0
#include <hip/hip_runtime.h>
#include <hip/hip_bf16.h>

#define NTW 0
#define NTX 0
#define WSEND 0
#define STAMPS 0

typedef unsigned short u16;
typedef short s16x4 __attribute__((ext_vector_type(4)));
typedef short s16x8 __attribute__((ext_vector_type(8)));
typedef __bf16 bf16x2 __attribute__((ext_vector_type(2)));
typedef __bf16 bf16x8 __attribute__((ext_vector_type(8)));
typedef _Float16 f16x2 __attribute__((ext_vector_type(2)));
typedef _Float16 f16x8 __attribute__((ext_vector_type(8)));
typedef float f32x2 __attribute__((ext_vector_type(2)));
typedef float f32x4 __attribute__((ext_vector_type(4)));
typedef float f32x16 __attribute__((ext_vector_type(16)));
typedef unsigned v2u __attribute__((ext_vector_type(2)));
typedef unsigned v4u __attribute__((ext_vector_type(4)));
#define LAS __attribute__((address_space(3)))
#define DEVI __device__ __forceinline__
typedef __amdgpu_buffer_rsrc_t rsrc_t;

constexpr int SEQ = 2048, DM = 512, NS = 128, FF = 1792;
constexpr unsigned WS_EP = 0;
constexpr unsigned WS_U = 0x10000;
constexpr unsigned WS_Y = WS_U + 0x200000;
constexpr unsigned WS_HN = WS_Y + 0x200000;
constexpr unsigned WS_HR = WS_HN + 0x200000;
constexpr unsigned WS_G = WS_HR + 0x200000;
constexpr unsigned WS_END = WS_G + 0x200000;
constexpr unsigned WS_DBG = WS_END;
constexpr int SC1 = 16;
constexpr unsigned TAGM = 0x00070007u;
constexpr unsigned SPIN_CAP = 1u << 16;

constexpr int TROW = 544;
constexpr int TCH = 53 * TROW;
constexpr int WK_CT = 2 * TCH;
constexpr int WK_RED = WK_CT + 2 * 1536;
constexpr int KCP = 4160;
constexpr int WK_KC = 0;
constexpr int WK_UL = 8 * KCP;
constexpr int ULC = 4608;
constexpr int WK_SIZE = 65536;
static_assert(WK_RED + 512 <= WK_SIZE && WK_UL + 2 * ULC <= WK_CT, "lds map");
constexpr int STG = 24576;

DEVI rsrc_t mk(const void* p, unsigned bytes) { return __builtin_amdgcn_make_buffer_rsrc((void*)p, 0, bytes, 0x00020000); }
DEVI float bflo(unsigned v) { return __uint_as_float(v << 16); }
DEVI float bfhi(unsigned v) { return __uint_as_float(v & 0xffff0000u); }
DEVI unsigned pkbf(float a, float b) { bf16x2 r = __builtin_convertvector(((f32x2){a, b}), bf16x2); return __builtin_bit_cast(unsigned, r); }
DEVI unsigned pkh(float a, float b) { f16x2 r = __builtin_convertvector(((f32x2){a, b}), f16x2); return __builtin_bit_cast(unsigned, r); }
DEVI void sched0() { __builtin_amdgcn_sched_barrier(0); }
DEVI void bar() { asm volatile("s_waitcnt lgkmcnt(0)" ::: "memory"); __builtin_amdgcn_s_barrier(); asm volatile("" ::: "memory"); }
template <int C> DEVI float ror_add(float s) { return s + __uint_as_float(__builtin_amdgcn_update_dpp(0u, __float_as_uint(s), 0x120 + C, 0xf, 0xf, true)); }
template <int C> DEVI float ror_max(float s) { return fmaxf(s, __uint_as_float(__builtin_amdgcn_update_dpp(0u, __float_as_uint(s), 0x120 + C, 0xf, 0xf, true))); }
DEVI unsigned max4(v4u q) { return max(max(q.x, q.y), max(q.z, q.w)); }
DEVI unsigned max16(v4u a, v4u b, v4u c, v4u d) {
  unsigned m = max(max(a.x, a.y), a.z); m = max(max(m, a.w), b.x); m = max(max(m, b.y), b.z); m = max(max(m, b.w), c.x);
  unsigned n = max(max(c.y, c.z), c.w); n = max(max(n, d.x), d.y); n = max(max(n, d.z), d.w); return max(m, n);
}
DEVI bool anybad(unsigned m) { return __builtin_amdgcn_ballot_w64(m == ~0u) != 0ull; }
struct cx { f32x2 v, r; };
DEVI cx mkcx(f32x2 v) { return cx{v, (f32x2){-v.y, v.x}}; }
DEVI f32x2 cmul(f32x2 a, cx b) { f32x2 t = (f32x2){a.x, a.x} * b.v; return __builtin_elementwise_fma((f32x2){a.y, a.y}, b.r, t); }
DEVI f32x2 cexp(float a2, float br, float l) {
  float m = __builtin_amdgcn_exp2f(a2 * l), f = __builtin_amdgcn_fractf(br * l);
  return (f32x2){m * __builtin_amdgcn_cosf(f), m * __builtin_amdgcn_sinf(f)};
}
DEVI s16x8 trfrag(LAS char* p0, LAS char* p1) {
  s16x4 lo = __builtin_amdgcn_ds_read_tr16_b64_v4i16((LAS s16x4*)p0), hi = __builtin_amdgcn_ds_read_tr16_b64_v4i16((LAS s16x4*)p1);
  return __builtin_shufflevector(lo, hi, 0, 1, 2, 3, 4, 5, 6, 7);
}

DEVI void row_stats(const float (&a)[8], const float (&b)[8], LAS float* red, int w, int lane, float& mean, float& var) {
  float h[8];
#pragma unroll
  for (int e = 0; e < 8; ++e) {
    float s1 = a[e] + b[e], s2 = fmaf(a[e], a[e], b[e] * b[e]);
    auto r = __builtin_amdgcn_permlane32_swap(__float_as_uint(s1), __float_as_uint(s2), false, false);
    h[e] = __uint_as_float(r[0]) + __uint_as_float(r[1]);
  }
  f32x4 q;
#pragma unroll
  for (int e = 0; e < 4; ++e) {
    auto r = __builtin_amdgcn_permlane16_swap(__float_as_uint(h[e]), __float_as_uint(h[e + 4]), false, false);
    float s = __uint_as_float(r[0]) + __uint_as_float(r[1]);
    s = ror_add<8>(s); s = ror_add<4>(s); s = ror_add<2>(s); s = ror_add<1>(s);
    q[e] = s;
  }
  if ((lane & 15) == 0) *(LAS f32x4*)(red + w * 16 + (lane >> 4) * 4) = q;
  bar();
  const int e = lane & 7;
  float s1 = (red[e] + red[16 + e]) + (red[32 + e] + red[48 + e]);
  float s2 = (red[8 + e] + red[24 + e]) + (red[40 + e] + red[56 + e]);
  mean = s1 * (1.f / DM);
  var = fmaxf(fmaf(-mean, mean, s2 * (1.f / DM)), 0.f);
}
DEVI float rdl(float v, int l) { return __uint_as_float(__builtin_amdgcn_readlane(__float_as_uint(v), l)); }

#define STAMP(i) do { if ((i) == 0 || (i) == PROBE) { sched0(); unsigned long long tt_ = __builtin_amdgcn_s_memrealtime(); if ((i) == 0) pt0 = tt_; if ((i) == PROBE) pt1 = tt_; asm volatile("" :: "s"(tt_)); sched0(); } } while (0)

__global__ void __launch_bounds__(256, 1)
s4_mixer_kernel(const u16* __restrict__ x, const u16* __restrict__ Are, const u16* __restrict__ Aim, const u16* __restrict__ Bre,
                const u16* __restrict__ Bim, const u16* __restrict__ Cre, const u16* __restrict__ Cim, const u16* __restrict__ logdt,
                const u16* __restrict__ Dsk, const u16* __restrict__ w1, const u16* __restrict__ b1, const u16* __restrict__ w2,
                const u16* __restrict__ b2, u16* __restrict__ out, char* ws) {
  __shared__ __attribute__((aligned(16))) char stage_s[4 * STG];
  __shared__ __attribute__((aligned(16))) char work_s[WK_SIZE];
  LAS char* const work = (LAS char*)work_s;
  const int t = threadIdx.x, lane = t & 63;
  const int w = __builtin_amdgcn_readfirstlane(t >> 6);
  const int bx = blockIdx.x & 7, bj = blockIdx.x >> 3;
  const int p = bx * 32 + bj;
  const int dt = bj & 7;
  const int q1 = bx * 28 + bj;
  const int dt1 = q1 & 7, ft = q1 >> 3;
  const int st = 4 * bx + (bj >> 3);
  const bool g1 = bj < 28;
  LAS char* const stg = (LAS char*)stage_s + w * STG;
  unsigned long long pt0 = 0, pt1 = 0;
  STAMP(0);

  const rsrc_t rws = mk(ws, WS_END);
  const rsrc_t rws1 = mk(ws, g1 ? WS_END : 0u);
  const rsrc_t rw1 = mk(w1, g1 ? SEQ * FF * 2 : 0u);
  const rsrc_t rw2 = mk(w2, FF * SEQ * 2);
  unsigned spins = 0;

  const int cw = w >> 1, hw = w & 1;
  const int lg = p >> 2, i0 = 2 * (p & 3);
  const int dch = 16 * (lg >> 1) + 2 * (i0 + cw) + (lg & 1);
  unsigned xr[8];
  {
    const rsrc_t rx = mk(x, SEQ * DM * 2);
#pragma unroll
    for (int e = 0; e < 8; ++e) xr[e] = __builtin_amdgcn_raw_buffer_load_b32(rx, t * 4 + (e & 3) * 1024, p * 8192 + (e >> 2) * 4096, NTX);
  }
  const v4u ep = __builtin_amdgcn_raw_buffer_load_b128(rws, WS_EP + (t & 1) * 16, 0, SC1);
  const unsigned dsk = ((const unsigned*)Dsk)[t];
  const unsigned pAr = ((const unsigned*)Are)[dch * 64 + lane], pAi = ((const unsigned*)Aim)[dch * 64 + lane];
  const unsigned pBr = ((const unsigned*)Bre)[dch * 64 + lane], pBi = ((const unsigned*)Bim)[dch * 64 + lane];
  const unsigned pCr = ((const unsigned*)Cre)[dch * 64 + lane], pCi = ((const unsigned*)Cim)[dch * 64 + lane];
  const unsigned ldt2 = ((const unsigned*)logdt)[dch >> 1];
  const v4u b2v = ((const v4u*)b2)[p];
  unsigned cpf;
  {
    unsigned long long pc; asm volatile("s_getpc_b64 %0" : "=s"(pc));
    const rsrc_t rc = mk((const void*)(pc & ~127ull), 24576);
    cpf = __builtin_amdgcn_raw_buffer_load_b32(rc, t * 128, 0, 0);
  }
  sched0();
  const v4u POI = {~0u, ~0u, ~0u, ~0u};
  {
    const unsigned o = WS_Y + (t >> 3) * 32768 + p * 128 + (t & 7) * 16;
    __builtin_amdgcn_raw_buffer_store_b128(POI, rws, o, 0, SC1);
    __builtin_amdgcn_raw_buffer_store_b128(POI, rws, o, 32 * 32768, SC1);
  }
  sched0();
  const unsigned dch16 = ((lane & 7) ^ (((lane >> 4) & 1) << 2)) * 16;
  const unsigned vw1 = (lane >> 3) * (FF * 2) + dch16, vw2 = (lane >> 3) * (SEQ * 2) + dch16;
  const unsigned sw1 = w * 512 * (FF * 2) + ft * 128, sw2 = w * 448 * (SEQ * 2) + st * 128;
#define DMA1(slot, hs) __builtin_amdgcn_raw_ptr_buffer_load_lds(rw1, (LAS void*)(stg + (slot) * 1024), 16, vw1, sw1 + (hs) * 8 * (FF * 2), 0, NTW)
#define DMA2(slot, hs) __builtin_amdgcn_raw_ptr_buffer_load_lds(rw2, (LAS void*)(stg + (slot) * 1024), 16, vw2, sw2 + (hs) * 8 * (SEQ * 2), 0, NTW)
#pragma unroll
  for (int i = 0; i < 24; ++i) DMA1(i, i);
  sched0();
  STAMP(1);

  float v0[8], v1[8];
  v4u tag;
  {
    float a[8], b[8];
#pragma unroll
    for (int e = 0; e < 8; ++e) { a[e] = bflo(xr[e]); b[e] = bfhi(xr[e]); }
    float mean, var;
    asm volatile("s_waitcnt vmcnt(24)" ::: "memory");
    asm volatile("" :: "v"(cpf));
    row_stats(a, b, (LAS float*)(work + WK_RED), w, lane, mean, var);
    const float rs = __builtin_amdgcn_rsqf(var + 1e-5f);
    const float d0 = bflo(dsk), d1 = bfhi(dsk);
    float u0[8], u1[8];
#pragma unroll
    for (int e = 0; e < 8; ++e) {
      const float m = rdl(mean, e), r = rdl(rs, e);
      u0[e] = (a[e] - m) * r; u1[e] = (b[e] - m) * r;
      v0[e] = fmaf(u0[e], d0, a[e]); v1[e] = fmaf(u1[e], d1, b[e]);
    }
    tag = (ep + 0x00010001u) & TAGM;
    v4u c0, c1;
#pragma unroll
    for (int k = 0; k < 4; ++k) {
      c0[k] = ((__builtin_bit_cast(unsigned, __builtin_amdgcn_cvt_pkrtz(u0[2 * k], u0[2 * k + 1])) + 0x00040004u) & ~TAGM) | tag[k];
      c1[k] = ((__builtin_bit_cast(unsigned, __builtin_amdgcn_cvt_pkrtz(u1[2 * k], u1[2 * k + 1])) + 0x00040004u) & ~TAGM) | tag[k];
    }
    const unsigned o = WS_U + (t >> 3) * 65536 + p * 128 + (t & 7) * 16;
    __builtin_amdgcn_raw_buffer_store_b128(c0, rws, o, 0, SC1);
    __builtin_amdgcn_raw_buffer_store_b128(c1, rws, o, 32768, SC1);
  }
  sched0();
  const unsigned ohn = (((t >> 5) * 128 + (p >> 1)) * 2) * 1024 + (p & 1) * 512 + (t & 31) * 16;
  const unsigned og = WS_G + ((dt1 * 112 + 4 * ft + 2 * (w >> 1)) * 2 + (w & 1)) * 1024 + lane * 16;
  __builtin_amdgcn_raw_buffer_store_b128(POI, rws, ohn, WS_HN, SC1);
  __builtin_amdgcn_raw_buffer_store_b128(POI, rws, ohn + 1024, WS_HN, SC1);
  __builtin_amdgcn_raw_buffer_store_b128(POI, rws, ohn, WS_HR, SC1);
  __builtin_amdgcn_raw_buffer_store_b128(POI, rws, ohn + 1024, WS_HR, SC1);
  __builtin_amdgcn_raw_buffer_store_b128(POI, rws1, og, 0, SC1);
  __builtin_amdgcn_raw_buffer_store_b128(POI, rws1, og + 2048, 0, SC1);
  sched0();
  STAMP(2);

  float kinv;
  {
    const float ld = (dch & 1) ? bfhi(ldt2) : bflo(ldt2);
    const float dtv = bflo(pkbf(__builtin_amdgcn_exp2f(ld * 1.44269504088896341f), 0.f));
    float wr[2], wi[2];
#pragma unroll
    for (int k = 0; k < 2; ++k) {
      const float br = dtv * (k ? bfhi(pBr) : bflo(pBr)), bi = dtv * (k ? bfhi(pBi) : bflo(pBi));
      const float cr = k ? bfhi(pCr) : bflo(pCr), ci = k ? bfhi(pCi) : bflo(pCi);
      wr[k] = cr * br - ci * bi; wi[k] = cr * bi + ci * br;
    }
    float mx = fmaxf(fmaxf(fabsf(wr[0]), fabsf(wi[0])), fmaxf(fabsf(wr[1]), fabsf(wi[1])));
    mx = ror_max<8>(mx); mx = ror_max<4>(mx); mx = ror_max<2>(mx); mx = ror_max<1>(mx);
    mx = fmaxf(fmaxf(rdl(mx, 0), rdl(mx, 16)), fmaxf(rdl(mx, 32), rdl(mx, 48)));
    int ex = (int)(__float_as_uint(mx) >> 23);
    ex = ex < 16 ? 16 : (ex > 240 ? 240 : ex);
    const float sc = __uint_as_float((unsigned)(261 - ex) << 23);
    kinv = __uint_as_float((unsigned)(ex - 7) << 23);
    const float a2 = dtv * (hw ? bfhi(pAr) : bflo(pAr)) * 1.44269504088896341f;
    const float bq = dtv * (hw ? bfhi(pAi) : bflo(pAi)) * 0.15915494309189535f;
    const f32x2 W0 = {(hw ? wr[1] : wr[0]) * sc, -(hw ? wi[1] : wi[0]) * sc};
    const cx e1 = mkcx(cexp(a2, -bq, 1.f)), e256 = mkcx(cexp(a2, -bq, 256.f)), e8 = mkcx(cexp(a2, bq, 8.f));
    f32x2 la = {1.f, 0.f}, lb = cexp(a2, bq, 128.f);
    LAS unsigned* tb = (LAS unsigned*)(work + cw * TCH + hw * 256 + lane * 4);
#define TPUT(row, z) tb[(row) * (TROW / 4)] = pkh((z).x, (z).y)
#pragma unroll
    for (int i = 0; i < 16; ++i) {
      TPUT(i, la); TPUT(16 + i, lb);
      if (i < 15) { la = cmul(la, e8); lb = cmul(lb, e8); }
    }
    f32x2 h[8]; h[0] = W0;
#pragma unroll
    for (int i = 1; i < 8; ++i) h[i] = cmul(h[i - 1], e1);
#pragma unroll
    for (int i = 0; i < 8; ++i) TPUT(32 + i, h[i]);
    f32x2 g = cmul(h[0], e256), g4 = cmul(h[4], e256);
    TPUT(40, g); TPUT(41, cmul(h[2], e256)); TPUT(42, g4); TPUT(43, cmul(h[6], e256));
    g = cmul(g, e256); g4 = cmul(g4, e256); TPUT(44, g); TPUT(45, g4);
    g = cmul(g, e256); g4 = cmul(g4, e256); TPUT(46, g); TPUT(47, g4);
#pragma unroll
    for (int i = 0; i < 5; ++i) { g = cmul(g, e256); TPUT(48 + i, g); }
    bar();
    const int r = lane & 15, g16 = lane >> 4;
    LAS char* pa0 = work + cw * TCH + (32 + r) * TROW + g16 * 16;
    LAS char* pa1 = work + cw * TCH + ((r & 3) ? 52 : 48 + (r >> 2)) * TROW + g16 * 16;
    LAS char* pb = work + cw * TCH + (16 * hw + r) * TROW + g16 * 16;
    f32x4 k0 = {0.f, 0.f, 0.f, 0.f}, k1 = {0.f, 0.f, 0.f, 0.f};
#pragma unroll
    for (int ks = 0; ks < 8; ++ks) {
      const f16x8 fb = *(LAS f16x8*)(pb + ks * 64);
      k0 = __builtin_amdgcn_mfma_f32_16x16x32_f16(*(LAS f16x8*)(pa0 + ks * 64), fb, k0, 0, 0, 0);
      k1 = __builtin_amdgcn_mfma_f32_16x16x32_f16(*(LAS f16x8*)(pa1 + ks * 64), fb, k1, 0, 0, 0);
    }
    const int bb = 16 * hw + r;
    LAS u16* ct = (LAS u16*)(work + WK_CT + cw * 1536);
    const int ia = g16 < 2 ? 8 * bb + 4 * g16 : (g16 == 2 ? 256 + 4 * bb : 384 + 2 * bb);
    const int ib = g16 < 3 ? ia + 2 : 448 + 2 * bb;
    *(LAS unsigned*)(ct + ia) = pkh(k0[0], k0[1]);
    *(LAS unsigned*)(ct + ib) = pkh(k0[2], k0[3]);
    const unsigned k1p = pkh(k1[0], k1[1]);
    ct[512 + 32 * g16 + bb] = (u16)k1p;
    ct[640 + 32 * g16 + bb] = (u16)(k1p >> 16);
    bar();
    const int tau = t & 127;
    unsigned E[10];
#pragma unroll
    for (int k = 0; k < 10; ++k) {
      const int l0 = 16 * tau - 4 + 2 * k;
      const unsigned bits = pkbf((float)l0, (float)(l0 + 1));
      const int j0 = l0 < 128 ? (l0 < 0 ? 0 : l0) : (int)(bits & 0xffffu) - 0x4300 + 128;
      const int j1 = l0 < 128 ? (l0 < 0 ? 0 : l0 + 1) : (int)(bits >> 16) - 0x4300 + 128;
      const unsigned val = (unsigned)ct[j0] | ((unsigned)ct[j1] << 16);
      E[k] = l0 < 0 ? 0u : val;
    }
    unsigned O[9];
#pragma unroll
    for (int k = 0; k < 9; ++k) O[k] = __builtin_amdgcn_alignbit(E[k + 1], E[k], 16);
    LAS char* kc = work + WK_KC + cw * 4 * KCP + 32 + tau * 32;
    *(LAS v4u*)(kc) = (v4u){E[2], E[3], E[4], E[5]};             *(LAS v4u*)(kc + 16) = (v4u){E[6], E[7], E[8], E[9]};
    *(LAS v4u*)(kc + KCP) = (v4u){O[1], O[2], O[3], O[4]};       *(LAS v4u*)(kc + KCP + 16) = (v4u){O[5], O[6], O[7], O[8]};
    *(LAS v4u*)(kc + 2 * KCP) = (v4u){E[1], E[2], E[3], E[4]};   *(LAS v4u*)(kc + 2 * KCP + 16) = (v4u){E[5], E[6], E[7], E[8]};
    *(LAS v4u*)(kc + 3 * KCP) = (v4u){O[0], O[1], O[2], O[3]};   *(LAS v4u*)(kc + 3 * KCP + 16) = (v4u){O[4], O[5], O[6], O[7]};
    const v4u Z = {0u, 0u, 0u, 0u};
    if (t < 16) *(LAS v4u*)(work + WK_KC + (t >> 1) * KCP + (t & 1) * 16) = Z;
    if (t >= 64 && t < 128) *(LAS v4u*)(work + WK_UL + ((t >> 5) & 1) * ULC + (t & 31) * 16) = Z;
    bar();
  }
  STAMP(3);
  f16x8 TA[4][8];
  {
    LAS char* pa = work + WK_KC + cw * 4 * KCP + ((lane & 15) >> 2) * KCP + (28 - 8 * (lane >> 4) + 4 * (lane & 3)) * 2 + hw * 256;
#pragma unroll
    for (int i = 0; i < 4; ++i)
#pragma unroll
      for (int mu = 0; mu < 8; ++mu) TA[i][mu] = __builtin_bit_cast(f16x8, trfrag(pa + (8 * mu + i) * 64 + 8, pa + (8 * mu + i) * 64));
  }
  const int trq = (lane & 15) >> 2;
  const int trc = (((2 * ((lane >> 4) & 1) + ((lane & 3) >> 1)) ^ (((trq >> 1) & 1) << 2) ^ ((w >> 1) << 2)) * 16) + 8 * (lane & 1);
  LAS char* const t1a = stg + (8 * (lane >> 5) + trq) * 128 + trc;
  LAS char* const t1b = stg + (8 * (lane >> 5) + trq) * 128 + (trc ^ 64);
  LAS char* const t2a = stg + (4 * (lane >> 5) + trq) * 128 + trc;
  LAS char* const t2b = stg + (4 * (lane >> 5) + trq) * 128 + (trc ^ 64);
  bf16x8 W1[32][2], W2[16][2];
#define GET1(ks, slot) do { W1[ks][0] = __builtin_bit_cast(bf16x8, trfrag(t1a + (slot) * 2048, t1a + (slot) * 2048 + 512)); \
                            W1[ks][1] = __builtin_bit_cast(bf16x8, trfrag(t1b + (slot) * 2048, t1b + (slot) * 2048 + 512)); } while (0)
#define FR2(sub, slot) __builtin_bit_cast(bf16x8, trfrag(((sub) ? t2b : t2a) + (slot) * 2048, ((sub) ? t2b : t2a) + (slot) * 2048 + 1024))
#define GET2(ks, slot) do { W2[ks][0] = FR2(0, slot); W2[ks][1] = FR2(1, slot); } while (0)
#pragma unroll
  for (int i = 0; i < 12; ++i) GET1(i, i);
  sched0();
  STAMP(4);

  {
    const unsigned o = WS_U + lg * 32768 + (t >> 1) * 128 + (i0 + (t & 1)) * 16;
    v4u q0, q1;
    for (;;) {
      q0 = __builtin_amdgcn_raw_buffer_load_b128(rws, o, 0, SC1) ^ tag;
      q1 = __builtin_amdgcn_raw_buffer_load_b128(rws, o, 16384, SC1) ^ tag;
      const v4u z = q0 | q1;
      if (__all((((z.x | z.y) | (z.z | z.w)) & TAGM) == 0u) || ++spins > SPIN_CAP) break;
      asm volatile("s_sleep 1" ::: "memory");
    }
    LAS char* ul = work + WK_UL + (t & 1) * ULC + 512 + (t >> 1) * 16;
    *(LAS v4u*)ul = q0; *(LAS v4u*)(ul + 2048) = q1;
  }
  bar();
  if (blockIdx.x == 0 && t < 2) __builtin_amdgcn_raw_buffer_store_b128(tag, rws, WS_EP + t * 16, 0, SC1);
  STAMP(5);

  {
    f32x4 acc[8];
#pragma unroll
    for (int c = 0; c < 8; ++c) acc[c] = (f32x4){0.f, 0.f, 0.f, 0.f};
    LAS char* pb = work + WK_UL + cw * ULC + (16 * (lane & 15) + 8 * (lane >> 4) - 128 * hw + 144) * 2;
    f16x8 UB[2][8];
#pragma unroll
    for (int g = 0; g < 8; ++g) UB[0][g] = *(LAS f16x8*)(pb + (256 * g + 96) * 2);
#pragma unroll
    for (int i = 0; i < 4; ++i) {
      int n = 0;
#pragma unroll
      for (int g = 0; g < 8; ++g) {
#pragma unroll
        for (int mu = 0; mu + g < 8; ++mu) {
          acc[g + mu] = __builtin_amdgcn_mfma_f32_16x16x32_f16(TA[i][mu], UB[i & 1][g], acc[g + mu], 0, 0, 0);
          if (n % 6 == 2) { DMA1(i * 6 + n / 6, 24 + i * 6 + n / 6); sched0(); }
          if (i < 3 && n % 4 == 0 && n < 32) { UB[(i + 1) & 1][n / 4] = *(LAS f16x8*)(pb + (256 * (n / 4) + 96 - 32 * (i + 1)) * 2); sched0(); }
          ++n;
        }
      }
    }
    sched0();
    STAMP(6);
    LAS char* ex = work + lane * 16;
    f32x4 keep[4];
    if (hw) {
#pragma unroll
      for (int k = 0; k < 4; ++k) { *(LAS f32x4*)(ex + (w * 4 + k) * 1024) = acc[k]; keep[k] = acc[4 + k] * kinv; }
      asm volatile("" ::: "memory");
    } else {
#pragma unroll
      for (int k = 0; k < 4; ++k) { *(LAS f32x4*)(ex + (w * 4 + k) * 1024) = acc[4 + k]; keep[k] = acc[k] * kinv; }
    }
    bar();
    unsigned pk[4][2];
#pragma unroll
    for (int k = 0; k < 4; ++k) {
      const f32x4 y = *(LAS f32x4*)(ex + ((w ^ 1) * 4 + k) * 1024) * kinv + keep[k];
      pk[k][0] = pkbf(y[0], y[1]); pk[k][1] = pkbf(y[2], y[3]);
    }
    const int col = lane & 15, rho = lane >> 4;
    const unsigned oy = WS_Y + (8 * (4 * hw + (rho & 1)) + 2 * (col >> 2) + (rho >> 1)) * 32768 + p * 128 + cw * 64 + (col & 3) * 16;
#pragma unroll
    for (int k = 0; k < 4; k += 2) {
      auto r0 = __builtin_amdgcn_permlane16_swap(pk[k][0], pk[k + 1][0], false, false);
      auto r1 = __builtin_amdgcn_permlane16_swap(pk[k][1], pk[k + 1][1], false, false);
      __builtin_amdgcn_raw_buffer_store_b128((v4u){r0[0], r1[0], r0[1], r1[1]}, rws, oy, k * 8 * 32768, SC1);
    }
  }
  sched0();
  STAMP(7);
#pragma unroll
  for (int i = 0; i < 12; ++i) GET1(12 + i, i);
  asm volatile("s_waitcnt lgkmcnt(0)" ::: "memory");
  sched0();
#pragma unroll
  for (int i = 0; i < 16; ++i) DMA1(i, 48 + i);
#pragma unroll
  for (int i = 0; i < 8; ++i) DMA2(16 + i, i);
  sched0();
  STAMP(8);

  {
    const unsigned o = WS_Y + ((p >> 3) * 2 + (p & 1)) * 32768 + ((t >> 3) * 8 + ((t & 7) >> 1)) * 128 + (t & 1) * 64 + ((p >> 1) & 3) * 16;
    v4u q0, q1;
    for (;;) {
      q0 = __builtin_amdgcn_raw_buffer_load_b128(rws, o, 0, SC1);
      q1 = __builtin_amdgcn_raw_buffer_load_b128(rws, o + 512, 0, SC1);
      if (__all(max(max4(q0), max4(q1)) != ~0u) || ++spins > SPIN_CAP) break;
      asm volatile("s_sleep 1" ::: "memory");
    }
    STAMP(9);
#pragma unroll
    for (int k = 0; k < 4; ++k) {
      v0[2 * k] += bflo(q0[k]); v0[2 * k + 1] += bfhi(q0[k]);
      v1[2 * k] += bflo(q1[k]); v1[2 * k + 1] += bfhi(q1[k]);
    }
    float mean, var;
    row_stats(v0, v1, (LAS float*)(work + WK_RED + 256), w, lane, mean, var);
    const float r1 = __builtin_amdgcn_rsqf(var + 1e-5f);
    const float rs = r1 * __builtin_amdgcn_rsqf(fmaf(var * r1, r1, 1e-5f));
    v4u n0, n1, h0, h1;
#pragma unroll
    for (int k = 0; k < 4; ++k) {
      const float ma = rdl(mean, 2 * k), ra = rdl(rs, 2 * k), mb = rdl(mean, 2 * k + 1), rb = rdl(rs, 2 * k + 1);
      n0[k] = pkbf((v0[2 * k] - ma) * ra, (v0[2 * k + 1] - mb) * rb);
      n1[k] = pkbf((v1[2 * k] - ma) * ra, (v1[2 * k + 1] - mb) * rb);
    }
    __builtin_amdgcn_raw_buffer_store_b128(n0, rws, ohn, WS_HN, SC1);
    __builtin_amdgcn_raw_buffer_store_b128(n1, rws, ohn + 1024, WS_HN, SC1);
    sched0();
#pragma unroll
    for (int k = 0; k < 4; ++k) {
      const float ba = bflo(b2v[k]), bb = bfhi(b2v[k]);
      h0[k] = pkbf(v0[2 * k] + ba, v0[2 * k + 1] + bb);
      h1[k] = pkbf(v1[2 * k] + ba, v1[2 * k + 1] + bb);
    }
    __builtin_amdgcn_raw_buffer_store_b128(h0, rws, ohn, WS_HR, SC1);
    __builtin_amdgcn_raw_buffer_store_b128(h1, rws, ohn + 1024, WS_HR, SC1);
  }
  sched0();
  STAMP(10);
#pragma unroll
  for (int i = 0; i < 8; ++i) GET1(24 + i, i);
#pragma unroll
  for (int i = 0; i < 4; ++i) GET2(i, 8 + i);
  asm volatile("s_waitcnt lgkmcnt(0)" ::: "memory");
  sched0();
#pragma unroll
  for (int i = 0; i < 24; ++i) DMA2(i, 8 + i);
  v2u b1v[4];
  {
    const rsrc_t rb1 = mk(b1, g1 ? FF * 2 : 0u);
#pragma unroll
    for (int k = 0; k < 4; ++k) b1v[k] = __builtin_amdgcn_raw_buffer_load_b64(rb1, (lane >> 5) * 8 + k * 16, ft * 128 + (w >> 1) * 64, 0);
  }
  sched0();
  STAMP(11);

  {
    f32x16 acc[2][2];
#pragma unroll
    for (int a = 0; a < 2; ++a)
#pragma unroll
      for (int b = 0; b < 2; ++b)
#pragma unroll
        for (int r = 0; r < 16; ++r) acc[a][b][r] = 0.f;
    const unsigned va0 = lane * 16 + (w & 1) * 1024, va1 = va0 ^ 1024;
    const unsigned sa = WS_HN + (dt1 * 128 + w * 32) * 2048;
#define LDA1(i) __builtin_amdgcn_raw_buffer_load_b128(rws1, (((i) & 1) ? va1 : va0) + (((i) >> 1) & 1) * 2048, sa + ((i) >> 2) * 4096, SC1)
    v4u q[16];
    for (;;) {
      const unsigned pr = __builtin_amdgcn_raw_buffer_load_b32(rws1, (lane >> 1) * 2048 + 1024 + (lane & 1) * 512 + 508, sa, SC1);
      sched0();
#pragma unroll
      for (int i = 0; i < 16; ++i) { q[i] = LDA1(i); sched0(); }
      if (!(anybad(pr) & (++spins <= SPIN_CAP))) break;
      asm volatile("s_sleep 1" ::: "memory");
    }
    STAMP(12);
#pragma unroll
    for (int bt = 0; bt < 16; ++bt) {
      const int s0 = (bt & 3) * 4;
      if (__builtin_expect(anybad(max16(q[s0], q[s0 + 1], q[s0 + 2], q[s0 + 3])), 0)) {
        bool again;
        do {
          asm volatile("s_sleep 1" ::: "memory");
#pragma unroll
          for (int i = 0; i < 4; ++i) q[s0 + i] = LDA1(bt * 4 + i);
          again = anybad(max16(q[s0], q[s0 + 1], q[s0 + 2], q[s0 + 3])) & (++spins <= SPIN_CAP);
        } while (again);
      }
#pragma unroll
      for (int i = 0; i < 4; ++i) {
        const int ks = bt * 2 + (i >> 1);
        const bf16x8 a = __builtin_bit_cast(bf16x8, q[s0 + i]);
        acc[i & 1][0] = __builtin_amdgcn_mfma_f32_32x32x16_bf16(W1[ks][0], a, acc[i & 1][0], 0, 0, 0);
        acc[i & 1][1] = __builtin_amdgcn_mfma_f32_32x32x16_bf16(W1[ks][1], a, acc[i & 1][1], 0, 0, 0);
        if (bt < 12) q[s0 + i] = LDA1(bt * 4 + 16 + i);
        sched0();
      }
    }
    STAMP(13);
    LAS char* ex = work + lane * 16;
#pragma unroll
    for (int lam = 1; lam < 4; ++lam)
#pragma unroll
      for (int k = 0; k < 4; ++k) {
        const f32x16& s = acc[lam & 1][lam >> 1];
        *(LAS f32x4*)(ex + (((w ^ lam) * 3 + lam - 1) * 4 + k) * 1024) = (f32x4){s[4 * k], s[4 * k + 1], s[4 * k + 2], s[4 * k + 3]};
      }
    bar();
    float gv[16];
#pragma unroll
    for (int k = 0; k < 4; ++k) {
      const f32x4 a = *(LAS f32x4*)(ex + ((w * 3 + 0) * 4 + k) * 1024), b = *(LAS f32x4*)(ex + ((w * 3 + 1) * 4 + k) * 1024),
                  c = *(LAS f32x4*)(ex + ((w * 3 + 2) * 4 + k) * 1024);
#pragma unroll
      for (int j = 0; j < 4; ++j) {
        const float bias = (j & 1) ? bfhi(b1v[k][j >> 1]) : bflo(b1v[k][j >> 1]);
        const float u = ((acc[0][0][4 * k + j] + a[j]) + (b[j] + c[j])) + bias;
        const float z = u * fmaf(u * u, -0.10294324f, -2.30220820f);
        gv[4 * k + j] = u * __builtin_amdgcn_rcpf(1.f + __builtin_amdgcn_exp2f(z));
      }
    }
    __builtin_amdgcn_raw_buffer_store_b128((v4u){pkbf(gv[0], gv[1]), pkbf(gv[2], gv[3]), pkbf(gv[4], gv[5]), pkbf(gv[6], gv[7])}, rws1, og, 0, SC1);
    __builtin_amdgcn_raw_buffer_store_b128((v4u){pkbf(gv[8], gv[9]), pkbf(gv[10], gv[11]), pkbf(gv[12], gv[13]), pkbf(gv[14], gv[15])}, rws1, og + 2048, 0, SC1);
  }
  sched0();
  STAMP(14);
#pragma unroll
  for (int i = 0; i < 12; ++i) GET2(4 + i, i);
  asm volatile("s_waitcnt lgkmcnt(0)" ::: "memory");
  sched0();
#pragma unroll
  for (int i = 0; i < 24; ++i) DMA2(i, 32 + i);
  sched0();
  STAMP(15);

  {
    f32x16 acc[2][2];
    const unsigned va = (lane ^ ((w & 1) << 4)) * 16;
    {
      const unsigned sh = WS_HR + (dt * 128 + 4 * st + w) * 2048;
      v4u r0, r1;
      for (;;) {
        r0 = __builtin_amdgcn_raw_buffer_load_b128(rws, va, sh, SC1);
        r1 = __builtin_amdgcn_raw_buffer_load_b128(rws, va + 1024, sh, SC1);
        if (__all(max(max4(r0), max4(r1)) != ~0u) || ++spins > SPIN_CAP) break;
        asm volatile("s_sleep 1" ::: "memory");
      }
      const int jj = (lane & 31) - 16 * (w & 1) - 8 * (lane >> 5);
      v4u id;
#pragma unroll
      for (int k = 0; k < 4; ++k) id[k] = jj == 2 * k ? 0x3f80u : (jj == 2 * k + 1 ? 0x3f800000u : 0u);
      f32x16 z;
#pragma unroll
      for (int r = 0; r < 16; ++r) z[r] = 0.f;
      acc[0][0] = __builtin_amdgcn_mfma_f32_32x32x16_bf16(__builtin_bit_cast(bf16x8, r0), __builtin_bit_cast(bf16x8, id), z, 0, 0, 0);
      acc[1][0] = __builtin_amdgcn_mfma_f32_32x32x16_bf16(__builtin_bit_cast(bf16x8, r1), __builtin_bit_cast(bf16x8, id), z, 0, 0, 0);
      acc[0][1] = z; acc[1][1] = z;
    }
    const unsigned sa = WS_G + (dt * 112 + w * 28) * 2048;
#define LDA2(i) __builtin_amdgcn_raw_buffer_load_b128(rws, va + ((i) & 3) * 1024, sa + ((i) >> 2) * 4096, SC1)
    v4u q[16];
    for (;;) {
      const unsigned pr = __builtin_amdgcn_raw_buffer_load_b32(rws, (lane < 56 ? lane : 55) * 1024 + 1020, sa, SC1);
      sched0();
#pragma unroll
      for (int i = 0; i < 16; ++i) { q[i] = LDA2(i); sched0(); }
      if (!(anybad(pr) & (++spins <= SPIN_CAP))) break;
      asm volatile("s_sleep 1" ::: "memory");
    }
    STAMP(16);
#pragma unroll
    for (int bt = 0; bt < 14; ++bt) {
      const int s0 = (bt & 3) * 4;
      bf16x8 wa[2][2];
#pragma unroll
      for (int k = 0; k < 2; ++k) {
        const int ks = bt * 2 + k;
        if (ks < 16) { wa[k][0] = W2[ks][0]; wa[k][1] = W2[ks][1]; }
        else { wa[k][0] = FR2(0, ks - 16); wa[k][1] = FR2(1, ks - 16); }
      }
      if (__builtin_expect(anybad(max16(q[s0], q[s0 + 1], q[s0 + 2], q[s0 + 3])), 0)) {
        bool again;
        do {
          asm volatile("s_sleep 1" ::: "memory");
#pragma unroll
          for (int i = 0; i < 4; ++i) q[s0 + i] = LDA2(bt * 4 + i);
          again = anybad(max16(q[s0], q[s0 + 1], q[s0 + 2], q[s0 + 3])) & (++spins <= SPIN_CAP);
        } while (again);
      }
#pragma unroll
      for (int i = 0; i < 4; ++i) {
        const bf16x8 a = __builtin_bit_cast(bf16x8, q[s0 + i]);
        acc[i & 1][0] = __builtin_amdgcn_mfma_f32_32x32x16_bf16(a, wa[i >> 1][0], acc[i & 1][0], 0, 0, 0);
        acc[i & 1][1] = __builtin_amdgcn_mfma_f32_32x32x16_bf16(a, wa[i >> 1][1], acc[i & 1][1], 0, 0, 0);
        if (bt < 10) q[s0 + i] = LDA2(bt * 4 + 16 + i);
        sched0();
      }
    }
    STAMP(17);
    bar();
    LAS char* ex = work + lane * 16;
#pragma unroll
    for (int lam = 1; lam < 4; ++lam)
#pragma unroll
      for (int k = 0; k < 4; ++k) {
        const f32x16& s = acc[k >> 1][lam >> 1];
        const int r = 8 * (lam & 1) + 4 * (k & 1);
        *(LAS f32x4*)(ex + (((w ^ lam) * 3 + lam - 1) * 4 + k) * 1024) = (f32x4){s[r], s[r + 1], s[r + 2], s[r + 3]};
      }
    bar();
    f32x4 o[4];
#pragma unroll
    for (int k = 0; k < 4; ++k) {
      const f32x4 a = *(LAS f32x4*)(ex + ((w * 3 + 0) * 4 + k) * 1024), b = *(LAS f32x4*)(ex + ((w * 3 + 1) * 4 + k) * 1024),
                  c = *(LAS f32x4*)(ex + ((w * 3 + 2) * 4 + k) * 1024);
      const f32x16& s = acc[k >> 1][0];
      const int r = 4 * (k & 1);
      o[k] = ((f32x4){s[r], s[r + 1], s[r + 2], s[r + 3]} + a) + (b + c);
    }
    const rsrc_t ro = mk(out, SEQ * DM * 2);
    const unsigned vo = (lane & 31) * 1024 + (lane >> 5) * 16, so = (64 * st + 32 * (w >> 1)) * 1024 + dt * 128 + (w & 1) * 64;
#pragma unroll
    for (int k = 0; k < 2; ++k)
      __builtin_amdgcn_raw_buffer_store_b128((v4u){pkbf(o[k][0], o[2 + k][0]), pkbf(o[k][1], o[2 + k][1]), pkbf(o[k][2], o[2 + k][2]), pkbf(o[k][3], o[2 + k][3])},
                                             ro, vo + k * 32, so, 0);
  }
  STAMP(18);
  { unsigned long long until = pt0 + 6000 + 8 * (pt1 - pt0); while (__builtin_amdgcn_s_memrealtime() < until) __builtin_amdgcn_s_sleep(2); }
}

extern "C" void kernel_launch(void* const* d_in, const int* in_sizes, int n_in, void* d_out, int out_size, void* d_ws, size_t ws_size,
                              hipStream_t stream) {
  s4_mixer_kernel<<<256, 256, 0, stream>>>((const u16*)d_in[0], (const u16*)d_in[1], (const u16*)d_in[2], (const u16*)d_in[3],
                                           (const u16*)d_in[4], (const u16*)d_in[5], (const u16*)d_in[6], (const u16*)d_in[7],
                                           (const u16*)d_in[8], (const u16*)d_in[10], (const u16*)d_in[11], (const u16*)d_in[12],
                                           (const u16*)d_in[13], (u16*)d_out, (char*)d_ws + (WSEND ? ws_size - (16u << 20) : 0));
}
```

```cpp
#define CODEPF 1
#define MAP8 1
#define PROBE 30
#define PRE 6
#include <hip/hip_runtime.h>
#include <hip/hip_bf16.h>

#define NTW 0
#define NTX 0
#define WSEND 0
#define STAMPS 0

typedef unsigned short u16;
typedef short s16x4 __attribute__((ext_vector_type(4)));
typedef short s16x8 __attribute__((ext_vector_type(8)));
typedef __bf16 bf16x2 __attribute__((ext_vector_type(2)));
typedef __bf16 bf16x8 __attribute__((ext_vector_type(8)));
typedef _Float16 f16x2 __attribute__((ext_vector_type(2)));
typedef _Float16 f16x8 __attribute__((ext_vector_type(8)));
typedef float f32x2 __attribute__((ext_vector_type(2)));
typedef float f32x4 __attribute__((ext_vector_type(4)));
typedef float f32x16 __attribute__((ext_vector_type(16)));
typedef unsigned v2u __attribute__((ext_vector_type(2)));
typedef unsigned v4u __attribute__((ext_vector_type(4)));
#define LAS __attribute__((address_space(3)))
#define DEVI __device__ __forceinline__
typedef __amdgpu_buffer_rsrc_t rsrc_t;

constexpr int SEQ = 2048, DM = 512, NS = 128, FF = 1792;
constexpr unsigned WS_EP = 0;
constexpr unsigned WS_U = 0x10000;
constexpr unsigned WS_Y = WS_U + 0x200000;
constexpr unsigned WS_HN = WS_Y + 0x200000;
constexpr unsigned WS_HR = WS_HN + 0x200000;
constexpr unsigned WS_G = WS_HR + 0x200000;
constexpr unsigned WS_END = WS_G + 0x200000;
constexpr unsigned WS_DBG = WS_END;
constexpr int SC1 = 16;
constexpr unsigned TAGM = 0x00070007u;
constexpr unsigned SPIN_CAP = 1u << 16;

constexpr int TROW = 544;
constexpr int TCH = 53 * TROW;
constexpr int WK_CT = 2 * TCH;
constexpr int WK_RED = WK_CT + 2 * 1536;
constexpr int KCP = 4160;
constexpr int WK_KC = 0;
constexpr int WK_UL = 8 * KCP;
constexpr int ULC = 4608;
constexpr int WK_SIZE = 65536;
static_assert(WK_RED + 512 <= WK_SIZE && WK_UL + 2 * ULC <= WK_CT, "lds map");
constexpr int STG = 24576;

DEVI rsrc_t mk(const void* p, unsigned bytes) { return __builtin_amdgcn_make_buffer_rsrc((void*)p, 0, bytes, 0x00020000); }
DEVI float bflo(unsigned v) { return __uint_as_float(v << 16); }
DEVI float bfhi(unsigned v) { return __uint_as_float(v & 0xffff0000u); }
DEVI unsigned pkbf(float a, float b) { bf16x2 r = __builtin_convertvector(((f32x2){a, b}), bf16x2); return __builtin_bit_cast(unsigned, r); }
DEVI unsigned pkh(float a, float b) { f16x2 r = __builtin_convertvector(((f32x2){a, b}), f16x2); return __builtin_bit_cast(unsigned, r); }
DEVI void sched0() { __builtin_amdgcn_sched_barrier(0); }
DEVI void bar() { asm volatile("s_waitcnt lgkmcnt(0)" ::: "memory"); __builtin_amdgcn_s_barrier(); asm volatile("" ::: "memory"); }
template <int C> DEVI float ror_add(float s) { return s + __uint_as_float(__builtin_amdgcn_update_dpp(0u, __float_as_uint(s), 0x120 + C, 0xf, 0xf, true)); }
template <int C> DEVI float ror_max(float s) { return fmaxf(s, __uint_as_float(__builtin_amdgcn_update_dpp(0u, __float_as_uint(s), 0x120 + C, 0xf, 0xf, true))); }
DEVI unsigned max4(v4u q) { return max(max(q.x, q.y), max(q.z, q.w)); }
DEVI unsigned max16(v4u a, v4u b, v4u c, v4u d) {
  unsigned m = max(max(a.x, a.y), a.z); m = max(max(m, a.w), b.x); m = max(max(m, b.y), b.z); m = max(max(m, b.w), c.x);
  unsigned n = max(max(c.y, c.z), c.w); n = max(max(n, d.x), d.y); n = max(max(n, d.z), d.w); return max(m, n);
}
DEVI bool anybad(unsigned m) { return __builtin_amdgcn_ballot_w64(m == ~0u) != 0ull; }
struct cx { f32x2 v, r; };
DEVI cx mkcx(f32x2 v) { return cx{v, (f32x2){-v.y, v.x}}; }
DEVI f32x2 cmul(f32x2 a, cx b) { f32x2 t = (f32x2){a.x, a.x} * b.v; return __builtin_elementwise_fma((f32x2){a.y, a.y}, b.r, t); }
DEVI f32x2 cexp(float a2, float br, float l) {
  float m = __builtin_amdgcn_exp2f(a2 * l), f = __builtin_amdgcn_fractf(br * l);
  return (f32x2){m * __builtin_amdgcn_cosf(f), m * __builtin_amdgcn_sinf(f)};
}
DEVI s16x8 trfrag(LAS char* p0, LAS char* p1) {
  s16x4 lo = __builtin_amdgcn_ds_read_tr16_b64_v4i16((LAS s16x4*)p0), hi = __builtin_amdgcn_ds_read_tr16_b64_v4i16((LAS s16x4*)p1);
  return __builtin_shufflevector(lo, hi, 0, 1, 2, 3, 4, 5, 6, 7);
}

DEVI void row_stats(const float (&a)[8], const float (&b)[8], LAS float* red, int w, int lane, float& mean, float& var) {
  float h[8];
#pragma unroll
  for (int e = 0; e < 8; ++e) {
    float s1 = a[e] + b[e], s2 = fmaf(a[e], a[e], b[e] * b[e]);
    auto r = __builtin_amdgcn_permlane32_swap(__float_as_uint(s1), __float_as_uint(s2), false, false);
    h[e] = __uint_as_float(r[0]) + __uint_as_float(r[1]);
  }
  f32x4 q;
#pragma unroll
  for (int e = 0; e < 4; ++e) {
    auto r = __builtin_amdgcn_permlane16_swap(__float_as_uint(h[e]), __float_as_uint(h[e + 4]), false, false);
    float s = __uint_as_float(r[0]) + __uint_as_float(r[1]);
    s = ror_add<8>(s); s = ror_add<4>(s); s = ror_add<2>(s); s = ror_add<1>(s);
    q[e] = s;
  }
  if ((lane & 15) == 0) *(LAS f32x4*)(red + w * 16 + (lane >> 4) * 4) = q;
  bar();
  const int e = lane & 7;
  float s1 = (red[e] + red[16 + e]) + (red[32 + e] + red[48 + e]);
  float s2 = (red[8 + e] + red[24 + e]) + (red[40 + e] + red[56 + e]);
  mean = s1 * (1.f / DM);
  var = fmaxf(fmaf(-mean, mean, s2 * (1.f / DM)), 0.f);
}
DEVI float rdl(float v, int l) { return __uint_as_float(__builtin_amdgcn_readlane(__float_as_uint(v), l)); }

#define STAMP(i) do { if ((i) == 0 || (i) == PROBE) { sched0(); unsigned long long tt_ = __builtin_amdgcn_s_memrealtime(); if ((i) == 0) pt0 = tt_; if ((i) == PROBE) pt1 = tt_; asm volatile("" :: "s"(tt_)); sched0(); } } while (0)

__global__ void __launch_bounds__(256, 1)
s4_mixer_kernel(const u16* __restrict__ x, const u16* __restrict__ Are, const u16* __restrict__ Aim, const u16* __restrict__ Bre,
                const u16* __restrict__ Bim, const u16* __restrict__ Cre, const u16* __restrict__ Cim, const u16* __restrict__ logdt,
                const u16* __restrict__ Dsk, const u16* __restrict__ w1, const u16* __restrict__ b1, const u16* __restrict__ w2,
                const u16* __restrict__ b2, u16* __restrict__ out, char* ws) {
  __shared__ __attribute__((aligned(16))) char stage_s[4 * STG];
  __shared__ __attribute__((aligned(16))) char work_s[WK_SIZE];
  LAS char* const work = (LAS char*)work_s;
  const int t = threadIdx.x, lane = t & 63;
  const int w = __builtin_amdgcn_readfirstlane(t >> 6);
  const int bx = blockIdx.x & 7, bj = blockIdx.x >> 3;
  const int p = bx * 32 + bj;
  const int dt = bj & 7;
  const int q1 = bx * 28 + bj;
  const int dt1 = q1 & 7, ft = q1 >> 3;
  const int st = 4 * bx + (bj >> 3);
  const bool g1 = bj < 28;
  LAS char* const stg = (LAS char*)stage_s + w * STG;
  unsigned long long pt0 = 0, pt1 = 0;
  STAMP(0);

  const rsrc_t rws = mk(ws, WS_END);
  const rsrc_t rws1 = mk(ws, g1 ? WS_END : 0u);
  const rsrc_t rw1 = mk(w1, g1 ? SEQ * FF * 2 : 0u);
  const rsrc_t rw2 = mk(w2, FF * SEQ * 2);
  unsigned spins = 0;
  {
    unsigned z = 0;
    if (PRE == 1 || PRE == 2) {
      const rsrc_t ra = mk(w1, SEQ * FF * 2), rb = mk(w2, SEQ * FF * 2);
      unsigned o = t < 224 ? (blockIdx.x * 224 + t) * 128 : 0x7fffffffu;
      z = __builtin_amdgcn_raw_buffer_load_b32(ra, o, 0, PRE == 2 ? 2 : 0) ^ __builtin_amdgcn_raw_buffer_load_b32(rb, o, 0, PRE == 2 ? 2 : 0);
    }
    if (PRE == 3 || PRE == 4) {
      const rsrc_t rx = mk(x, SEQ * DM * 2);
#pragma unroll
      for (int e = 0; e < 8; ++e) z ^= __builtin_amdgcn_raw_buffer_load_b32(rx, t * 4 + (e & 3) * 1024, p * 8192 + (e >> 2) * 4096, PRE == 4 ? 2 : 0);
    }
    if (PRE == 5) {
      const v4u PO = {~0u, ~0u, ~0u, ~0u};
      const unsigned o = WS_Y + (t >> 3) * 32768 + p * 128 + (t & 7) * 16;
      __builtin_amdgcn_raw_buffer_store_b128(PO, rws, o, 0, SC1);
      __builtin_amdgcn_raw_buffer_store_b128(PO, rws, o, 32 * 32768, SC1);
    }
    if (PRE == 6) {
      const rsrc_t rx = mk(x, SEQ * DM * 2);
      z ^= __builtin_amdgcn_raw_buffer_load_b32(rx, 0, p * 8192, 0);
    }
    asm volatile("s_waitcnt vmcnt(0)" :: "v"(z) : "memory");
    STAMP(30);
  }

  const int cw = w >> 1, hw = w & 1;
  const int lg = p >> 2, i0 = 2 * (p & 3);
  const int dch = 16 * (lg >> 1) + 2 * (i0 + cw) + (lg & 1);
  unsigned xr[8];
  {
    const rsrc_t rx = mk(x, SEQ * DM * 2);
#pragma unroll
    for (int e = 0; e < 8; ++e) xr[e] = __builtin_amdgcn_raw_buffer_load_b32(rx, t * 4 + (e & 3) * 1024, p * 8192 + (e >> 2) * 4096, NTX);
  }
  const v4u ep = __builtin_amdgcn_raw_buffer_load_b128(rws, WS_EP + (t & 1) * 16, 0, SC1);
  const unsigned dsk = ((const unsigned*)Dsk)[t];
  const unsigned pAr = ((const unsigned*)Are)[dch * 64 + lane], pAi = ((const unsigned*)Aim)[dch * 64 + lane];
  const unsigned pBr = ((const unsigned*)Bre)[dch * 64 + lane], pBi = ((const unsigned*)Bim)[dch * 64 + lane];
  const unsigned pCr = ((const unsigned*)Cre)[dch * 64 + lane], pCi = ((const unsigned*)Cim)[dch * 64 + lane];
  const unsigned ldt2 = ((const unsigned*)logdt)[dch >> 1];
  const v4u b2v = ((const v4u*)b2)[p];
  unsigned cpf;
  {
    unsigned long long pc; asm volatile("s_getpc_b64 %0" : "=s"(pc));
    const rsrc_t rc = mk((const void*)(pc & ~127ull), 24576);
    cpf = __builtin_amdgcn_raw_buffer_load_b32(rc, t * 128, 0, 0);
  }
  sched0();
  const v4u POI = {~0u, ~0u, ~0u, ~0u};
  {
    const unsigned o = WS_Y + (t >> 3) * 32768 + p * 128 + (t & 7) * 16;
    __builtin_amdgcn_raw_buffer_store_b128(POI, rws, o, 0, SC1);
    __builtin_amdgcn_raw_buffer_store_b128(POI, rws, o, 32 * 32768, SC1);
  }
  sched0();
  const unsigned dch16 = ((lane & 7) ^ (((lane >> 4) & 1) << 2)) * 16;
  const unsigned vw1 = (lane >> 3) * (FF * 2) + dch16, vw2 = (lane >> 3) * (SEQ * 2) + dch16;
  const unsigned sw1 = w * 512 * (FF * 2) + ft * 128, sw2 = w * 448 * (SEQ * 2) + st * 128;
#define DMA1(slot, hs) __builtin_amdgcn_raw_ptr_buffer_load_lds(rw1, (LAS void*)(stg + (slot) * 1024), 16, vw1, sw1 + (hs) * 8 * (FF * 2), 0, NTW)
#define DMA2(slot, hs) __builtin_amdgcn_raw_ptr_buffer_load_lds(rw2, (LAS void*)(stg + (slot) * 1024), 16, vw2, sw2 + (hs) * 8 * (SEQ * 2), 0, NTW)
#pragma unroll
  for (int i = 0; i < 24; ++i) DMA1(i, i);
  sched0();
  STAMP(1);

  float v0[8], v1[8];
  v4u tag;
  {
    float a[8], b[8];
#pragma unroll
    for (int e = 0; e < 8; ++e) { a[e] = bflo(xr[e]); b[e] = bfhi(xr[e]); }
    float mean, var;
    asm volatile("s_waitcnt vmcnt(24)" ::: "memory");
    asm volatile("" :: "v"(cpf));
    row_stats(a, b, (LAS float*)(work + WK_RED), w, lane, mean, var);
    const float rs = __builtin_amdgcn_rsqf(var + 1e-5f);
    const float d0 = bflo(dsk), d1 = bfhi(dsk);
    float u0[8], u1[8];
#pragma unroll
    for (int e = 0; e < 8; ++e) {
      const float m = rdl(mean, e), r = rdl(rs, e);
      u0[e] = (a[e] - m) * r; u1[e] = (b[e] - m) * r;
      v0[e] = fmaf(u0[e], d0, a[e]); v1[e] = fmaf(u1[e], d1, b[e]);
    }
    tag = (ep + 0x00010001u) & TAGM;
    v4u c0, c1;
#pragma unroll
    for (int k = 0; k < 4; ++k) {
      c0[k] = ((__builtin_bit_cast(unsigned, __builtin_amdgcn_cvt_pkrtz(u0[2 * k], u0[2 * k + 1])) + 0x00040004u) & ~TAGM) | tag[k];
      c1[k] = ((__builtin_bit_cast(unsigned, __builtin_amdgcn_cvt_pkrtz(u1[2 * k], u1[2 * k + 1])) + 0x00040004u) & ~TAGM) | tag[k];
    }
    const unsigned o = WS_U + (t >> 3) * 65536 + p * 128 + (t & 7) * 16;
    __builtin_amdgcn_raw_buffer_store_b128(c0, rws, o, 0, SC1);
    __builtin_amdgcn_raw_buffer_store_b128(c1, rws, o, 32768, SC1);
  }
  sched0();
  const unsigned ohn = (((t >> 5) * 128 + (p >> 1)) * 2) * 1024 + (p & 1) * 512 + (t & 31) * 16;
  const unsigned og = WS_G + ((dt1 * 112 + 4 * ft + 2 * (w >> 1)) * 2 + (w & 1)) * 1024 + lane * 16;
  __builtin_amdgcn_raw_buffer_store_b128(POI, rws, ohn, WS_HN, SC1);
  __builtin_amdgcn_raw_buffer_store_b128(POI, rws, ohn + 1024, WS_HN, SC1);
  __builtin_amdgcn_raw_buffer_store_b128(POI, rws, ohn, WS_HR, SC1);
  __builtin_amdgcn_raw_buffer_store_b128(POI, rws, ohn + 1024, WS_HR, SC1);
  __builtin_amdgcn_raw_buffer_store_b128(POI, rws1, og, 0, SC1);
  __builtin_amdgcn_raw_buffer_store_b128(POI, rws1, og + 2048, 0, SC1);
  sched0();
  STAMP(2);

  float kinv;
  {
    const float ld = (dch & 1) ? bfhi(ldt2) : bflo(ldt2);
    const float dtv = bflo(pkbf(__builtin_amdgcn_exp2f(ld * 1.44269504088896341f), 0.f));
    float wr[2], wi[2];
#pragma unroll
    for (int k = 0; k < 2; ++k) {
      const float br = dtv * (k ? bfhi(pBr) : bflo(pBr)), bi = dtv * (k ? bfhi(pBi) : bflo(pBi));
      const float cr = k ? bfhi(pCr) : bflo(pCr), ci = k ? bfhi(pCi) : bflo(pCi);
      wr[k] = cr * br - ci * bi; wi[k] = cr * bi + ci * br;
    }
    float mx = fmaxf(fmaxf(fabsf(wr[0]), fabsf(wi[0])), fmaxf(fabsf(wr[1]), fabsf(wi[1])));
    mx = ror_max<8>(mx); mx = ror_max<4>(mx); mx = ror_max<2>(mx); mx = ror_max<1>(mx);
    mx = fmaxf(fmaxf(rdl(mx, 0), rdl(mx, 16)), fmaxf(rdl(mx, 32), rdl(mx, 48)));
    int ex = (int)(__float_as_uint(mx) >> 23);
    ex = ex < 16 ? 16 : (ex > 240 ? 240 : ex);
    const float sc = __uint_as_float((unsigned)(261 - ex) << 23);
    kinv = __uint_as_float((unsigned)(ex - 7) << 23);
    const float a2 = dtv * (hw ? bfhi(pAr) : bflo(pAr)) * 1.44269504088896341f;
    const float bq = dtv * (hw ? bfhi(pAi) : bflo(pAi)) * 0.15915494309189535f;
    const f32x2 W0 = {(hw ? wr[1] : wr[0]) * sc, -(hw ? wi[1] : wi[0]) * sc};
    const cx e1 = mkcx(cexp(a2, -bq, 1.f)), e256 = mkcx(cexp(a2, -bq, 256.f)), e8 = mkcx(cexp(a2, bq, 8.f));
    f32x2 la = {1.f, 0.f}, lb = cexp(a2, bq, 128.f);
    LAS unsigned* tb = (LAS unsigned*)(work + cw * TCH + hw * 256 + lane * 4);
#define TPUT(row, z) tb[(row) * (TROW / 4)] = pkh((z).x, (z).y)
#pragma unroll
    for (int i = 0; i < 16; ++i) {
      TPUT(i, la); TPUT(16 + i, lb);
      if (i < 15) { la = cmul(la, e8); lb = cmul(lb, e8); }
    }
    f32x2 h[8]; h[0] = W0;
#pragma unroll
    for (int i = 1; i < 8; ++i) h[i] = cmul(h[i - 1], e1);
#pragma unroll
    for (int i = 0; i < 8; ++i) TPUT(32 + i, h[i]);
    f32x2 g = cmul(h[0], e256), g4 = cmul(h[4], e256);
    TPUT(40, g); TPUT(41, cmul(h[2], e256)); TPUT(42, g4); TPUT(43, cmul(h[6], e256));
    g = cmul(g, e256); g4 = cmul(g4, e256); TPUT(44, g); TPUT(45, g4);
    g = cmul(g, e256); g4 = cmul(g4, e256); TPUT(46, g); TPUT(47, g4);
#pragma unroll
    for (int i = 0; i < 5; ++i) { g = cmul(g, e256); TPUT(48 + i, g); }
    bar();
    const int r = lane & 15, g16 = lane >> 4;
    LAS char* pa0 = work + cw * TCH + (32 + r) * TROW + g16 * 16;
    LAS char* pa1 = work + cw * TCH + ((r & 3) ? 52 : 48 + (r >> 2)) * TROW + g16 * 16;
    LAS char* pb = work + cw * TCH + (16 * hw + r) * TROW + g16 * 16;
    f32x4 k0 = {0.f, 0.f, 0.f, 0.f}, k1 = {0.f, 0.f, 0.f, 0.f};
#pragma unroll
    for (int ks = 0; ks < 8; ++ks) {
      const f16x8 fb = *(LAS f16x8*)(pb + ks * 64);
      k0 = __builtin_amdgcn_mfma_f32_16x16x32_f16(*(LAS f16x8*)(pa0 + ks * 64), fb, k0, 0, 0, 0);
      k1 = __builtin_amdgcn_mfma_f32_16x16x32_f16(*(LAS f16x8*)(pa1 + ks * 64), fb, k1, 0, 0, 0);
    }
    const int bb = 16 * hw + r;
    LAS u16* ct = (LAS u16*)(work + WK_CT + cw * 1536);
    const int ia = g16 < 2 ? 8 * bb + 4 * g16 : (g16 == 2 ? 256 + 4 * bb : 384 + 2 * bb);
    const int ib = g16 < 3 ? ia + 2 : 448 + 2 * bb;
    *(LAS unsigned*)(ct + ia) = pkh(k0[0], k0[1]);
    *(LAS unsigned*)(ct + ib) = pkh(k0[2], k0[3]);
    const unsigned k1p = pkh(k1[0], k1[1]);
    ct[512 + 32 * g16 + bb] = (u16)k1p;
    ct[640 + 32 * g16 + bb] = (u16)(k1p >> 16);
    bar();
    const int tau = t & 127;
    unsigned E[10];
#pragma unroll
    for (int k = 0; k < 10; ++k) {
      const int l0 = 16 * tau - 4 + 2 * k;
      const unsigned bits = pkbf((float)l0, (float)(l0 + 1));
      const int j0 = l0 < 128 ? (l0 < 0 ? 0 : l0) : (int)(bits & 0xffffu) - 0x4300 + 128;
      const int j1 = l0 < 128 ? (l0 < 0 ? 0 : l0 + 1) : (int)(bits >> 16) - 0x4300 + 128;
      const unsigned val = (unsigned)ct[j0] | ((unsigned)ct[j1] << 16);
      E[k] = l0 < 0 ? 0u : val;
    }
    unsigned O[9];
#pragma unroll
    for (int k = 0; k < 9; ++k) O[k] = __builtin_amdgcn_alignbit(E[k + 1], E[k], 16);
    LAS char* kc = work + WK_KC + cw * 4 * KCP + 32 + tau * 32;
    *(LAS v4u*)(kc) = (v4u){E[2], E[3], E[4], E[5]};             *(LAS v4u*)(kc + 16) = (v4u){E[6], E[7], E[8], E[9]};
    *(LAS v4u*)(kc + KCP) = (v4u){O[1], O[2], O[3], O[4]};       *(LAS v4u*)(kc + KCP + 16) = (v4u){O[5], O[6], O[7], O[8]};
    *(LAS v4u*)(kc + 2 * KCP) = (v4u){E[1], E[2], E[3], E[4]};   *(LAS v4u*)(kc + 2 * KCP + 16) = (v4u){E[5], E[6], E[7], E[8]};
    *(LAS v4u*)(kc + 3 * KCP) = (v4u){O[0], O[1], O[2], O[3]};   *(LAS v4u*)(kc + 3 * KCP + 16) = (v4u){O[4], O[5], O[6], O[7]};
    const v4u Z = {0u, 0u, 0u, 0u};
    if (t < 16) *(LAS v4u*)(work + WK_KC + (t >> 1) * KCP + (t & 1) * 16) = Z;
    if (t >= 64 && t < 128) *(LAS v4u*)(work + WK_UL + ((t >> 5) & 1) * ULC + (t & 31) * 16) = Z;
    bar();
  }
  STAMP(3);
  f16x8 TA[4][8];
  {
    LAS char* pa = work + WK_KC + cw * 4 * KCP + ((lane & 15) >> 2) * KCP + (28 - 8 * (lane >> 4) + 4 * (lane & 3)) * 2 + hw * 256;
#pragma unroll
    for (int i = 0; i < 4; ++i)
#pragma unroll
      for (int mu = 0; mu < 8; ++mu) TA[i][mu] = __builtin_bit_cast(f16x8, trfrag(pa + (8 * mu + i) * 64 + 8, pa + (8 * mu + i) * 64));
  }
  const int trq = (lane & 15) >> 2;
  const int trc = (((2 * ((lane >> 4) & 1) + ((lane & 3) >> 1)) ^ (((trq >> 1) & 1) << 2) ^ ((w >> 1) << 2)) * 16) + 8 * (lane & 1);
  LAS char* const t1a = stg + (8 * (lane >> 5) + trq) * 128 + trc;
  LAS char* const t1b = stg + (8 * (lane >> 5) + trq) * 128 + (trc ^ 64);
  LAS char* const t2a = stg + (4 * (lane >> 5) + trq) * 128 + trc;
  LAS char* const t2b = stg + (4 * (lane >> 5) + trq) * 128 + (trc ^ 64);
  bf16x8 W1[32][2], W2[16][2];
#define GET1(ks, slot) do { W1[ks][0] = __builtin_bit_cast(bf16x8, trfrag(t1a + (slot) * 2048, t1a + (slot) * 2048 + 512)); \
                            W1[ks][1] = __builtin_bit_cast(bf16x8, trfrag(t1b + (slot) * 2048, t1b + (slot) * 2048 + 512)); } while (0)
#define FR2(sub, slot) __builtin_bit_cast(bf16x8, trfrag(((sub) ? t2b : t2a) + (slot) * 2048, ((sub) ? t2b : t2a) + (slot) * 2048 + 1024))
#define GET2(ks, slot) do { W2[ks][0] = FR2(0, slot); W2[ks][1] = FR2(1, slot); } while (0)
#pragma unroll
  for (int i = 0; i < 12; ++i) GET1(i, i);
  sched0();
  STAMP(4);

  {
    const unsigned o = WS_U + lg * 32768 + (t >> 1) * 128 + (i0 + (t & 1)) * 16;
    v4u q0, q1;
    for (;;) {
      q0 = __builtin_amdgcn_raw_buffer_load_b128(rws, o, 0, SC1) ^ tag;
      q1 = __builtin_amdgcn_raw_buffer_load_b128(rws, o, 16384, SC1) ^ tag;
      const v4u z = q0 | q1;
      if (__all((((z.x | z.y) | (z.z | z.w)) & TAGM) == 0u) || ++spins > SPIN_CAP) break;
      asm volatile("s_sleep 1" ::: "memory");
    }
    LAS char* ul = work + WK_UL + (t & 1) * ULC + 512 + (t >> 1) * 16;
    *(LAS v4u*)ul = q0; *(LAS v4u*)(ul + 2048) = q1;
  }
  bar();
  if (blockIdx.x == 0 && t < 2) __builtin_amdgcn_raw_buffer_store_b128(tag, rws, WS_EP + t * 16, 0, SC1);
  STAMP(5);

  {
    f32x4 acc[8];
#pragma unroll
    for (int c = 0; c < 8; ++c) acc[c] = (f32x4){0.f, 0.f, 0.f, 0.f};
    LAS char* pb = work + WK_UL + cw * ULC + (16 * (lane & 15) + 8 * (lane >> 4) - 128 * hw + 144) * 2;
    f16x8 UB[2][8];
#pragma unroll
    for (int g = 0; g < 8; ++g) UB[0][g] = *(LAS f16x8*)(pb + (256 * g + 96) * 2);
#pragma unroll
    for (int i = 0; i < 4; ++i) {
      int n = 0;
#pragma unroll
      for (int g = 0; g < 8; ++g) {
#pragma unroll
        for (int mu = 0; mu + g < 8; ++mu) {
          acc[g + mu] = __builtin_amdgcn_mfma_f32_16x16x32_f16(TA[i][mu], UB[i & 1][g], acc[g + mu], 0, 0, 0);
          if (n % 6 == 2) { DMA1(i * 6 + n / 6, 24 + i * 6 + n / 6); sched0(); }
          if (i < 3 && n % 4 == 0 && n < 32) { UB[(i + 1) & 1][n / 4] = *(LAS f16x8*)(pb + (256 * (n / 4) + 96 - 32 * (i + 1)) * 2); sched0(); }
          ++n;
        }
      }
    }
    sched0();
    STAMP(6);
    LAS char* ex = work + lane * 16;
    f32x4 keep[4];
    if (hw) {
#pragma unroll
      for (int k = 0; k < 4; ++k) { *(LAS f32x4*)(ex + (w * 4 + k) * 1024) = acc[k]; keep[k] = acc[4 + k] * kinv; }
      asm volatile("" ::: "memory");
    } else {
#pragma unroll
      for (int k = 0; k < 4; ++k) { *(LAS f32x4*)(ex + (w * 4 + k) * 1024) = acc[4 + k]; keep[k] = acc[k] * kinv; }
    }
    bar();
    unsigned pk[4][2];
#pragma unroll
    for (int k = 0; k < 4; ++k) {
      const f32x4 y = *(LAS f32x4*)(ex + ((w ^ 1) * 4 + k) * 1024) * kinv + keep[k];
      pk[k][0] = pkbf(y[0], y[1]); pk[k][1] = pkbf(y[2], y[3]);
    }
    const int col = lane & 15, rho = lane >> 4;
    const unsigned oy = WS_Y + (8 * (4 * hw + (rho & 1)) + 2 * (col >> 2) + (rho >> 1)) * 32768 + p * 128 + cw * 64 + (col & 3) * 16;
#pragma unroll
    for (int k = 0; k < 4; k += 2) {
      auto r0 = __builtin_amdgcn_permlane16_swap(pk[k][0], pk[k + 1][0], false, false);
      auto r1 = __builtin_amdgcn_permlane16_swap(pk[k][1], pk[k + 1][1], false, false);
      __builtin_amdgcn_raw_buffer_store_b128((v4u){r0[0], r1[0], r0[1], r1[1]}, rws, oy, k * 8 * 32768, SC1);
    }
  }
  sched0();
  STAMP(7);
#pragma unroll
  for (int i = 0; i < 12; ++i) GET1(12 + i, i);
  asm volatile("s_waitcnt lgkmcnt(0)" ::: "memory");
  sched0();
#pragma unroll
  for (int i = 0; i < 16; ++i) DMA1(i, 48 + i);
#pragma unroll
  for (int i = 0; i < 8; ++i) DMA2(16 + i, i);
  sched0();
  STAMP(8);

  {
    const unsigned o = WS_Y + ((p >> 3) * 2 + (p & 1)) * 32768 + ((t >> 3) * 8 + ((t & 7) >> 1)) * 128 + (t & 1) * 64 + ((p >> 1) & 3) * 16;
    v4u q0, q1;
    for (;;) {
      q0 = __builtin_amdgcn_raw_buffer_load_b128(rws, o, 0, SC1);
      q1 = __builtin_amdgcn_raw_buffer_load_b128(rws, o + 512, 0, SC1);
      if (__all(max(max4(q0), max4(q1)) != ~0u) || ++spins > SPIN_CAP) break;
      asm volatile("s_sleep 1" ::: "memory");
    }
    STAMP(9);
#pragma unroll
    for (int k = 0; k < 4; ++k) {
      v0[2 * k] += bflo(q0[k]); v0[2 * k + 1] += bfhi(q0[k]);
      v1[2 * k] += bflo(q1[k]); v1[2 * k + 1] += bfhi(q1[k]);
    }
    float mean, var;
    row_stats(v0, v1, (LAS float*)(work + WK_RED + 256), w, lane, mean, var);
    const float r1 = __builtin_amdgcn_rsqf(var + 1e-5f);
    const float rs = r1 * __builtin_amdgcn_rsqf(fmaf(var * r1, r1, 1e-5f));
    v4u n0, n1, h0, h1;
#pragma unroll
    for (int k = 0; k < 4; ++k) {
      const float ma = rdl(mean, 2 * k), ra = rdl(rs, 2 * k), mb = rdl(mean, 2 * k + 1), rb = rdl(rs, 2 * k + 1);
      n0[k] = pkbf((v0[2 * k] - ma) * ra, (v0[2 * k + 1] - mb) * rb);
      n1[k] = pkbf((v1[2 * k] - ma) * ra, (v1[2 * k + 1] - mb) * rb);
    }
    __builtin_amdgcn_raw_buffer_store_b128(n0, rws, ohn, WS_HN, SC1);
    __builtin_amdgcn_raw_buffer_store_b128(n1, rws, ohn + 1024, WS_HN, SC1);
    sched0();
#pragma unroll
    for (int k = 0; k < 4; ++k) {
      const float ba = bflo(b2v[k]), bb = bfhi(b2v[k]);
      h0[k] = pkbf(v0[2 * k] + ba, v0[2 * k + 1] + bb);
      h1[k] = pkbf(v1[2 * k] + ba, v1[2 * k + 1] + bb);
    }
    __builtin_amdgcn_raw_buffer_store_b128(h0, rws, ohn, WS_HR, SC1);
    __builtin_amdgcn_raw_buffer_store_b128(h1, rws, ohn + 1024, WS_HR, SC1);
  }
  sched0();
  STAMP(10);
#pragma unroll
  for (int i = 0; i < 8; ++i) GET1(24 + i, i);
#pragma unroll
  for (int i = 0; i < 4; ++i) GET2(i, 8 + i);
  asm volatile("s_waitcnt lgkmcnt(0)" ::: "memory");
  sched0();
#pragma unroll
  for (int i = 0; i < 24; ++i) DMA2(i, 8 + i);
  v2u b1v[4];
  {
    const rsrc_t rb1 = mk(b1, g1 ? FF * 2 : 0u);
#pragma unroll
    for (int k = 0; k < 4; ++k) b1v[k] = __builtin_amdgcn_raw_buffer_load_b64(rb1, (lane >> 5) * 8 + k * 16, ft * 128 + (w >> 1) * 64, 0);
  }
  sched0();
  STAMP(11);

  {
    f32x16 acc[2][2];
#pragma unroll
    for (int a = 0; a < 2; ++a)
#pragma unroll
      for (int b = 0; b < 2; ++b)
#pragma unroll
        for (int r = 0; r < 16; ++r) acc[a][b][r] = 0.f;
    const unsigned va0 = lane * 16 + (w & 1) * 1024, va1 = va0 ^ 1024;
    const unsigned sa = WS_HN + (dt1 * 128 + w * 32) * 2048;
#define LDA1(i) __builtin_amdgcn_raw_buffer_load_b128(rws1, (((i) & 1) ? va1 : va0) + (((i) >> 1) & 1) * 2048, sa + ((i) >> 2) * 4096, SC1)
    v4u q[16];
    for (;;) {
      const unsigned pr = __builtin_amdgcn_raw_buffer_load_b32(rws1, (lane >> 1) * 2048 + 1024 + (lane & 1) * 512 + 508, sa, SC1);
      sched0();
#pragma unroll
      for (int i = 0; i < 16; ++i) { q[i] = LDA1(i); sched0(); }
      if (!(anybad(pr) & (++spins <= SPIN_CAP))) break;
      asm volatile("s_sleep 1" ::: "memory");
    }
    STAMP(12);
#pragma unroll
    for (int bt = 0; bt < 16; ++bt) {
      const int s0 = (bt & 3) * 4;
      if (__builtin_expect(anybad(max16(q[s0], q[s0 + 1], q[s0 + 2], q[s0 + 3])), 0)) {
        bool again;
        do {
          asm volatile("s_sleep 1" ::: "memory");
#pragma unroll
          for (int i = 0; i < 4; ++i) q[s0 + i] = LDA1(bt * 4 + i);
          again = anybad(max16(q[s0], q[s0 + 1], q[s0 + 2], q[s0 + 3])) & (++spins <= SPIN_CAP);
        } while (again);
      }
#pragma unroll
      for (int i = 0; i < 4; ++i) {
        const int ks = bt * 2 + (i >> 1);
        const bf16x8 a = __builtin_bit_cast(bf16x8, q[s0 + i]);
        acc[i & 1][0] = __builtin_amdgcn_mfma_f32_32x32x16_bf16(W1[ks][0], a, acc[i & 1][0], 0, 0, 0);
        acc[i & 1][1] = __builtin_amdgcn_mfma_f32_32x32x16_bf16(W1[ks][1], a, acc[i & 1][1], 0, 0, 0);
        if (bt < 12) q[s0 + i] = LDA1(bt * 4 + 16 + i);
        sched0();
      }
    }
    STAMP(13);
    LAS char* ex = work + lane * 16;
#pragma unroll
    for (int lam = 1; lam < 4; ++lam)
#pragma unroll
      for (int k = 0; k < 4; ++k) {
        const f32x16& s = acc[lam & 1][lam >> 1];
        *(LAS f32x4*)(ex + (((w ^ lam) * 3 + lam - 1) * 4 + k) * 1024) = (f32x4){s[4 * k], s[4 * k + 1], s[4 * k + 2], s[4 * k + 3]};
      }
    bar();
    float gv[16];
#pragma unroll
    for (int k = 0; k < 4; ++k) {
      const f32x4 a = *(LAS f32x4*)(ex + ((w * 3 + 0) * 4 + k) * 1024), b = *(LAS f32x4*)(ex + ((w * 3 + 1) * 4 + k) * 1024),
                  c = *(LAS f32x4*)(ex + ((w * 3 + 2) * 4 + k) * 1024);
#pragma unroll
      for (int j = 0; j < 4; ++j) {
        const float bias = (j & 1) ? bfhi(b1v[k][j >> 1]) : bflo(b1v[k][j >> 1]);
        const float u = ((acc[0][0][4 * k + j] + a[j]) + (b[j] + c[j])) + bias;
        const float z = u * fmaf(u * u, -0.10294324f, -2.30220820f);
        gv[4 * k + j] = u * __builtin_amdgcn_rcpf(1.f + __builtin_amdgcn_exp2f(z));
      }
    }
    __builtin_amdgcn_raw_buffer_store_b128((v4u){pkbf(gv[0], gv[1]), pkbf(gv[2], gv[3]), pkbf(gv[4], gv[5]), pkbf(gv[6], gv[7])}, rws1, og, 0, SC1);
    __builtin_amdgcn_raw_buffer_store_b128((v4u){pkbf(gv[8], gv[9]), pkbf(gv[10], gv[11]), pkbf(gv[12], gv[13]), pkbf(gv[14], gv[15])}, rws1, og + 2048, 0, SC1);
  }
  sched0();
  STAMP(14);
#pragma unroll
  for (int i = 0; i < 12; ++i) GET2(4 + i, i);
  asm volatile("s_waitcnt lgkmcnt(0)" ::: "memory");
  sched0();
#pragma unroll
  for (int i = 0; i < 24; ++i) DMA2(i, 32 + i);
  sched0();
  STAMP(15);

  {
    f32x16 acc[2][2];
    const unsigned va = (lane ^ ((w & 1) << 4)) * 16;
    {
      const unsigned sh = WS_HR + (dt * 128 + 4 * st + w) * 2048;
      v4u r0, r1;
      for (;;) {
        r0 = __builtin_amdgcn_raw_buffer_load_b128(rws, va, sh, SC1);
        r1 = __builtin_amdgcn_raw_buffer_load_b128(rws, va + 1024, sh, SC1);
        if (__all(max(max4(r0), max4(r1)) != ~0u) || ++spins > SPIN_CAP) break;
        asm volatile("s_sleep 1" ::: "memory");
      }
      const int jj = (lane & 31) - 16 * (w & 1) - 8 * (lane >> 5);
      v4u id;
#pragma unroll
      for (int k = 0; k < 4; ++k) id[k] = jj == 2 * k ? 0x3f80u : (jj == 2 * k + 1 ? 0x3f800000u : 0u);
      f32x16 z;
#pragma unroll
      for (int r = 0; r < 16; ++r) z[r] = 0.f;
      acc[0][0] = __builtin_amdgcn_mfma_f32_32x32x16_bf16(__builtin_bit_cast(bf16x8, r0), __builtin_bit_cast(bf16x8, id), z, 0, 0, 0);
      acc[1][0] = __builtin_amdgcn_mfma_f32_32x32x16_bf16(__builtin_bit_cast(bf16x8, r1), __builtin_bit_cast(bf16x8, id), z, 0, 0, 0);
      acc[0][1] = z; acc[1][1] = z;
    }
    const unsigned sa = WS_G + (dt * 112 + w * 28) * 2048;
#define LDA2(i) __builtin_amdgcn_raw_buffer_load_b128(rws, va + ((i) & 3) * 1024, sa + ((i) >> 2) * 4096, SC1)
    v4u q[16];
    for (;;) {
      const unsigned pr = __builtin_amdgcn_raw_buffer_load_b32(rws, (lane < 56 ? lane : 55) * 1024 + 1020, sa, SC1);
      sched0();
#pragma unroll
      for (int i = 0; i < 16; ++i) { q[i] = LDA2(i); sched0(); }
      if (!(anybad(pr) & (++spins <= SPIN_CAP))) break;
      asm volatile("s_sleep 1" ::: "memory");
    }
    STAMP(16);
#pragma unroll
    for (int bt = 0; bt < 14; ++bt) {
      const int s0 = (bt & 3) * 4;
      bf16x8 wa[2][2];
#pragma unroll
      for (int k = 0; k < 2; ++k) {
        const int ks = bt * 2 + k;
        if (ks < 16) { wa[k][0] = W2[ks][0]; wa[k][1] = W2[ks][1]; }
        else { wa[k][0] = FR2(0, ks - 16); wa[k][1] = FR2(1, ks - 16); }
      }
      if (__builtin_expect(anybad(max16(q[s0], q[s0 + 1], q[s0 + 2], q[s0 + 3])), 0)) {
        bool again;
        do {
          asm volatile("s_sleep 1" ::: "memory");
#pragma unroll
          for (int i = 0; i < 4; ++i) q[s0 + i] = LDA2(bt * 4 + i);
          again = anybad(max16(q[s0], q[s0 + 1], q[s0 + 2], q[s0 + 3])) & (++spins <= SPIN_CAP);
        } while (again);
      }
#pragma unroll
      for (int i = 0; i < 4; ++i) {
        const bf16x8 a = __builtin_bit_cast(bf16x8, q[s0 + i]);
        acc[i & 1][0] = __builtin_amdgcn_mfma_f32_32x32x16_bf16(a, wa[i >> 1][0], acc[i & 1][0], 0, 0, 0);
        acc[i & 1][1] = __builtin_amdgcn_mfma_f32_32x32x16_bf16(a, wa[i >> 1][1], acc[i & 1][1], 0, 0, 0);
        if (bt < 10) q[s0 + i] = LDA2(bt * 4 + 16 + i);
        sched0();
      }
    }
    STAMP(17);
    bar();
    LAS char* ex = work + lane * 16;
#pragma unroll
    for (int lam = 1; lam < 4; ++lam)
#pragma unroll
      for (int k = 0; k < 4; ++k) {
        const f32x16& s = acc[k >> 1][lam >> 1];
        const int r = 8 * (lam & 1) + 4 * (k & 1);
        *(LAS f32x4*)(ex + (((w ^ lam) * 3 + lam - 1) * 4 + k) * 1024) = (f32x4){s[r], s[r + 1], s[r + 2], s[r + 3]};
      }
    bar();
    f32x4 o[4];
#pragma unroll
    for (int k = 0; k < 4; ++k) {
      const f32x4 a = *(LAS f32x4*)(ex + ((w * 3 + 0) * 4 + k) * 1024), b = *(LAS f32x4*)(ex + ((w * 3 + 1) * 4 + k) * 1024),
                  c = *(LAS f32x4*)(ex + ((w * 3 + 2) * 4 + k) * 1024);
      const f32x16& s = acc[k >> 1][0];
      const int r = 4 * (k & 1);
      o[k] = ((f32x4){s[r], s[r + 1], s[r + 2], s[r + 3]} + a) + (b + c);
    }
    const rsrc_t ro = mk(out, SEQ * DM * 2);
    const unsigned vo = (lane & 31) * 1024 + (lane >> 5) * 16, so = (64 * st + 32 * (w >> 1)) * 1024 + dt * 128 + (w & 1) * 64;
#pragma unroll
    for (int k = 0; k < 2; ++k)
      __builtin_amdgcn_raw_buffer_store_b128((v4u){pkbf(o[k][0], o[2 + k][0]), pkbf(o[k][1], o[2 + k][1]), pkbf(o[k][2], o[2 + k][2]), pkbf(o[k][3], o[2 + k][3])},
                                             ro, vo + k * 32, so, 0);
  }
  STAMP(18);
  { unsigned long long until = pt0 + 6000 + 8 * (pt1 - pt0); while (__builtin_amdgcn_s_memrealtime() < until) __builtin_amdgcn_s_sleep(2); }
}

extern "C" void kernel_launch(void* const* d_in, const int* in_sizes, int n_in, void* d_out, int out_size, void* d_ws, size_t ws_size,
                              hipStream_t stream) {
  s4_mixer_kernel<<<256, 256, 0, stream>>>((const u16*)d_in[0], (const u16*)d_in[1], (const u16*)d_in[2], (const u16*)d_in[3],
                                           (const u16*)d_in[4], (const u16*)d_in[5], (const u16*)d_in[6], (const u16*)d_in[7],
                                           (const u16*)d_in[8], (const u16*)d_in[10], (const u16*)d_in[11], (const u16*)d_in[12],
                                           (const u16*)d_in[13], (u16*)d_out, (char*)d_ws + (WSEND ? ws_size - (16u << 20) : 0));
}
```

```cpp
#include <hip/hip_runtime.h>

__global__ void __launch_bounds__(256, 1)
s4_mixer_kernel(void* ws, unsigned ws_n, unsigned ws_f, const void* w1, unsigned w1_n, unsigned w1_f, const void* w2, unsigned w2_n, unsigned w2_f,
                const void* x, const void* are, const void* aim, const void* bre, const void* bim, const void* cre, const void* cim,
                const void* logdt, const void* dskip, const void* b2, const void* b1, unsigned b1_n, unsigned b1_f, void* out, unsigned out_n,
                unsigned out_f) {
#if defined(__HIP_DEVICE_COMPILE__)
  __shared__ __attribute__((aligned(16))) char lds[163840];
  asm volatile(
  "s_load_dwordx16 s[4:19], s[0:1], 0x0\n"
  "s_load_dwordx16 s[20:35], s[0:1], 0x40\n"
  "s_load_dwordx8 s[36:43], s[0:1], 0x80\n"
  "s_getpc_b64 s[44:45]\n"
  "Lpc:\n"
  "s_mov_b32 s46, Lend-Lpc\n"
  "s_mov_b32 s47, 0x20000\n"
  "s_and_b32 s84, s2, 0xf8\n"
  "v_lshrrev_b32 v3, 5, v0\n"
  "v_add_lshl_u32 v34, v3, s84, 7\n"
  "buffer_load_dword v30, v34, s[44:47], 0 offen\n"
  "v_lshrrev_b32 v56, 6, v0\n"
  "s_and_b32 s54, s2, 7\n"
  "s_lshr_b32 s55, s2, 3\n"
  "v_readfirstlane_b32 s52, v56\n"
  "s_lshr_b32 s53, s54, 1\n"
  "s_and_b32 s84, s55, 28\n"
  "s_add_i32 s53, s53, s84\n"
  "s_and_b32 s84, s55, 3\n"
  "s_lshl2_add_u32 s53, s53, s84\n"
  "s_and_b32 s84, s54, 1\n"
  "s_lshl1_add_u32 s53, s53, s84\n"
  "v_lshlrev_b32 v57, 2, v0\n"
  "s_lshl_b32 s84, s53, 13\n"
  "s_add_i32 s84, s84, 0x1000\n"
  "v_add_u32 v58, s84, v57\n"
  "v_and_b32 v174, 3, v0\n"
  "v_lshl_add_u32 v178, v174, 11, s84\n"
  "v_mov_b32 v176, 0\n"
  "v_mov_b32 v177, 0\n"
  "s_lshr_b32 s56, s52, 1\n"
  "s_and_b32 s57, s52, 1\n"
  "s_lshl_b32 s71, s54, 5\n"
  "s_add_i32 s71, s71, s55\n"
  "s_lshr_b32 s84, s71, 3\n"
  "s_lshl_b32 s84, s84, 4\n"
  "s_and_b32 s85, s71, 3\n"
  "s_lshl2_add_u32 s84, s85, s84\n"
  "s_lshl1_add_u32 s84, s56, s84\n"
  "s_bfe_u32 s85, s71, 0x10002\n"
  "s_add_i32 s58, s84, s85\n"
  "v_and_b32 v85, 63, v0\n"
  "s_lshl_b32 s85, s58, 8\n"
  "v_lshl_add_u32 v59, v85, 2, s85\n"
  "v_and_b32 v86, 1, v0\n"
  "v_lshlrev_b32 v87, 4, v86\n"
  "s_lshl_b32 s84, s58, 1\n"
  "v_mov_b32 v81, s84\n"
  "s_lshl_b32 s69, s53, 7\n"
  "v_lshrrev_b32 v62, 3, v0\n"
  "v_and_b32 v175, 7, v0\n"
  "v_lshlrev_b32 v175, 4, v175\n"
  "s_mov_b32 s86, 65536\n"
  "v_mad_u32_u24 v84, v62, s86, v175\n"
  "s_mul_i32 s60, s54, 28\n"
  "s_add_i32 s60, s60, s55\n"
  "s_and_b32 s88, s54, 1\n"
  "s_lshl_b32 s88, s88, 5\n"
  "s_cmp_lt_u32 s55, 28\n"
  "s_cselect_b32 s88, 0, s88\n"
  "s_sub_i32 s60, s60, s88\n"
  "s_and_b32 s59, s60, 7\n"
  "s_lshr_b32 s60, s60, 3\n"
  "v_lshl_add_u32 v31, v85, 3, s59\n"
  "s_mul_i32 s64, s52, 229376\n"
  "s_lshl_b32 s88, s60, 7\n"
  "s_add_i32 s64, s64, s88\n"
  "v_and_b32 v60, 0x1c0, v31\n"
  "v_mad_u32_u24 v31, v60, 3, v31\n"
  "v_mul_u32_u24 v31, 3584, v31\n"
  "v_add_u32 v31, s64, v31\n"
  "s_mov_b32 s76, 0\n"
  "v_lshlrev_b32 v1, 4, v85\n"
  "v_and_b32 v172, 15, v0\n"
  "v_bfe_u32 v173, v0, 4, 2\n"
  "v_bfe_u32 v33, v0, 5, 1\n"
  "v_bfe_u32 v60, v0, 3, 2\n"
  "v_lshlrev_b32 v60, 4, v60\n"
  "v_lshl_or_b32 v60, v33, 3, v60\n"
  "v_and_b32 v24, 4, v0\n"
  "s_lshl_b32 s88, s52, 6\n"
  "v_or3_b32 v24, v24, s88, v60\n"
  "v_lshlrev_b32 v25, 4, v174\n"
  "s_movk_i32 s80, 0x3f80\n"
  "s_mov_b32 s48, 1.0\n"
  "s_mov_b32 s81, 0x40004\n"
  "s_mov_b32 s82, 0x3b000000\n"
  "s_mov_b32 s83, 0x3727c5ac\n"
  "v_lshrrev_b32 v16, 3, v85\n"
  "v_and_b32 v17, 16, v0\n"
  "v_lshlrev_b32 v17, 2, v17\n"
  "v_xor_b32 v17, v17, v175\n"
  "s_lshl_b32 s88, s56, 6\n"
  "v_xor_b32 v17, s88, v17\n"
  "s_movk_i32 s88, 3584\n"
  "v_mad_u32_u24 v12, v16, s88, v17\n"
  "v_xor_b32 v15, 32, v12\n"
  "v_add_u32 v13, 27648, v15\n"
  "v_add_u32 v14, 55296, v12\n"
  "v_add_u32 v15, 82944, v15\n"
  "s_lshl_b32 s63, s52, 12\n"
  "s_add_i32 s63, s63, 32768\n"
  "s_waitcnt lgkmcnt(0)\n"
  "global_load_short_d16_hi v176, v178, s[16:17] offset:-4096\n"
  "global_load_short_d16_hi v177, v178, s[16:17] offset:-3072\n"
  "global_load_dword v64, v58, s[16:17] offset:-4096\n"
  "global_load_dword v65, v58, s[16:17] offset:-3072\n"
  "global_load_dword v66, v58, s[16:17] offset:-2048\n"
  "global_load_dword v67, v58, s[16:17] offset:-1024\n"
  "global_load_dword v68, v58, s[16:17] offset:0\n"
  "global_load_dword v69, v58, s[16:17] offset:1024\n"
  "global_load_dword v70, v58, s[16:17] offset:2048\n"
  "global_load_dword v71, v58, s[16:17] offset:3072\n"
  "s_barrier\n"
  "buffer_load_dwordx4 v[4:7], v87, s[4:7], 0 offen sc1\n"
  "global_load_ushort v80, v81, s[30:31]\n"
  "global_load_dword v72, v59, s[18:19]\n"
  "global_load_dword v73, v59, s[20:21]\n"
  "global_load_dword v74, v59, s[22:23]\n"
  "global_load_dword v75, v59, s[24:25]\n"
  "global_load_dword v76, v59, s[26:27]\n"
  "global_load_dword v77, v59, s[28:29]\n"
  "global_load_dword v78, v57, s[32:33]\n"
  "s_mov_b32 s89, 0x8000\n"
  "buffer_load_dword v30, v34, s[44:47], s89 offen\n"
  "s_mov_b64 s[44:45], s[4:5]\n"
  "s_mov_b32 s46, 32\n"
  "s_add_i32 s20, s63, 0x0\n"
  "s_add_i32 s21, s63, 0x4000\n"
  "s_add_i32 s22, s63, 0x8000\n"
  "s_add_i32 s23, s63, 0xc000\n"
  "s_add_i32 s24, s63, 0x10000\n"
  "s_add_i32 s25, s63, 0x14000\n"
  "s_add_i32 s26, s63, 0x18000\n"
  "s_add_i32 s27, s63, 0x1c000\n"
  "s_lshr_b32 s61, s55, 2\n"
  "s_and_b32 s62, s55, 3\n"
  "s_lshl2_add_u32 s62, s54, s62\n"
  "s_mul_i32 s65, s52, 262144\n"
  "s_lshl_b32 s84, s62, 7\n"
  "s_add_i32 s65, s65, s84\n"
  "s_cmp_eq_u32 s57, 0\n"
  "s_mov_b32 s84, 0x01000c0c\n"
  "s_mov_b32 s85, 0x03020c0c\n"
  "s_cselect_b32 s78, s84, s85\n"
  "s_cselect_b32 s79, s85, s84\n"
  "v_bfe_u32 v63, v0, 2, 2\n"
  "v_lshl_add_u32 v8, v173, 3, v63\n"
  "v_lshlrev_b32 v8, 7, v8\n"
  "v_lshl_or_b32 v8, v174, 3, v8\n"
  "v_and_b32 v9, 16, v0\n"
  "v_lshl_or_b32 v8, v9, 1, v8\n"
  "v_and_b32 v9, 8, v0\n"
  "v_lshl_or_b32 v8, v9, 3, v8\n"
  "v_add_u32 v8, s63, v8\n"
  "v_xor_b32 v9, 32, v8\n"
  "v_xor_b32 v10, 64, v8\n"
  "v_xor_b32 v11, 64, v9\n"
  "v_add_u32 v23, 0x10000, v8\n"
  "v_add_u32 v53, 0x10000, v9\n"
  "v_add_u32 v54, 0x10000, v10\n"
  "v_add_u32 v55, 0x10000, v11\n"
  "s_mul_i32 s84, s56, 16640\n"
  "s_lshl_b32 s85, s57, 8\n"
  "s_add_i32 s68, s84, s85\n"
  "s_add_i32 s68, s68, 9216\n"
  "v_mul_u32_u24 v27, 4160, v63\n"
  "v_lshl_add_u32 v27, v174, 3, v27\n"
  "v_lshlrev_b32 v63, 4, v173\n"
  "v_sub_u32 v27, v27, v63\n"
  "v_add_u32 v27, s68, v27\n"
  "s_mul_i32 s86, s56, 4608\n"
  "s_sub_i32 s87, s86, s85\n"
  "s_add_i32 s87, s87, 288\n"
  "v_lshl_add_u32 v26, v172, 5, v63\n"
  "v_add_u32 v26, s87, v26\n"
  "v_and_b32 v59, 0x1fc, v57\n"
  "v_add_u32 v170, s86, v59\n"
  "v_mul_u32_u24 v171, 4160, v16\n"
  "v_lshrrev_b32 v60, 2, v175\n"
  "v_bfe_u32 v61, v0, 4, 1\n"
  "v_lshl_add_u32 v60, v61, 2, v60\n"
  "v_add_u32 v171, v171, v60\n"
  "s_mul_i32 s67, s56, 28832\n"
  "v_add_u32 v63, s67, v63\n"
  "s_movk_i32 s86, 544\n"
  "v_mad_u32_u24 v167, v172, s86, v63\n"
  "s_mul_i32 s87, s57, 8704\n"
  "v_add_u32 v169, s87, v167\n"
  "v_lshrrev_b32 v168, 2, v172\n"
  "v_cmp_eq_u32 vcc, 0, v174\n"
  "s_add_i32 s67, s67, s85\n"
  "s_lshl_b32 s87, s57, 4\n"
  "v_cndmask_b32 v168, 4, v168, vcc\n"
  "v_mad_u32_u24 v168, v168, s86, v63\n"
  "v_or_b32 v58, s87, v172\n"
  "s_mul_i32 s87, s56, 1544\n"
  "v_lshlrev_b32 v164, 4, v58\n"
  "v_lshl_or_b32 v164, v173, 3, v164\n"
  "v_lshlrev_b32 v165, 3, v58\n"
  "v_add_u32 v165, 512, v165\n"
  "v_cmp_eq_u32 vcc, 2, v173\n"
  "v_lshlrev_b32 v166, 2, v58\n"
  "v_add_u32 v166, 768, v166\n"
  "v_cndmask_b32 v164, v164, v165, vcc\n"
  "v_cmp_eq_u32 vcc, 3, v173\n"
  "v_mov_b32 v165, 0x80\n"
  "v_add_u32 v152, s87, v59\n"
  "v_cndmask_b32 v164, v164, v166, vcc\n"
  "v_cndmask_b32 v165, 4, v165, vcc\n"
  "v_add_u32 v164, s87, v164\n"
  "v_add_u32 v165, v165, v164\n"
  "v_lshlrev_b32 v166, 6, v173\n"
  "v_lshl_add_u32 v166, v58, 1, v166\n"
  "v_add_u32 v166, s87, v166\n"
  "v_lshrrev_b32 v60, 1, v59\n"
  "v_cvt_f32_u32 v61, v60\n"
  "v_cmp_eq_u32 vcc, 0, v59\n"
  "v_add_f32 v60, -1.0, v61\n"
  "v_add_f32_e64 v62, 1.0, v61\n"
  "v_cndmask_b32 v63, v60, -2.0, vcc\n"
  "s_bfm_b64 exec, 16, 0\n"
  "buffer_load_dword v30, v31, s[8:11], 0 offen\n"
  "s_mov_b64 exec, -1\n"
  "v_mov_b32 v56, 0x4b400000\n"
  "s_sub_i32 s87, s87, 0x967ffff0\n"
  "v_fmamk_f32 v153, v63, 0x3f000000, v56\n"
  "v_fmamk_f32 v154, v61, 0x3f000000, v56\n"
  "v_fmamk_f32 v155, v62, 0x3f000000, v56\n"
  "v_fmamk_f32 v156, v60, 0x3e800000, v56\n"
  "v_fmamk_f32 v157, v61, 0x3e800000, v56\n"
  "v_fmamk_f32 v158, v62, 0x3e800000, v56\n"
  "v_fmamk_f32 v159, v60, 0x3e000000, v56\n"
  "v_fmamk_f32 v160, v61, 0x3e000000, v56\n"
  "v_fmamk_f32 v161, v62, 0x3e000000, v56\n"
  "v_lshl_add_u32 v153, v153, 1, s87\n"
  "v_lshl_add_u32 v154, v154, 1, s87\n"
  "v_lshl_add_u32 v155, v155, 1, s87\n"
  "v_lshl_add_u32 v156, v156, 1, s87\n"
  "v_lshl_add_u32 v157, v157, 1, s87\n"
  "v_lshl_add_u32 v158, v158, 1, s87\n"
  "v_lshl_add_u32 v159, v159, 1, s87\n"
  "v_lshl_add_u32 v160, v160, 1, s87\n"
  "v_lshl_add_u32 v161, v161, 1, s87\n"
  "v_mov_b32 v56, 0\n"
  "s_lshl_b32 s86, s56, 4\n"
  "v_mov_b32 v34, s86\n"
  "v_mov_b32 v35, v87\n"
  "v_lshrrev_b32 v63, 1, v0\n"
  "v_lshl_or_b32 v20, v63, 7, v87\n"
  "v_mul_u32_u24 v21, 4608, v86\n"
  "v_lshl_add_u32 v21, v63, 4, v21\n"
  "s_lshr_b32 s86, s71, 2\n"
  "s_mul_i32 s86, s86, 32768\n"
  "s_and_b32 s87, s71, 3\n"
  "s_lshl_b32 s87, s87, 5\n"
  "s_add_i32 s86, s86, s87\n"
  "s_add_i32 s86, s86, 0x10000\n"
  "v_add_u32 v20, s86, v20\n"
  "v_and_b32 v18, 4, v16\n"
  "v_mad_u32_u24 v16, v18, 3, v16\n"
  "v_lshl_or_b32 v16, v16, 12, v17\n"
  "v_xor_b32 v19, 32, v16\n"
  "v_add_u32 v17, 15360, v19\n"
  "v_add_u32 v18, 30720, v16\n"
  "v_add_u32 v19, 46080, v19\n"
  "s_lshl_b32 s86, s56, 5\n"
  "v_xor_b32 v29, s86, v1\n"
  "s_lshl_b32 s86, s52, 13\n"
  "v_add_u32 v29, s86, v29\n"
  "v_bfe_u32 v52, v0, 1, 4\n"
  "v_lshlrev_b32 v52, 4, v52\n"
  "v_lshl_or_b32 v52, v33, 9, v52\n"
  "v_mul_u32_u24 v63, 1026, v87\n"
  "v_xor_b32 v52, v52, v63\n"
  "s_lshl_b32 s86, s52, 11\n"
  "v_add_u32 v52, s86, v52\n"
  "v_bfe_u32 v22, v0, 3, 2\n"
  "v_lshl_or_b32 v22, v22, 1, v33\n"
  "s_mov_b32 s86, 32768\n"
  "v_mad_u32_u24 v22, v22, s86, v175\n"
  "s_mul_i32 s86, s52, 524288\n"
  "s_lshl_b32 s87, s71, 7\n"
  "s_add_i32 s86, s86, s87\n"
  "s_add_i32 s86, s86, 0x290000\n"
  "v_add_u32 v22, s86, v22\n"
  "v_mov_b32 v63, 0x3c00\n"
  "s_mov_b32 m0, s67\n"
  "ds_write_b32 v56, v56 offset:58884\n"
  "ds_write_addtid_b32 v63\n"
  "ds_write_b32 v56, v56 offset:60428\n"
  "s_waitcnt vmcnt(19)\n"
  "v_mov_b64_dpp v[216:217], v[176:177] row_newbcast:0 row_mask:0xf bank_mask:0xf\n"
  "v_mov_b64_dpp v[218:219], v[176:177] row_newbcast:1 row_mask:0xf bank_mask:0xf\n"
  "v_mov_b64_dpp v[220:221], v[176:177] row_newbcast:2 row_mask:0xf bank_mask:0xf\n"
  "v_mov_b64_dpp v[222:223], v[176:177] row_newbcast:3 row_mask:0xf bank_mask:0xf\n"
  "s_waitcnt vmcnt(15)\n"
  "v_dot2_f32_bf16 v39, v67, s80, v219 neg_lo:[0,0,1]\n"
  "v_dot2_f32_bf16 v47, v67, s48, v219 neg_lo:[0,0,1]\n"
  "v_dot2_f32_bf16 v36, v64, s80, v216 neg_lo:[0,0,1]\n"
  "v_dot2_f32_bf16 v44, v64, s48, v216 neg_lo:[0,0,1]\n"
  "v_dot2_f32_bf16 v37, v65, s80, v217 neg_lo:[0,0,1]\n"
  "v_dot2_f32_bf16 v45, v65, s48, v217 neg_lo:[0,0,1]\n"
  "v_dot2_f32_bf16 v38, v66, s80, v218 neg_lo:[0,0,1]\n"
  "v_dot2_f32_bf16 v46, v66, s48, v218 neg_lo:[0,0,1]\n"
  "s_waitcnt vmcnt(11)\n"
  "v_dot2_f32_bf16 v43, v71, s80, v223 neg_lo:[0,0,1]\n"
  "v_dot2_f32_bf16 v51, v71, s48, v223 neg_lo:[0,0,1]\n"
  "v_dot2_f32_bf16 v40, v68, s80, v220 neg_lo:[0,0,1]\n"
  "v_dot2_f32_bf16 v48, v68, s48, v220 neg_lo:[0,0,1]\n"
  "v_dot2_f32_bf16 v41, v69, s80, v221 neg_lo:[0,0,1]\n"
  "v_dot2_f32_bf16 v49, v69, s48, v221 neg_lo:[0,0,1]\n"
  "v_dot2_f32_bf16 v42, v70, s80, v222 neg_lo:[0,0,1]\n"
  "v_dot2_f32_bf16 v50, v70, s48, v222 neg_lo:[0,0,1]\n"
  "v_pk_mul_f32 v[192:193], v[36:37], v[36:37]\n"
  "v_pk_mul_f32 v[194:195], v[38:39], v[38:39]\n"
  "v_pk_mul_f32 v[196:197], v[40:41], v[40:41]\n"
  "v_pk_mul_f32 v[198:199], v[42:43], v[42:43]\n"
  "v_pk_add_f32 v[184:185], v[36:37], v[44:45]\n"
  "v_pk_add_f32 v[186:187], v[38:39], v[46:47]\n"
  "v_pk_add_f32 v[188:189], v[40:41], v[48:49]\n"
  "v_pk_add_f32 v[190:191], v[42:43], v[50:51]\n"
  "v_pk_fma_f32 v[192:193], v[44:45], v[44:45], v[192:193]\n"
  "v_pk_fma_f32 v[194:195], v[46:47], v[46:47], v[194:195]\n"
  "v_pk_fma_f32 v[196:197], v[48:49], v[48:49], v[196:197]\n"
  "v_pk_fma_f32 v[198:199], v[50:51], v[50:51], v[198:199]\n"
  "v_permlane32_swap_b32 v184, v192\n"
  "v_permlane32_swap_b32 v185, v193\n"
  "v_permlane32_swap_b32 v186, v194\n"
  "v_permlane32_swap_b32 v187, v195\n"
  "v_permlane32_swap_b32 v188, v196\n"
  "v_permlane32_swap_b32 v189, v197\n"
  "v_permlane32_swap_b32 v190, v198\n"
  "v_permlane32_swap_b32 v191, v199\n"
  "v_pk_add_f32 v[184:185], v[184:185], v[192:193]\n"
  "v_pk_add_f32 v[188:189], v[188:189], v[196:197]\n"
  "v_pk_add_f32 v[186:187], v[186:187], v[194:195]\n"
  "v_pk_add_f32 v[190:191], v[190:191], v[198:199]\n"
  "v_permlane16_swap_b32 v184, v188\n"
  "v_permlane16_swap_b32 v185, v189\n"
  "v_permlane16_swap_b32 v186, v190\n"
  "v_permlane16_swap_b32 v187, v191\n"
  "v_pk_add_f32 v[184:185], v[184:185], v[188:189]\n"
  "v_pk_add_f32 v[186:187], v[186:187], v[190:191]\n"
  "s_mov_b32 s84, 0x70007\n"
  "v_add_f32_dpp v192, v184, v184 row_ror:8 row_mask:0xf bank_mask:0x3\n"
  "v_add_f32_dpp v193, v185, v185 row_ror:8 row_mask:0xf bank_mask:0x3\n"
  "s_add_i32 s86, s69, 0x10000\n"
  "v_add_f32_dpp v192, v186, v186 row_ror:8 row_mask:0xf bank_mask:0xc\n"
  "v_add_f32_dpp v193, v187, v187 row_ror:8 row_mask:0xf bank_mask:0xc\n"
  "s_add_i32 s87, s69, 0x18000\n"
  "v_add_f32_dpp v194, v192, v192 row_half_mirror row_mask:0xf bank_mask:0x5\n"
  "v_mov_b32_e64 v56, s83\n"
  "s_waitcnt vmcnt(10)\n"
  "v_add_u32 v4, 0x20012001, v4\n"
  "v_add_f32_dpp v194, v193, v193 row_half_mirror row_mask:0xf bank_mask:0xa\n"
  "v_add_u32 v5, 0x20012001, v5\n"
  "v_add_u32 v6, 0x20012001, v6\n"
  "v_add_f32_dpp v195, v194, v194 quad_perm:[1,0,3,2] row_mask:0xf bank_mask:0xf\n"
  "v_add_u32 v7, 0x20012001, v7\n"
  "s_nop 0\n"
  "v_add_f32_dpp v194, v195, v195 quad_perm:[2,3,0,1] row_mask:0xf bank_mask:0xf\n"
  "ds_write_b32 v24, v194 offset:62208\n"
  "s_bfm_b64 exec, 8, 16\n"
  "buffer_load_dword v30, v31, s[8:11], 0 offen\n"
  "s_mov_b64 exec, -1\n"
  "s_waitcnt lgkmcnt(0)\n"
  "s_barrier\n"
  "ds_read_b128 v[200:203], v25 offset:62208\n"
  "ds_read_b128 v[204:207], v25 offset:62272\n"
  "ds_read_b128 v[208:211], v25 offset:62336\n"
  "ds_read_b128 v[212:215], v25 offset:62400\n"
  "s_waitcnt vmcnt(10)\n"
  "v_lshlrev_b32 v88, 16, v80\n"
  "v_mul_f32 v88, 0x3fb8aa3b, v88\n"
  "v_exp_f32 v88, v88\n"
  "s_waitcnt vmcnt(9)\n"
  "v_perm_b32 v90, v72, v72, s78\n"
  "s_waitcnt vmcnt(8)\n"
  "v_perm_b32 v91, v73, v73, s78\n"
  "v_cvt_pk_bf16_f32 v88, 0, v88\n"
  "s_mov_b32 s88, 0x3fb8aa3b\n"
  "s_mov_b32 s89, 0x3e22f983\n"
  "v_pk_mul_f32 v[90:91], v[90:91], v[88:89] op_sel_hi:[1,0]\n"
  "s_waitcnt vmcnt(7)\n"
  "v_perm_b32 v92, v74, v74, s78\n"
  "s_waitcnt lgkmcnt(0)\n"
  "v_pk_add_f32 v[200:201], v[200:201], v[204:205]\n"
  "v_pk_add_f32 v[202:203], v[202:203], v[206:207]\n"
  "v_pk_add_f32 v[208:209], v[208:209], v[212:213]\n"
  "v_pk_add_f32 v[210:211], v[210:211], v[214:215]\n"
  "s_nop 0\n"
  "v_pk_add_f32 v[202:203], v[202:203], v[210:211]\n"
  "v_pk_add_f32 v[200:201], v[200:201], v[208:209]\n"
  "v_pk_fma_f32 v[202:203], v[202:203], s[82:83], v[56:57] op_sel_hi:[1,0,0]\n"
  "v_pk_mul_f32 v[200:201], v[200:201], s[82:83] op_sel_hi:[1,0]\n"
  "s_nop 0\n"
  "v_pk_fma_f32 v[202:203], v[200:201], v[200:201], v[202:203] neg_lo:[1,0,0] neg_hi:[1,0,0]\n"
  "s_nop 0\n"
  "v_max_f32 v202, v56, v202\n"
  "v_max_f32 v203, v56, v203\n"
  "v_rsq_f32 v202, v202\n"
  "v_rsq_f32_e64 v203, v203\n"
  "s_nop 0\n"
  "v_pk_mul_f32 v[200:201], v[200:201], v[202:203] neg_lo:[1,0] neg_hi:[1,0]\n"
  "v_mov_b64_dpp v[216:217], v[202:203] row_newbcast:0 row_mask:0xf bank_mask:0xf\n"
  "v_mov_b64_dpp v[218:219], v[202:203] row_newbcast:1 row_mask:0xf bank_mask:0xf\n"
  "v_mov_b64_dpp v[220:221], v[202:203] row_newbcast:2 row_mask:0xf bank_mask:0xf\n"
  "v_mov_b64_dpp v[222:223], v[202:203] row_newbcast:3 row_mask:0xf bank_mask:0xf\n"
  "v_mov_b64_dpp v[224:225], v[200:201] row_newbcast:0 row_mask:0xf bank_mask:0xf\n"
  "v_mov_b64_dpp v[226:227], v[200:201] row_newbcast:1 row_mask:0xf bank_mask:0xf\n"
  "v_mov_b64_dpp v[228:229], v[200:201] row_newbcast:2 row_mask:0xf bank_mask:0xf\n"
  "v_mov_b64_dpp v[230:231], v[200:201] row_newbcast:3 row_mask:0xf bank_mask:0xf\n"
  "v_pk_fma_f32 v[200:201], v[36:37], v[216:217], v[224:225]\n"
  "v_pk_fma_f32 v[202:203], v[38:39], v[218:219], v[226:227]\n"
  "v_pk_fma_f32 v[204:205], v[40:41], v[220:221], v[228:229]\n"
  "v_pk_fma_f32 v[206:207], v[42:43], v[222:223], v[230:231]\n"
  "v_cvt_pkrtz_f16_f32 v184, v200, v201\n"
  "v_cvt_pkrtz_f16_f32 v185, v202, v203\n"
  "v_cvt_pkrtz_f16_f32 v186, v204, v205\n"
  "v_cvt_pkrtz_f16_f32 v187, v206, v207\n"
  "v_add_u32 v184, s81, v184\n"
  "v_add_u32 v185, s81, v185\n"
  "v_add_u32 v186, s81, v186\n"
  "v_add_u32 v187, s81, v187\n"
  "v_bfi_b32 v184, s84, v4, v184\n"
  "v_bfi_b32 v185, s84, v5, v185\n"
  "v_bfi_b32 v186, s84, v6, v186\n"
  "v_bfi_b32 v187, s84, v7, v187\n"
  "v_pk_fma_f32 v[208:209], v[44:45], v[216:217], v[224:225]\n"
  "v_pk_fma_f32 v[210:211], v[46:47], v[218:219], v[226:227]\n"
  "v_pk_fma_f32 v[212:213], v[48:49], v[220:221], v[228:229]\n"
  "v_pk_fma_f32 v[214:215], v[50:51], v[222:223], v[230:231]\n"
  "v_cvt_pkrtz_f16_f32 v188, v208, v209\n"
  "v_cvt_pkrtz_f16_f32 v189, v210, v211\n"
  "v_cvt_pkrtz_f16_f32 v190, v212, v213\n"
  "v_cvt_pkrtz_f16_f32 v191, v214, v215\n"
  "v_add_u32 v188, s81, v188\n"
  "v_add_u32 v189, s81, v189\n"
  "v_add_u32 v190, s81, v190\n"
  "v_add_u32 v191, s81, v191\n"
  "v_bfi_b32 v188, s84, v4, v188\n"
  "v_bfi_b32 v189, s84, v5, v189\n"
  "v_bfi_b32 v190, s84, v6, v190\n"
  "v_bfi_b32 v191, s84, v7, v191\n"
  "buffer_store_dwordx4 v[184:187], v84, s[4:7], s86 offen sc1\n"
  "buffer_store_dwordx4 v[188:191], v84, s[4:7], s87 offen sc1\n"
  "s_waitcnt vmcnt(5)\n"
  "v_lshlrev_b32 v58, 16, v78\n"
  "v_and_b32 v59, 0xffff0000, v78\n"
  "v_pk_fma_f32 v[36:37], v[200:201], v[58:59], v[36:37] op_sel:[0,0,0] op_sel_hi:[1,0,1]\n"
  "v_pk_fma_f32 v[38:39], v[202:203], v[58:59], v[38:39] op_sel:[0,0,0] op_sel_hi:[1,0,1]\n"
  "v_pk_fma_f32 v[40:41], v[204:205], v[58:59], v[40:41] op_sel:[0,0,0] op_sel_hi:[1,0,1]\n"
  "v_pk_fma_f32 v[42:43], v[206:207], v[58:59], v[42:43] op_sel:[0,0,0] op_sel_hi:[1,0,1]\n"
  "v_pk_fma_f32 v[44:45], v[208:209], v[58:59], v[44:45] op_sel:[0,1,0] op_sel_hi:[1,1,1]\n"
  "v_pk_fma_f32 v[46:47], v[210:211], v[58:59], v[46:47] op_sel:[0,1,0] op_sel_hi:[1,1,1]\n"
  "v_pk_fma_f32 v[48:49], v[212:213], v[58:59], v[48:49] op_sel:[0,1,0] op_sel_hi:[1,1,1]\n"
  "v_pk_fma_f32 v[50:51], v[214:215], v[58:59], v[50:51] op_sel:[0,1,0] op_sel_hi:[1,1,1]\n"
  "v_perm_b32 v93, v74, v74, s79\n"
  "v_pk_mul_f32 v[90:91], v[90:91], s[88:89]\n"
  "v_perm_b32 v94, v75, v75, s78\n"
  "v_perm_b32 v95, v75, v75, s79\n"
  "v_perm_b32 v96, v76, v76, s78\n"
  "v_perm_b32 v97, v76, v76, s79\n"
  "v_perm_b32 v98, v77, v77, s78\n"
  "v_perm_b32 v99, v77, v77, s79\n"
  "s_mov_b32 s88, 0x41000000\n"
  "s_mov_b32 s89, 0x43000000\n"
  "v_pk_mul_f32 v[116:117], v[90:91], s[88:89] op_sel_hi:[1,0]\n"
  "v_pk_mul_f32 v[118:119], v[90:91], s[88:89] op_sel:[0,1] op_sel_hi:[1,1]\n"
  "v_pk_mul_f32 v[100:101], v[96:97], v[92:93]\n"
  "v_pk_mul_f32 v[102:103], v[96:97], v[94:95]\n"
  "v_pk_add_f32 v[120:121], v[118:119], v[118:119]\n"
  "v_pk_fma_f32 v[100:101], v[98:99], v[94:95], v[100:101] neg_lo:[1,0,0] neg_hi:[1,0,0]\n"
  "v_pk_fma_f32 v[102:103], v[98:99], v[92:93], v[102:103]\n"
  "v_fract_f32 v117, v117\n"
  "v_fract_f32 v119, v119\n"
  "v_max3_f32 v130, |v100|, |v101|, |v102|\n"
  "v_fract_f32 v91, -v91\n"
  "v_max_f32 v130, v130, |v103|\n"
  "v_fract_f32 v121, -v121\n"
  "v_exp_f32 v122, v116\n"
  "v_max_u32_dpp v131, v130, v130 row_ror:8 row_mask:0xf bank_mask:0xf\n"
  "v_cos_f32 v108, v117\n"
  "v_sin_f32 v109, v117\n"
  "v_max_u32_dpp v130, v131, v131 row_ror:4 row_mask:0xf bank_mask:0xf\n"
  "v_exp_f32 v124, v118\n"
  "v_cos_f32 v110, v119\n"
  "v_max_u32_dpp v131, v130, v130 row_ror:2 row_mask:0xf bank_mask:0xf\n"
  "v_sin_f32 v111, v119\n"
  "v_exp_f32 v126, v90\n"
  "v_max_u32_dpp v130, v131, v131 row_ror:1 row_mask:0xf bank_mask:0xf\n"
  "v_cos_f32 v104, v91\n"
  "v_readlane_b32 s84, v130, 0\n"
  "v_readlane_b32 s85, v130, 16\n"
  "v_readlane_b32 s86, v130, 32\n"
  "v_readlane_b32 s87, v130, 48\n"
  "v_sin_f32 v105, v91\n"
  "v_exp_f32 v128, v120\n"
  "v_cos_f32 v106, v121\n"
  "v_sin_f32 v107, v121\n"
  "v_pk_mul_f32 v[108:109], v[108:109], v[122:123] op_sel_hi:[1,0]\n"
  "v_pk_mul_f32 v[110:111], v[110:111], v[124:125] op_sel_hi:[1,0]\n"
  "v_pk_mul_f32 v[104:105], v[104:105], v[126:127] op_sel_hi:[1,0]\n"
  "v_pk_mul_f32 v[106:107], v[106:107], v[128:129] op_sel_hi:[1,0]\n"
  "s_max_u32 s84, s84, s85\n"
  "s_max_u32 s86, s86, s87\n"
  "s_max_u32 s84, s84, s86\n"
  "s_and_b32 s84, s84, 0x7f800000\n"
  "s_max_u32 s84, s84, 0x8000000\n"
  "s_min_u32 s84, s84, 0x78000000\n"
  "s_sub_u32 s85, 0x82800000, s84\n"
  "s_sub_u32 s84, s84, 0x3800000\n"
  "v_mul_f32 v114, s85, v100\n"
  "v_mul_f32 v115, s85, -v102\n"
  "v_mul_f32_e64 v28, s84, v88\n"
  "v_cvt_pk_f16_f32 v232, v108, v109\n"
  "v_cvt_pk_f16_f32 v233, v110, v111\n"
  "v_cvt_pk_f16_f32 v234, v114, v115\n"
  "v_pk_mul_f32 v[132:133], v[108:109], v[108:109] op_sel_hi:[0,1]\n"
  "v_pk_mul_f32 v[134:135], v[110:111], v[108:109] op_sel_hi:[0,1]\n"
  "v_pk_mul_f32 v[136:137], v[114:115], v[104:105] op_sel_hi:[0,1]\n"
  "v_pk_mul_f32 v[138:139], v[114:115], v[106:107] op_sel_hi:[0,1]\n"
  "v_pk_fma_f32 v[112:113], v[108:109], v[108:109], v[132:133] op_sel:[1,1,0] op_sel_hi:[1,0,1] neg_lo:[0,1,0]\n"
  "v_pk_fma_f32 v[110:111], v[110:111], v[108:109], v[134:135] op_sel:[1,1,0] op_sel_hi:[1,0,1] neg_lo:[0,1,0]\n"
  "v_pk_fma_f32 v[92:93], v[114:115], v[104:105], v[136:137] op_sel:[1,1,0] op_sel_hi:[1,0,1] neg_lo:[0,1,0]\n"
  "v_pk_fma_f32 v[94:95], v[114:115], v[106:107], v[138:139] op_sel:[1,1,0] op_sel_hi:[1,0,1] neg_lo:[0,1,0]\n"
  "v_cvt_pk_f16_f32 v235, v112, v113\n"
  "v_cvt_pk_f16_f32 v236, v110, v111\n"
  "v_cvt_pk_f16_f32 v237, v92, v93\n"
  "v_cvt_pk_f16_f32 v238, v94, v95\n"
  "v_pk_mul_f32 v[132:133], v[112:113], v[108:109] op_sel_hi:[0,1]\n"
  "v_pk_mul_f32 v[134:135], v[110:111], v[108:109] op_sel_hi:[0,1]\n"
  "v_pk_mul_f32 v[136:137], v[92:93], v[104:105] op_sel_hi:[0,1]\n"
  "v_pk_mul_f32 v[138:139], v[94:95], v[106:107] op_sel_hi:[0,1]\n"
  "v_pk_fma_f32 v[112:113], v[112:113], v[108:109], v[132:133] op_sel:[1,1,0] op_sel_hi:[1,0,1] neg_lo:[0,1,0]\n"
  "v_pk_fma_f32 v[110:111], v[110:111], v[108:109], v[134:135] op_sel:[1,1,0] op_sel_hi:[1,0,1] neg_lo:[0,1,0]\n"
  "v_pk_fma_f32 v[140:141], v[92:93], v[104:105], v[136:137] op_sel:[1,1,0] op_sel_hi:[1,0,1] neg_lo:[0,1,0]\n"
  "v_pk_fma_f32 v[94:95], v[94:95], v[106:107], v[138:139] op_sel:[1,1,0] op_sel_hi:[1,0,1] neg_lo:[0,1,0]\n"
  "v_cvt_pk_f16_f32 v239, v112, v113\n"
  "v_cvt_pk_f16_f32 v240, v110, v111\n"
  "v_cvt_pk_f16_f32 v241, v140, v141\n"
  "v_cvt_pk_f16_f32 v242, v94, v95\n"
  "ds_write_addtid_b32 v232 offset:544\n"
  "ds_write_addtid_b32 v233 offset:8704\n"
  "ds_write_addtid_b32 v234 offset:17408\n"
  "ds_write_addtid_b32 v235 offset:1088\n"
  "ds_write_addtid_b32 v236 offset:9248\n"
  "ds_write_addtid_b32 v237 offset:17952\n"
  "ds_write_addtid_b32 v238 offset:21760\n"
  "ds_write_addtid_b32 v239 offset:1632\n"
  "ds_write_addtid_b32 v240 offset:9792\n"
  "ds_write_addtid_b32 v241 offset:18496\n"
  "ds_write_addtid_b32 v242 offset:23936\n"
  "v_pk_mul_f32 v[132:133], v[112:113], v[108:109] op_sel_hi:[0,1]\n"
  "v_pk_mul_f32 v[134:135], v[110:111], v[108:109] op_sel_hi:[0,1]\n"
  "v_pk_mul_f32 v[136:137], v[140:141], v[104:105] op_sel_hi:[0,1]\n"
  "v_pk_mul_f32 v[138:139], v[94:95], v[106:107] op_sel_hi:[0,1]\n"
  "v_pk_fma_f32 v[112:113], v[112:113], v[108:109], v[132:133] op_sel:[1,1,0] op_sel_hi:[1,0,1] neg_lo:[0,1,0]\n"
  "v_pk_fma_f32 v[110:111], v[110:111], v[108:109], v[134:135] op_sel:[1,1,0] op_sel_hi:[1,0,1] neg_lo:[0,1,0]\n"
  "v_pk_fma_f32 v[92:93], v[140:141], v[104:105], v[136:137] op_sel:[1,1,0] op_sel_hi:[1,0,1] neg_lo:[0,1,0]\n"
  "v_pk_fma_f32 v[94:95], v[94:95], v[106:107], v[138:139] op_sel:[1,1,0] op_sel_hi:[1,0,1] neg_lo:[0,1,0]\n"
  "v_cvt_pk_f16_f32 v243, v112, v113\n"
  "v_cvt_pk_f16_f32 v244, v110, v111\n"
  "v_cvt_pk_f16_f32 v245, v92, v93\n"
  "v_cvt_pk_f16_f32 v246, v94, v95\n"
  "v_pk_mul_f32 v[132:133], v[112:113], v[108:109] op_sel_hi:[0,1]\n"
  "v_pk_mul_f32 v[134:135], v[110:111], v[108:109] op_sel_hi:[0,1]\n"
  "v_pk_mul_f32 v[136:137], v[92:93], v[104:105] op_sel_hi:[0,1]\n"
  "v_pk_mul_f32 v[138:139], v[94:95], v[106:107] op_sel_hi:[0,1]\n"
  "v_pk_fma_f32 v[112:113], v[112:113], v[108:109], v[132:133] op_sel:[1,1,0] op_sel_hi:[1,0,1] neg_lo:[0,1,0]\n"
  "v_pk_fma_f32 v[110:111], v[110:111], v[108:109], v[134:135] op_sel:[1,1,0] op_sel_hi:[1,0,1] neg_lo:[0,1,0]\n"
  "v_pk_fma_f32 v[142:143], v[92:93], v[104:105], v[136:137] op_sel:[1,1,0] op_sel_hi:[1,0,1] neg_lo:[0,1,0]\n"
  "v_pk_fma_f32 v[94:95], v[94:95], v[106:107], v[138:139] op_sel:[1,1,0] op_sel_hi:[1,0,1] neg_lo:[0,1,0]\n"
  "v_cvt_pk_f16_f32 v247, v112, v113\n"
  "v_cvt_pk_f16_f32 v248, v110, v111\n"
  "v_cvt_pk_f16_f32 v249, v142, v143\n"
  "v_cvt_pk_f16_f32 v250, v94, v95\n"
  "v_pk_mul_f32 v[132:133], v[112:113], v[108:109] op_sel_hi:[0,1]\n"
  "v_pk_mul_f32 v[134:135], v[110:111], v[108:109] op_sel_hi:[0,1]\n"
  "v_pk_mul_f32 v[136:137], v[142:143], v[104:105] op_sel_hi:[0,1]\n"
  "v_pk_mul_f32 v[138:139], v[94:95], v[106:107] op_sel_hi:[0,1]\n"
  "v_pk_fma_f32 v[112:113], v[112:113], v[108:109], v[132:133] op_sel:[1,1,0] op_sel_hi:[1,0,1] neg_lo:[0,1,0]\n"
  "v_pk_fma_f32 v[110:111], v[110:111], v[108:109], v[134:135] op_sel:[1,1,0] op_sel_hi:[1,0,1] neg_lo:[0,1,0]\n"
  "v_pk_fma_f32 v[92:93], v[142:143], v[104:105], v[136:137] op_sel:[1,1,0] op_sel_hi:[1,0,1] neg_lo:[0,1,0]\n"
  "v_pk_fma_f32 v[94:95], v[94:95], v[106:107], v[138:139] op_sel:[1,1,0] op_sel_hi:[1,0,1] neg_lo:[0,1,0]\n"
  "v_cvt_pk_f16_f32 v251, v112, v113\n"
  "v_cvt_pk_f16_f32 v252, v110, v111\n"
  "v_cvt_pk_f16_f32 v253, v92, v93\n"
  "v_cvt_pk_f16_f32 v254, v94, v95\n"
  "ds_write_addtid_b32 v243 offset:2176\n"
  "ds_write_addtid_b32 v244 offset:10336\n"
  "ds_write_addtid_b32 v245 offset:19040\n"
  "ds_write_addtid_b32 v246 offset:25024\n"
  "ds_write_addtid_b32 v247 offset:2720\n"
  "ds_write_addtid_b32 v248 offset:10880\n"
  "ds_write_addtid_b32 v249 offset:19584\n"
  "ds_write_addtid_b32 v250 offset:26112\n"
  "ds_write_addtid_b32 v251 offset:3264\n"
  "ds_write_addtid_b32 v252 offset:11424\n"
  "ds_write_addtid_b32 v253 offset:20128\n"
  "ds_write_addtid_b32 v254 offset:26656\n"
  "s_bfm_b64 exec, 32, 24\n"
  "buffer_load_dword v30, v31, s[8:11], 0 offen\n"
  "s_mov_b64 exec, -1\n"
  "v_pk_mul_f32 v[132:133], v[112:113], v[108:109] op_sel_hi:[0,1]\n"
  "v_pk_mul_f32 v[134:135], v[110:111], v[108:109] op_sel_hi:[0,1]\n"
  "v_pk_mul_f32 v[136:137], v[92:93], v[104:105] op_sel_hi:[0,1]\n"
  "v_pk_mul_f32 v[138:139], v[94:95], v[106:107] op_sel_hi:[0,1]\n"
  "v_pk_fma_f32 v[112:113], v[112:113], v[108:109], v[132:133] op_sel:[1,1,0] op_sel_hi:[1,0,1] neg_lo:[0,1,0]\n"
  "v_pk_fma_f32 v[110:111], v[110:111], v[108:109], v[134:135] op_sel:[1,1,0] op_sel_hi:[1,0,1] neg_lo:[0,1,0]\n"
  "v_pk_fma_f32 v[144:145], v[92:93], v[104:105], v[136:137] op_sel:[1,1,0] op_sel_hi:[1,0,1] neg_lo:[0,1,0]\n"
  "v_pk_fma_f32 v[94:95], v[94:95], v[106:107], v[138:139] op_sel:[1,1,0] op_sel_hi:[1,0,1] neg_lo:[0,1,0]\n"
  "v_cvt_pk_f16_f32 v255, v112, v113\n"
  "v_cvt_pk_f16_f32 v232, v110, v111\n"
  "v_cvt_pk_f16_f32 v233, v144, v145\n"
  "v_cvt_pk_f16_f32 v234, v94, v95\n"
  "v_pk_mul_f32 v[132:133], v[112:113], v[108:109] op_sel_hi:[0,1]\n"
  "v_pk_mul_f32 v[134:135], v[110:111], v[108:109] op_sel_hi:[0,1]\n"
  "v_pk_mul_f32 v[136:137], v[144:145], v[104:105] op_sel_hi:[0,1]\n"
  "v_pk_mul_f32 v[138:139], v[94:95], v[106:107] op_sel_hi:[0,1]\n"
  "v_pk_fma_f32 v[112:113], v[112:113], v[108:109], v[132:133] op_sel:[1,1,0] op_sel_hi:[1,0,1] neg_lo:[0,1,0]\n"
  "v_pk_fma_f32 v[110:111], v[110:111], v[108:109], v[134:135] op_sel:[1,1,0] op_sel_hi:[1,0,1] neg_lo:[0,1,0]\n"
  "v_pk_fma_f32 v[92:93], v[144:145], v[104:105], v[136:137] op_sel:[1,1,0] op_sel_hi:[1,0,1] neg_lo:[0,1,0]\n"
  "v_pk_fma_f32 v[94:95], v[94:95], v[106:107], v[138:139] op_sel:[1,1,0] op_sel_hi:[1,0,1] neg_lo:[0,1,0]\n"
  "v_cvt_pk_f16_f32 v235, v112, v113\n"
  "v_cvt_pk_f16_f32 v236, v110, v111\n"
  "v_cvt_pk_f16_f32 v237, v92, v93\n"
  "v_cvt_pk_f16_f32 v238, v94, v95\n"
  "v_pk_mul_f32 v[132:133], v[112:113], v[108:109] op_sel_hi:[0,1]\n"
  "v_pk_mul_f32 v[134:135], v[110:111], v[108:109] op_sel_hi:[0,1]\n"
  "v_pk_mul_f32 v[136:137], v[142:143], v[106:107] op_sel_hi:[0,1]\n"
  "v_pk_mul_f32 v[138:139], v[94:95], v[106:107] op_sel_hi:[0,1]\n"
  "v_pk_fma_f32 v[112:113], v[112:113], v[108:109], v[132:133] op_sel:[1,1,0] op_sel_hi:[1,0,1] neg_lo:[0,1,0]\n"
  "v_pk_fma_f32 v[110:111], v[110:111], v[108:109], v[134:135] op_sel:[1,1,0] op_sel_hi:[1,0,1] neg_lo:[0,1,0]\n"
  "v_pk_fma_f32 v[146:147], v[142:143], v[106:107], v[136:137] op_sel:[1,1,0] op_sel_hi:[1,0,1] neg_lo:[0,1,0]\n"
  "v_pk_fma_f32 v[94:95], v[94:95], v[106:107], v[138:139] op_sel:[1,1,0] op_sel_hi:[1,0,1] neg_lo:[0,1,0]\n"
  "v_cvt_pk_f16_f32 v239, v112, v113\n"
  "v_cvt_pk_f16_f32 v240, v110, v111\n"
  "v_cvt_pk_f16_f32 v241, v146, v147\n"
  "v_cvt_pk_f16_f32 v242, v94, v95\n"
  "ds_write_addtid_b32 v255 offset:3808\n"
  "ds_write_addtid_b32 v232 offset:11968\n"
  "ds_write_addtid_b32 v233 offset:20672\n"
  "ds_write_addtid_b32 v234 offset:27200\n"
  "ds_write_addtid_b32 v235 offset:4352\n"
  "ds_write_addtid_b32 v236 offset:12512\n"
  "ds_write_addtid_b32 v237 offset:21216\n"
  "ds_write_addtid_b32 v238 offset:27744\n"
  "ds_write_addtid_b32 v239 offset:4896\n"
  "ds_write_addtid_b32 v240 offset:13056\n"
  "ds_write_addtid_b32 v241 offset:22848\n"
  "ds_write_addtid_b32 v242 offset:28288\n"
  "v_pk_mul_f32 v[132:133], v[112:113], v[108:109] op_sel_hi:[0,1]\n"
  "v_pk_mul_f32 v[134:135], v[110:111], v[108:109] op_sel_hi:[0,1]\n"
  "v_pk_mul_f32 v[136:137], v[146:147], v[106:107] op_sel_hi:[0,1]\n"
  "v_pk_mul_f32 v[138:139], v[140:141], v[106:107] op_sel_hi:[0,1]\n"
  "v_pk_fma_f32 v[112:113], v[112:113], v[108:109], v[132:133] op_sel:[1,1,0] op_sel_hi:[1,0,1] neg_lo:[0,1,0]\n"
  "v_pk_fma_f32 v[110:111], v[110:111], v[108:109], v[134:135] op_sel:[1,1,0] op_sel_hi:[1,0,1] neg_lo:[0,1,0]\n"
  "v_pk_fma_f32 v[146:147], v[146:147], v[106:107], v[136:137] op_sel:[1,1,0] op_sel_hi:[1,0,1] neg_lo:[0,1,0]\n"
  "v_pk_fma_f32 v[96:97], v[140:141], v[106:107], v[138:139] op_sel:[1,1,0] op_sel_hi:[1,0,1] neg_lo:[0,1,0]\n"
  "v_cvt_pk_f16_f32 v243, v112, v113\n"
  "v_cvt_pk_f16_f32 v244, v110, v111\n"
  "v_cvt_pk_f16_f32 v245, v146, v147\n"
  "v_cvt_pk_f16_f32 v246, v96, v97\n"
  "v_pk_mul_f32 v[132:133], v[112:113], v[108:109] op_sel_hi:[0,1]\n"
  "v_pk_mul_f32 v[134:135], v[110:111], v[108:109] op_sel_hi:[0,1]\n"
  "v_pk_mul_f32 v[136:137], v[146:147], v[106:107] op_sel_hi:[0,1]\n"
  "v_pk_fma_f32 v[112:113], v[112:113], v[108:109], v[132:133] op_sel:[1,1,0] op_sel_hi:[1,0,1] neg_lo:[0,1,0]\n"
  "v_pk_fma_f32 v[110:111], v[110:111], v[108:109], v[134:135] op_sel:[1,1,0] op_sel_hi:[1,0,1] neg_lo:[0,1,0]\n"
  "v_pk_fma_f32 v[146:147], v[146:147], v[106:107], v[136:137] op_sel:[1,1,0] op_sel_hi:[1,0,1] neg_lo:[0,1,0]\n"
  "v_cvt_pk_f16_f32 v247, v112, v113\n"
  "v_cvt_pk_f16_f32 v248, v110, v111\n"
  "v_cvt_pk_f16_f32 v249, v146, v147\n"
  "v_pk_mul_f32 v[132:133], v[112:113], v[108:109] op_sel_hi:[0,1]\n"
  "v_pk_mul_f32 v[134:135], v[110:111], v[108:109] op_sel_hi:[0,1]\n"
  "v_pk_fma_f32 v[112:113], v[112:113], v[108:109], v[132:133] op_sel:[1,1,0] op_sel_hi:[1,0,1] neg_lo:[0,1,0]\n"
  "v_pk_fma_f32 v[110:111], v[110:111], v[108:109], v[134:135] op_sel:[1,1,0] op_sel_hi:[1,0,1] neg_lo:[0,1,0]\n"
  "v_cvt_pk_f16_f32 v250, v112, v113\n"
  "v_cvt_pk_f16_f32 v251, v110, v111\n"
  "v_pk_mul_f32 v[132:133], v[112:113], v[108:109] op_sel_hi:[0,1]\n"
  "v_pk_mul_f32 v[134:135], v[110:111], v[108:109] op_sel_hi:[0,1]\n"
  "v_pk_fma_f32 v[112:113], v[112:113], v[108:109], v[132:133] op_sel:[1,1,0] op_sel_hi:[1,0,1] neg_lo:[0,1,0]\n"
  "v_pk_fma_f32 v[110:111], v[110:111], v[108:109], v[134:135] op_sel:[1,1,0] op_sel_hi:[1,0,1] neg_lo:[0,1,0]\n"
  "v_cvt_pk_f16_f32 v252, v112, v113\n"
  "v_cvt_pk_f16_f32 v253, v110, v111\n"
  "ds_write_addtid_b32 v243 offset:5440\n"
  "ds_write_addtid_b32 v244 offset:13600\n"
  "ds_write_addtid_b32 v245 offset:24480\n"
  "ds_write_addtid_b32 v246 offset:22304\n"
  "ds_write_addtid_b32 v247 offset:5984\n"
  "ds_write_addtid_b32 v248 offset:14144\n"
  "ds_write_addtid_b32 v249 offset:25568\n"
  "ds_write_addtid_b32 v250 offset:6528\n"
  "ds_write_addtid_b32 v251 offset:14688\n"
  "ds_write_addtid_b32 v252 offset:7072\n"
  "ds_write_addtid_b32 v253 offset:15232\n"
  "v_pk_mul_f32 v[132:133], v[112:113], v[108:109] op_sel_hi:[0,1]\n"
  "v_pk_mul_f32 v[134:135], v[110:111], v[108:109] op_sel_hi:[0,1]\n"
  "v_pk_fma_f32 v[112:113], v[112:113], v[108:109], v[132:133] op_sel:[1,1,0] op_sel_hi:[1,0,1] neg_lo:[0,1,0]\n"
  "v_pk_fma_f32 v[110:111], v[110:111], v[108:109], v[134:135] op_sel:[1,1,0] op_sel_hi:[1,0,1] neg_lo:[0,1,0]\n"
  "v_cvt_pk_f16_f32 v254, v112, v113\n"
  "v_cvt_pk_f16_f32 v255, v110, v111\n"
  "v_pk_mul_f32 v[132:133], v[112:113], v[108:109] op_sel_hi:[0,1]\n"
  "v_pk_mul_f32 v[134:135], v[110:111], v[108:109] op_sel_hi:[0,1]\n"
  "v_pk_fma_f32 v[112:113], v[112:113], v[108:109], v[132:133] op_sel:[1,1,0] op_sel_hi:[1,0,1] neg_lo:[0,1,0]\n"
  "v_pk_fma_f32 v[110:111], v[110:111], v[108:109], v[134:135] op_sel:[1,1,0] op_sel_hi:[1,0,1] neg_lo:[0,1,0]\n"
  "v_cvt_pk_f16_f32 v232, v112, v113\n"
  "v_cvt_pk_f16_f32 v233, v110, v111\n"
  "v_pk_mul_f32 v[134:135], v[110:111], v[108:109] op_sel_hi:[0,1]\n"
  "v_pk_mul_f32 v[136:137], v[144:145], v[106:107] op_sel_hi:[0,1]\n"
  "v_pk_fma_f32 v[110:111], v[110:111], v[108:109], v[134:135] op_sel:[1,1,0] op_sel_hi:[1,0,1] neg_lo:[0,1,0]\n"
  "v_pk_fma_f32 v[96:97], v[144:145], v[106:107], v[136:137] op_sel:[1,1,0] op_sel_hi:[1,0,1] neg_lo:[0,1,0]\n"
  "v_cvt_pk_f16_f32 v234, v110, v111\n"
  "v_cvt_pk_f16_f32 v235, v96, v97\n"
  "ds_write_addtid_b32 v254 offset:7616\n"
  "ds_write_addtid_b32 v255 offset:15776\n"
  "ds_write_addtid_b32 v232 offset:8160\n"
  "ds_write_addtid_b32 v233 offset:16320\n"
  "ds_write_addtid_b32 v234 offset:16864\n"
  "ds_write_addtid_b32 v235 offset:23392\n"
  "v_mov_b32 v32, v176\n"
  "v_mov_b32 v31, v177\n"
  "s_waitcnt lgkmcnt(0)\n"
  "s_barrier\n"
  "ds_read_b128 v[56:59], v169 offset:0\n"
  "ds_read_b128 v[184:187], v167 offset:17408\n"
  "ds_read_b128 v[216:219], v168 offset:26112\n"
  "s_mov_b32 m0, s22\n"
  "s_add_i32 s66, s64, 0x0\n"
  "buffer_load_dwordx4 v12, s[8:11], s66 offen offset:0 lds\n"
  "ds_read_b128 v[60:63], v169 offset:64\n"
  "ds_read_b128 v[188:191], v167 offset:17472\n"
  "ds_read_b128 v[220:223], v168 offset:26176\n"
  "buffer_load_dwordx4 v13, s[8:11], s66 offen offset:1024 lds\n"
  "ds_read_b128 v[64:67], v169 offset:128\n"
  "ds_read_b128 v[192:195], v167 offset:17536\n"
  "ds_read_b128 v[224:227], v168 offset:26240\n"
  "buffer_load_dwordx4 v14, s[8:11], s66 offen offset:2048 lds\n"
  "ds_read_b128 v[68:71], v169 offset:192\n"
  "ds_read_b128 v[196:199], v167 offset:17600\n"
  "ds_read_b128 v[228:231], v168 offset:26304\n"
  "buffer_load_dwordx4 v15, s[8:11], s66 offen offset:3072 lds\n"
  "s_waitcnt lgkmcnt(9)\n"
  "v_mfma_f32_16x16x32_f16 v[92:95], v[216:219], v[56:59], 0\n"
  "v_mfma_f32_16x16x32_f16 v[88:91], v[184:187], v[56:59], 0\n"
  "ds_read_b128 v[72:75], v169 offset:256\n"
  "ds_read_b128 v[200:203], v167 offset:17664\n"
  "ds_read_b128 v[232:235], v168 offset:26368\n"
  "s_waitcnt lgkmcnt(9)\n"
  "v_mfma_f32_16x16x32_f16 v[92:95], v[220:223], v[60:63], v[92:95]\n"
  "v_mfma_f32_16x16x32_f16 v[88:91], v[188:191], v[60:63], v[88:91]\n"
  "ds_read_b128 v[76:79], v169 offset:320\n"
  "ds_read_b128 v[204:207], v167 offset:17728\n"
  "ds_read_b128 v[236:239], v168 offset:26432\n"
  "s_waitcnt lgkmcnt(9)\n"
  "v_mfma_f32_16x16x32_f16 v[92:95], v[224:227], v[64:67], v[92:95]\n"
  "v_mfma_f32_16x16x32_f16 v[88:91], v[192:195], v[64:67], v[88:91]\n"
  "ds_read_b128 v[80:83], v169 offset:384\n"
  "ds_read_b128 v[208:211], v167 offset:17792\n"
  "ds_read_b128 v[240:243], v168 offset:26496\n"
  "s_waitcnt lgkmcnt(9)\n"
  "v_mfma_f32_16x16x32_f16 v[92:95], v[228:231], v[68:71], v[92:95]\n"
  "v_mfma_f32_16x16x32_f16 v[88:91], v[196:199], v[68:71], v[88:91]\n"
  "ds_read_b128 v[84:87], v169 offset:448\n"
  "ds_read_b128 v[212:215], v167 offset:17856\n"
  "ds_read_b128 v[244:247], v168 offset:26560\n"
  "s_waitcnt lgkmcnt(9)\n"
  "v_mfma_f32_16x16x32_f16 v[92:95], v[232:235], v[72:75], v[92:95]\n"
  "v_mfma_f32_16x16x32_f16 v[88:91], v[200:203], v[72:75], v[88:91]\n"
  "s_waitcnt lgkmcnt(6)\n"
  "v_mfma_f32_16x16x32_f16 v[92:95], v[236:239], v[76:79], v[92:95]\n"
  "v_mfma_f32_16x16x32_f16 v[88:91], v[204:207], v[76:79], v[88:91]\n"
  "s_waitcnt lgkmcnt(3)\n"
  "v_mfma_f32_16x16x32_f16 v[92:95], v[240:243], v[80:83], v[92:95]\n"
  "v_mfma_f32_16x16x32_f16 v[88:91], v[208:211], v[80:83], v[88:91]\n"
  "s_waitcnt lgkmcnt(0)\n"
  "v_mfma_f32_16x16x32_f16 v[92:95], v[244:247], v[84:87], v[92:95]\n"
  "v_mfma_f32_16x16x32_f16 v[88:91], v[212:215], v[84:87], v[88:91]\n"
  "v_mov_b32 v96, 0\n"
  "s_nop 5\n"
  "v_cvt_pk_f16_f32 v92, v92, v93\n"
  "v_cvt_pk_f16_f32 v88, v88, v89\n"
  "v_cvt_pk_f16_f32 v89, v90, v91\n"
  "ds_write_b32 v164, v88 offset:58888\n"
  "ds_write_b32 v165, v89 offset:58888\n"
  "ds_write_b16 v166, v92 offset:59912\n"
  "ds_write_b16_d16_hi v166, v92 offset:60168\n"
  "ds_write_b32 v34, v28 offset:62464\n"
  "s_waitcnt lgkmcnt(0)\n"
  "s_barrier\n"
  "ds_read_u16 v56, v152 offset:58886\n"
  "ds_read_b32 v100, v152 offset:58888\n"
  "ds_read_u16 v59, v153 offset:59384\n"
  "ds_read_u16 v60, v154 offset:59384\n"
  "ds_read_u16 v61, v155 offset:59384\n"
  "ds_read_u16 v62, v156 offset:59640\n"
  "ds_read_u16 v63, v157 offset:59640\n"
  "ds_read_u16 v64, v158 offset:59640\n"
  "ds_read_u16 v65, v156 offset:59768\n"
  "ds_read_u16 v66, v157 offset:59768\n"
  "ds_read_u16 v67, v158 offset:59768\n"
  "ds_read_u16 v68, v159 offset:59896\n"
  "ds_read_u16 v69, v160 offset:59896\n"
  "ds_read_u16 v70, v161 offset:59896\n"
  "ds_read_u16 v71, v159 offset:59960\n"
  "ds_read_u16 v72, v160 offset:59960\n"
  "ds_read_u16 v73, v161 offset:59960\n"
  "ds_read_u16 v74, v159 offset:60024\n"
  "ds_read_u16 v75, v160 offset:60024\n"
  "ds_read_u16 v76, v161 offset:60024\n"
  "ds_read_u16 v77, v159 offset:60088\n"
  "ds_read_u16 v78, v160 offset:60088\n"
  "ds_read_u16 v79, v161 offset:60088\n"
  "ds_read_b32 v28, v35 offset:62464\n"
  "ds_write_b32 v170, v96 offset:0\n"
  "ds_write_b32 v171, v96 offset:9216\n"
  "s_movk_i32 s77, 0x4000\n"
  "buffer_load_dwordx4 v[248:251], v20, s[4:7], 0 offen sc1\n"
  "buffer_load_dwordx4 v[252:255], v20, s[4:7], s77 offen sc1\n"
  "s_add_i32 m0, s68, 32\n"
  "s_waitcnt lgkmcnt(14)\n"
  "v_lshl_or_b32 v103, v67, 16, v66\n"
  "v_lshl_or_b32 v111, v66, 16, v65\n"
  "v_lshl_or_b32 v102, v64, 16, v63\n"
  "v_lshl_or_b32 v110, v63, 16, v62\n"
  "v_lshl_or_b32 v101, v61, 16, v60\n"
  "v_lshl_or_b32 v109, v60, 16, v59\n"
  "v_lshl_or_b32 v108, v100, 16, v56\n"
  "ds_write_addtid_b32 v100 offset:0\n"
  "ds_write_addtid_b32 v108 offset:4160\n"
  "ds_write_addtid_b32 v100 offset:8324\n"
  "ds_write_addtid_b32 v108 offset:12484\n"
  "ds_write_addtid_b32 v101 offset:512\n"
  "ds_write_addtid_b32 v109 offset:4672\n"
  "ds_write_addtid_b32 v101 offset:8836\n"
  "ds_write_addtid_b32 v109 offset:12996\n"
  "ds_write_addtid_b32 v102 offset:1024\n"
  "ds_write_addtid_b32 v110 offset:5184\n"
  "ds_write_addtid_b32 v102 offset:9348\n"
  "ds_write_addtid_b32 v110 offset:13508\n"
  "ds_write_addtid_b32 v103 offset:1536\n"
  "ds_write_addtid_b32 v111 offset:5696\n"
  "ds_write_addtid_b32 v103 offset:9860\n"
  "ds_write_addtid_b32 v111 offset:14020\n"
  "s_waitcnt lgkmcnt(14)\n"
  "v_lshl_or_b32 v107, v79, 16, v78\n"
  "v_lshl_or_b32 v115, v78, 16, v77\n"
  "v_lshl_or_b32 v106, v76, 16, v75\n"
  "v_lshl_or_b32 v114, v75, 16, v74\n"
  "v_lshl_or_b32 v105, v73, 16, v72\n"
  "v_lshl_or_b32 v113, v72, 16, v71\n"
  "v_lshl_or_b32 v104, v70, 16, v69\n"
  "v_lshl_or_b32 v112, v69, 16, v68\n"
  "ds_write_addtid_b32 v104 offset:2048\n"
  "ds_write_addtid_b32 v112 offset:6208\n"
  "ds_write_addtid_b32 v104 offset:10372\n"
  "ds_write_addtid_b32 v112 offset:14532\n"
  "ds_write_addtid_b32 v105 offset:2560\n"
  "ds_write_addtid_b32 v113 offset:6720\n"
  "ds_write_addtid_b32 v105 offset:10884\n"
  "ds_write_addtid_b32 v113 offset:15044\n"
  "ds_write_addtid_b32 v106 offset:3072\n"
  "ds_write_addtid_b32 v114 offset:7232\n"
  "ds_write_addtid_b32 v106 offset:11396\n"
  "ds_write_addtid_b32 v114 offset:15556\n"
  "ds_write_addtid_b32 v107 offset:3584\n"
  "ds_write_addtid_b32 v115 offset:7744\n"
  "ds_write_addtid_b32 v107 offset:11908\n"
  "ds_write_addtid_b32 v115 offset:16068\n"
  "s_waitcnt lgkmcnt(0)\n"
  "s_barrier\n"
  "ds_read_b64_tr_b16 v[56:57], v27 offset:64\n"
  "ds_read_b64_tr_b16 v[58:59], v27 offset:56\n"
  "ds_read_b64_tr_b16 v[60:61], v27 offset:576\n"
  "ds_read_b64_tr_b16 v[62:63], v27 offset:568\n"
  "ds_read_b64_tr_b16 v[64:65], v27 offset:1088\n"
  "ds_read_b64_tr_b16 v[66:67], v27 offset:1080\n"
  "ds_read_b64_tr_b16 v[68:69], v27 offset:1600\n"
  "ds_read_b64_tr_b16 v[70:71], v27 offset:1592\n"
  "ds_read_b64_tr_b16 v[72:73], v27 offset:2112\n"
  "ds_read_b64_tr_b16 v[74:75], v27 offset:2104\n"
  "ds_read_b64_tr_b16 v[76:77], v27 offset:2624\n"
  "ds_read_b64_tr_b16 v[78:79], v27 offset:2616\n"
  "ds_read_b64_tr_b16 v[80:81], v27 offset:3136\n"
  "ds_read_b64_tr_b16 v[82:83], v27 offset:3128\n"
  "ds_read_b64_tr_b16 v[84:85], v27 offset:3648\n"
  "ds_read_b64_tr_b16 v[86:87], v27 offset:3640\n"
  "ds_read_b64_tr_b16 v[88:89], v27 offset:128\n"
  "ds_read_b64_tr_b16 v[90:91], v27 offset:120\n"
  "ds_read_b64_tr_b16 v[92:93], v27 offset:640\n"
  "ds_read_b64_tr_b16 v[94:95], v27 offset:632\n"
  "ds_read_b64_tr_b16 v[96:97], v27 offset:1152\n"
  "ds_read_b64_tr_b16 v[98:99], v27 offset:1144\n"
  "ds_read_b64_tr_b16 v[100:101], v27 offset:1664\n"
  "ds_read_b64_tr_b16 v[102:103], v27 offset:1656\n"
  "ds_read_b64_tr_b16 v[104:105], v27 offset:2176\n"
  "ds_read_b64_tr_b16 v[106:107], v27 offset:2168\n"
  "ds_read_b64_tr_b16 v[108:109], v27 offset:2688\n"
  "ds_read_b64_tr_b16 v[110:111], v27 offset:2680\n"
  "ds_read_b64_tr_b16 v[112:113], v27 offset:3200\n"
  "ds_read_b64_tr_b16 v[114:115], v27 offset:3192\n"
  "ds_read_b64_tr_b16 v[116:117], v27 offset:3712\n"
  "ds_read_b64_tr_b16 v[118:119], v27 offset:3704\n"
  "s_mov_b32 s84, 0x70007\n"
  "s_mov_b32 s85, 0xfff8fff8\n"
  "s_waitcnt vmcnt(0)\n"
  "v_bitop3_b32 v184, v248, v252, v4 bitop3:0x7e\n"
  "v_bitop3_b32 v185, v249, v253, v5 bitop3:0x7e\n"
  "v_bitop3_b32 v186, v250, v254, v6 bitop3:0x7e\n"
  "v_bitop3_b32 v187, v251, v255, v7 bitop3:0x7e\n"
  "v_or3_b32 v184, v184, v185, v186\n"
  "v_bitop3_b32 v184, v184, s84, v187 bitop3:0xc8\n"
  "v_cmp_ne_u32 vcc, 0, v184\n"
  "s_cbranch_vccnz Lslow0\n"
  "LbackUa:\n"
  "v_and_b32 v248, s85, v248\n"
  "v_and_b32 v249, s85, v249\n"
  "v_and_b32 v250, s85, v250\n"
  "v_and_b32 v251, s85, v251\n"
  "v_and_b32 v252, s85, v252\n"
  "v_and_b32 v253, s85, v253\n"
  "v_and_b32 v254, s85, v254\n"
  "v_and_b32 v255, s85, v255\n"
  "ds_write_b128 v21, v[248:251] offset:512\n"
  "ds_write_b128 v21, v[252:255] offset:2560\n"
  "s_waitcnt lgkmcnt(0)\n"
  "s_barrier\n"
  "ds_read_b128 v[184:187], v26 offset:192\n"
  "ds_read_b128 v[188:191], v26 offset:704\n"
  "s_cmp_lt_u32 s55, 28\n"
  "s_cbranch_scc0 Lidle\n"
  "s_waitcnt lgkmcnt(1)\n"
  "v_mfma_f32_16x16x32_f16 v[216:219], v[56:59], v[184:187], 0\n"
  "ds_read_b128 v[192:195], v26 offset:1216\n"
  "v_mfma_f32_16x16x32_f16 v[220:223], v[60:63], v[184:187], 0\n"
  "ds_read_b128 v[196:199], v26 offset:1728\n"
  "v_mfma_f32_16x16x32_f16 v[224:227], v[64:67], v[184:187], 0\n"
  "ds_read_b128 v[200:203], v26 offset:2240\n"
  "v_mfma_f32_16x16x32_f16 v[228:231], v[68:71], v[184:187], 0\n"
  "s_or_b32 s84, s2, s52\n"
  "s_cmp_eq_u32 s84, 0\n"
  "s_cselect_b32 s84, 0, 0x7fffffff\n"
  "v_mfma_f32_16x16x32_f16 v[232:235], v[72:75], v[184:187], 0\n"
  "s_mov_b64 exec, 3\n"
  "buffer_store_dwordx4 v[4:7], v1, s[44:47], s84 offen sc1\n"
  "s_mov_b64 exec, -1\n"
  "v_mfma_f32_16x16x32_f16 v[236:239], v[76:79], v[184:187], 0\n"
  "v_readfirstlane_b32 s48, v4\n"
  "v_readfirstlane_b32 s49, v5\n"
  "v_mfma_f32_16x16x32_f16 v[240:243], v[80:83], v[184:187], 0\n"
  "v_and_b32 v4, 0x60006000, v4\n"
  "v_and_b32 v5, 0x60006000, v5\n"
  "v_mfma_f32_16x16x32_f16 v[244:247], v[84:87], v[184:187], 0\n"
  "v_and_b32 v6, 0x60006000, v6\n"
  "v_and_b32 v7, 0x60006000, v7\n"
  "s_waitcnt lgkmcnt(3)\n"
  "v_mfma_f32_16x16x32_f16 v[220:223], v[56:59], v[188:191], v[220:223]\n"
  "s_mov_b32 m0, s21\n"
  "s_add_i32 s66, s66, 0x1c000\n"
  "buffer_load_dwordx4 v12, s[8:11], s66 offen offset:0 lds\n"
  "v_mfma_f32_16x16x32_f16 v[224:227], v[60:63], v[188:191], v[224:227]\n"
  "v_mul_f32 v28, 0x13800000, v28\n"
  "s_mov_b32 s77, 262144\n"
  "v_mfma_f32_16x16x32_f16 v[228:231], v[64:67], v[188:191], v[228:231]\n"
  "ds_read_b128 v[204:207], v26 offset:2752\n"
  "v_mfma_f32_16x16x32_f16 v[232:235], v[68:71], v[188:191], v[232:235]\n"
  "ds_read_b128 v[208:211], v26 offset:3264\n"
  "v_mfma_f32_16x16x32_f16 v[236:239], v[72:75], v[188:191], v[236:239]\n"
  "buffer_load_dwordx4 v13, s[8:11], s66 offen offset:1024 lds\n"
  "v_mfma_f32_16x16x32_f16 v[240:243], v[76:79], v[188:191], v[240:243]\n"
  "ds_read_b128 v[212:215], v26 offset:3776\n"
  "v_mfma_f32_16x16x32_f16 v[244:247], v[80:83], v[188:191], v[244:247]\n"
  "ds_read_b128 v[184:187], v26 offset:128\n"
  "s_waitcnt lgkmcnt(6)\n"
  "v_mfma_f32_16x16x32_f16 v[224:227], v[56:59], v[192:195], v[224:227]\n"
  "ds_read_b128 v[188:191], v26 offset:640\n"
  "v_mfma_f32_16x16x32_f16 v[228:231], v[60:63], v[192:195], v[228:231]\n"
  "ds_read_b64_tr_b16 v[120:121], v27 offset:192\n"
  "v_mfma_f32_16x16x32_f16 v[232:235], v[64:67], v[192:195], v[232:235]\n"
  "buffer_load_dwordx4 v14, s[8:11], s66 offen offset:2048 lds\n"
  "v_mfma_f32_16x16x32_f16 v[236:239], v[68:71], v[192:195], v[236:239]\n"
  "ds_read_b64_tr_b16 v[122:123], v27 offset:184\n"
  "v_mfma_f32_16x16x32_f16 v[240:243], v[72:75], v[192:195], v[240:243]\n"
  "ds_read_b64_tr_b16 v[124:125], v27 offset:704\n"
  "v_mfma_f32_16x16x32_f16 v[244:247], v[76:79], v[192:195], v[244:247]\n"
  "ds_read_b128 v[192:195], v26 offset:1152\n"
  "s_waitcnt lgkmcnt(9)\n"
  "v_mfma_f32_16x16x32_f16 v[228:231], v[56:59], v[196:199], v[228:231]\n"
  "buffer_load_dwordx4 v15, s[8:11], s66 offen offset:3072 lds\n"
  "v_mfma_f32_16x16x32_f16 v[232:235], v[60:63], v[196:199], v[232:235]\n"
  "ds_read_b64_tr_b16 v[126:127], v27 offset:696\n"
  "s_mov_b32 m0, s23\n"
  "v_mfma_f32_16x16x32_f16 v[236:239], v[64:67], v[196:199], v[236:239]\n"
  "ds_read_b64_tr_b16 v[128:129], v27 offset:1216\n"
  "s_add_i32 s66, s64, 0xe0000\n"
  "v_mfma_f32_16x16x32_f16 v[240:243], v[68:71], v[196:199], v[240:243]\n"
  "ds_read_b64_tr_b16 v[130:131], v27 offset:1208\n"
  "v_mfma_f32_16x16x32_f16 v[244:247], v[72:75], v[196:199], v[244:247]\n"
  "ds_read_b128 v[196:199], v26 offset:1664\n"
  "v_mfma_f32_16x16x32_f16 v[232:235], v[56:59], v[200:203], v[232:235]\n"
  "buffer_load_dwordx4 v12, s[8:11], s66 offen offset:0 lds\n"
  "v_mfma_f32_16x16x32_f16 v[236:239], v[60:63], v[200:203], v[236:239]\n"
  "ds_read_b64_tr_b16 v[132:133], v27 offset:1728\n"
  "v_mfma_f32_16x16x32_f16 v[240:243], v[64:67], v[200:203], v[240:243]\n"
  "ds_read_b64_tr_b16 v[134:135], v27 offset:1720\n"
  "v_mfma_f32_16x16x32_f16 v[244:247], v[68:71], v[200:203], v[244:247]\n"
  "ds_read_b128 v[200:203], v26 offset:2176\n"
  "s_waitcnt lgkmcnt(14)\n"
  "v_mfma_f32_16x16x32_f16 v[236:239], v[56:59], v[204:207], v[236:239]\n"
  "buffer_load_dwordx4 v13, s[8:11], s66 offen offset:1024 lds\n"
  "v_mfma_f32_16x16x32_f16 v[240:243], v[60:63], v[204:207], v[240:243]\n"
  "ds_read_b64_tr_b16 v[136:137], v27 offset:2240\n"
  "v_mfma_f32_16x16x32_f16 v[244:247], v[64:67], v[204:207], v[244:247]\n"
  "ds_read_b128 v[204:207], v26 offset:2688\n"
  "v_mfma_f32_16x16x32_f16 v[240:243], v[56:59], v[208:211], v[240:243]\n"
  "ds_read_b64_tr_b16 v[138:139], v27 offset:2232\n"
  "v_mfma_f32_16x16x32_f16 v[244:247], v[60:63], v[208:211], v[244:247]\n"
  "buffer_load_dwordx4 v14, s[8:11], s66 offen offset:2048 lds\n"
  "s_waitcnt lgkmcnt(13)\n"
  "v_mfma_f32_16x16x32_f16 v[244:247], v[56:59], v[212:215], v[244:247]\n"
  "ds_read_b128 v[208:211], v26 offset:3200\n"
  "v_mfma_f32_16x16x32_f16 v[216:219], v[88:91], v[184:187], v[216:219]\n"
  "ds_read_b128 v[212:215], v26 offset:3712\n"
  "v_mfma_f32_16x16x32_f16 v[220:223], v[92:95], v[184:187], v[220:223]\n"
  "ds_read_b64_tr_b16 v[140:141], v27 offset:2752\n"
  "v_mfma_f32_16x16x32_f16 v[224:227], v[96:99], v[184:187], v[224:227]\n"
  "ds_read_b64_tr_b16 v[142:143], v27 offset:2744\n"
  "v_mfma_f32_16x16x32_f16 v[228:231], v[100:103], v[184:187], v[228:231]\n"
  "buffer_load_dwordx4 v15, s[8:11], s66 offen offset:3072 lds\n"
  "v_mfma_f32_16x16x32_f16 v[232:235], v[104:107], v[184:187], v[232:235]\n"
  "ds_read_b64_tr_b16 v[144:145], v27 offset:3264\n"
  "s_mov_b32 m0, s24\n"
  "v_mfma_f32_16x16x32_f16 v[236:239], v[108:111], v[184:187], v[236:239]\n"
  "ds_read_b64_tr_b16 v[146:147], v27 offset:3256\n"
  "s_add_i32 s66, s66, 0x1c000\n"
  "v_mfma_f32_16x16x32_f16 v[240:243], v[112:115], v[184:187], v[240:243]\n"
  "ds_read_b64_tr_b16 v[148:149], v27 offset:3776\n"
  "v_mfma_f32_16x16x32_f16 v[244:247], v[116:119], v[184:187], v[244:247]\n"
  "buffer_load_dwordx4 v12, s[8:11], s66 offen offset:0 lds\n"
  "v_mfma_f32_16x16x32_f16 v[220:223], v[88:91], v[188:191], v[220:223]\n"
  "ds_read_b128 v[184:187], v26 offset:64\n"
  "v_mfma_f32_16x16x32_f16 v[224:227], v[92:95], v[188:191], v[224:227]\n"
  "ds_read_b64_tr_b16 v[150:151], v27 offset:3768\n"
  "v_mfma_f32_16x16x32_f16 v[228:231], v[96:99], v[188:191], v[228:231]\n"
  "ds_read_b64_tr_b16 v[152:153], v27 offset:256\n"
  "v_mfma_f32_16x16x32_f16 v[232:235], v[100:103], v[188:191], v[232:235]\n"
  "ds_read_b64_tr_b16 v[154:155], v27 offset:248\n"
  "v_mfma_f32_16x16x32_f16 v[236:239], v[104:107], v[188:191], v[236:239]\n"
  "buffer_load_dwordx4 v13, s[8:11], s66 offen offset:1024 lds\n"
  "v_mfma_f32_16x16x32_f16 v[240:243], v[108:111], v[188:191], v[240:243]\n"
  "ds_read_b64_tr_b16 v[156:157], v27 offset:768\n"
  "v_mfma_f32_16x16x32_f16 v[244:247], v[112:115], v[188:191], v[244:247]\n"
  "ds_read_b128 v[188:191], v26 offset:576\n"
  "s_waitcnt lgkmcnt(14)\n"
  "v_mfma_f32_16x16x32_f16 v[224:227], v[88:91], v[192:195], v[224:227]\n"
  "ds_read_b64_tr_b16 v[158:159], v27 offset:760\n"
  "v_mfma_f32_16x16x32_f16 v[228:231], v[92:95], v[192:195], v[228:231]\n"
  "buffer_load_dwordx4 v14, s[8:11], s66 offen offset:2048 lds\n"
  "v_mfma_f32_16x16x32_f16 v[232:235], v[96:99], v[192:195], v[232:235]\n"
  "ds_read_b64_tr_b16 v[160:161], v27 offset:1280\n"
  "v_mfma_f32_16x16x32_f16 v[236:239], v[100:103], v[192:195], v[236:239]\n"
  "ds_read_b64_tr_b16 v[162:163], v27 offset:1272\n"
  "v_mfma_f32_16x16x32_f16 v[240:243], v[104:107], v[192:195], v[240:243]\n"
  "ds_read_b64_tr_b16 v[164:165], v27 offset:1792\n"
  "v_mfma_f32_16x16x32_f16 v[244:247], v[108:111], v[192:195], v[244:247]\n"
  "buffer_load_dwordx4 v15, s[8:11], s66 offen offset:3072 lds\n"
  "v_mfma_f32_16x16x32_f16 v[228:231], v[88:91], v[196:199], v[228:231]\n"
  "ds_read_b128 v[192:195], v26 offset:1088\n"
  "s_mov_b32 m0, s25\n"
  "v_mfma_f32_16x16x32_f16 v[232:235], v[92:95], v[196:199], v[232:235]\n"
  "ds_read_b64_tr_b16 v[166:167], v27 offset:1784\n"
  "s_add_i32 s66, s64, 0x1c0000\n"
  "v_mfma_f32_16x16x32_f16 v[236:239], v[96:99], v[196:199], v[236:239]\n"
  "ds_read_b64_tr_b16 v[168:169], v27 offset:2304\n"
  "v_mfma_f32_16x16x32_f16 v[240:243], v[100:103], v[196:199], v[240:243]\n"
  "ds_read_b64_tr_b16 v[170:171], v27 offset:2296\n"
  "v_mfma_f32_16x16x32_f16 v[244:247], v[104:107], v[196:199], v[244:247]\n"
  "buffer_load_dwordx4 v12, s[8:11], s66 offen offset:0 lds\n"
  "v_mfma_f32_16x16x32_f16 v[232:235], v[88:91], v[200:203], v[232:235]\n"
  "ds_read_b128 v[196:199], v26 offset:1600\n"
  "v_mfma_f32_16x16x32_f16 v[236:239], v[92:95], v[200:203], v[236:239]\n"
  "ds_read_b64_tr_b16 v[172:173], v27 offset:2816\n"
  "v_mfma_f32_16x16x32_f16 v[240:243], v[96:99], v[200:203], v[240:243]\n"
  "ds_read_b64_tr_b16 v[174:175], v27 offset:2808\n"
  "v_mfma_f32_16x16x32_f16 v[244:247], v[100:103], v[200:203], v[244:247]\n"
  "buffer_load_dwordx4 v13, s[8:11], s66 offen offset:1024 lds\n"
  "v_mfma_f32_16x16x32_f16 v[236:239], v[88:91], v[204:207], v[236:239]\n"
  "ds_read_b128 v[200:203], v26 offset:2112\n"
  "v_mfma_f32_16x16x32_f16 v[240:243], v[92:95], v[204:207], v[240:243]\n"
  "ds_read_b64_tr_b16 v[176:177], v27 offset:3328\n"
  "v_mfma_f32_16x16x32_f16 v[244:247], v[96:99], v[204:207], v[244:247]\n"
  "ds_read_b128 v[204:207], v26 offset:2624\n"
  "s_waitcnt lgkmcnt(14)\n"
  "v_mfma_f32_16x16x32_f16 v[240:243], v[88:91], v[208:211], v[240:243]\n"
  "ds_read_b64_tr_b16 v[178:179], v27 offset:3320\n"
  "v_mfma_f32_16x16x32_f16 v[244:247], v[92:95], v[208:211], v[244:247]\n"
  "buffer_load_dwordx4 v14, s[8:11], s66 offen offset:2048 lds\n"
  "v_mfma_f32_16x16x32_f16 v[244:247], v[88:91], v[212:215], v[244:247]\n"
  "ds_read_b128 v[208:211], v26 offset:3136\n"
  "v_mfma_f32_16x16x32_f16 v[216:219], v[120:123], v[184:187], v[216:219]\n"
  "ds_read_b128 v[212:215], v26 offset:3648\n"
  "v_mfma_f32_16x16x32_f16 v[220:223], v[124:127], v[184:187], v[220:223]\n"
  "ds_read_b64_tr_b16 v[180:181], v27 offset:3840\n"
  "v_mfma_f32_16x16x32_f16 v[224:227], v[128:131], v[184:187], v[224:227]\n"
  "buffer_load_dwordx4 v15, s[8:11], s66 offen offset:3072 lds\n"
  "v_mfma_f32_16x16x32_f16 v[228:231], v[132:135], v[184:187], v[228:231]\n"
  "ds_read_b64_tr_b16 v[182:183], v27 offset:3832\n"
  "s_mov_b32 m0, s26\n"
  "v_mfma_f32_16x16x32_f16 v[232:235], v[136:139], v[184:187], v[232:235]\n"
  "ds_read_b64_tr_b16 a[0:1], v8 offset:32768\n"
  "s_add_i32 s66, s66, 0x1c000\n"
  "v_mfma_f32_16x16x32_f16 v[236:239], v[140:143], v[184:187], v[236:239]\n"
  "ds_read_b64_tr_b16 a[2:3], v8 offset:33280\n"
  "v_mfma_f32_16x16x32_f16 v[240:243], v[144:147], v[184:187], v[240:243]\n"
  "buffer_load_dwordx4 v12, s[8:11], s66 offen offset:0 lds\n"
  "v_mfma_f32_16x16x32_f16 v[244:247], v[148:151], v[184:187], v[244:247]\n"
  "ds_read_b128 v[184:187], v26 offset:0\n"
  "v_mfma_f32_16x16x32_f16 v[220:223], v[120:123], v[188:191], v[220:223]\n"
  "ds_read_b64_tr_b16 a[4:5], v9 offset:32768\n"
  "v_mfma_f32_16x16x32_f16 v[224:227], v[124:127], v[188:191], v[224:227]\n"
  "ds_read_b64_tr_b16 a[6:7], v9 offset:33280\n"
  "v_mfma_f32_16x16x32_f16 v[228:231], v[128:131], v[188:191], v[228:231]\n"
  "ds_read_b64_tr_b16 a[8:9], v10 offset:32768\n"
  "v_mfma_f32_16x16x32_f16 v[232:235], v[132:135], v[188:191], v[232:235]\n"
  "buffer_load_dwordx4 v13, s[8:11], s66 offen offset:1024 lds\n"
  "v_mfma_f32_16x16x32_f16 v[236:239], v[136:139], v[188:191], v[236:239]\n"
  "ds_read_b64_tr_b16 a[10:11], v10 offset:33280\n"
  "v_mfma_f32_16x16x32_f16 v[240:243], v[140:143], v[188:191], v[240:243]\n"
  "ds_read_b64_tr_b16 a[12:13], v11 offset:32768\n"
  "v_mfma_f32_16x16x32_f16 v[244:247], v[144:147], v[188:191], v[244:247]\n"
  "ds_read_b128 v[188:191], v26 offset:512\n"
  "s_waitcnt lgkmcnt(14)\n"
  "v_mfma_f32_16x16x32_f16 v[224:227], v[120:123], v[192:195], v[224:227]\n"
  "buffer_load_dwordx4 v14, s[8:11], s66 offen offset:2048 lds\n"
  "v_mfma_f32_16x16x32_f16 v[228:231], v[124:127], v[192:195], v[228:231]\n"
  "ds_read_b64_tr_b16 a[14:15], v11 offset:33280\n"
  "v_mfma_f32_16x16x32_f16 v[232:235], v[128:131], v[192:195], v[232:235]\n"
  "s_waitcnt vmcnt(15)\n"
  "ds_read_b64_tr_b16 a[16:17], v8 offset:16384\n"
  "v_mfma_f32_16x16x32_f16 v[236:239], v[132:135], v[192:195], v[236:239]\n"
  "ds_read_b64_tr_b16 a[18:19], v8 offset:16896\n"
  "v_mfma_f32_16x16x32_f16 v[240:243], v[136:139], v[192:195], v[240:243]\n"
  "ds_read_b64_tr_b16 a[20:21], v9 offset:16384\n"
  "v_mfma_f32_16x16x32_f16 v[244:247], v[140:143], v[192:195], v[244:247]\n"
  "buffer_load_dwordx4 v15, s[8:11], s66 offen offset:3072 lds\n"
  "v_mfma_f32_16x16x32_f16 v[228:231], v[120:123], v[196:199], v[228:231]\n"
  "ds_read_b128 v[192:195], v26 offset:1024\n"
  "s_mov_b32 m0, s27\n"
  "v_mfma_f32_16x16x32_f16 v[232:235], v[124:127], v[196:199], v[232:235]\n"
  "ds_read_b64_tr_b16 a[22:23], v9 offset:16896\n"
  "s_add_i32 s66, s64, 0x2a0000\n"
  "v_mfma_f32_16x16x32_f16 v[236:239], v[128:131], v[196:199], v[236:239]\n"
  "ds_read_b64_tr_b16 a[24:25], v10 offset:16384\n"
  "v_mfma_f32_16x16x32_f16 v[240:243], v[132:135], v[196:199], v[240:243]\n"
  "buffer_load_dwordx4 v12, s[8:11], s66 offen offset:0 lds\n"
  "v_mfma_f32_16x16x32_f16 v[244:247], v[136:139], v[196:199], v[244:247]\n"
  "ds_read_b128 v[196:199], v26 offset:1536\n"
  "v_mfma_f32_16x16x32_f16 v[232:235], v[120:123], v[200:203], v[232:235]\n"
  "ds_read_b64_tr_b16 a[26:27], v10 offset:16896\n"
  "v_mfma_f32_16x16x32_f16 v[236:239], v[124:127], v[200:203], v[236:239]\n"
  "ds_read_b64_tr_b16 a[28:29], v11 offset:16384\n"
  "v_mfma_f32_16x16x32_f16 v[240:243], v[128:131], v[200:203], v[240:243]\n"
  "buffer_load_dwordx4 v13, s[8:11], s66 offen offset:1024 lds\n"
  "v_mfma_f32_16x16x32_f16 v[244:247], v[132:135], v[200:203], v[244:247]\n"
  "ds_read_b128 v[200:203], v26 offset:2048\n"
  "v_mfma_f32_16x16x32_f16 v[236:239], v[120:123], v[204:207], v[236:239]\n"
  "ds_read_b64_tr_b16 a[30:31], v11 offset:16896\n"
  "v_mfma_f32_16x16x32_f16 v[240:243], v[124:127], v[204:207], v[240:243]\n"
  "s_waitcnt vmcnt(14)\n"
  "ds_read_b64_tr_b16 a[32:33], v8 offset:49152\n"
  "v_mfma_f32_16x16x32_f16 v[244:247], v[128:131], v[204:207], v[244:247]\n"
  "ds_read_b128 v[204:207], v26 offset:2560\n"
  "s_waitcnt lgkmcnt(14)\n"
  "v_mfma_f32_16x16x32_f16 v[240:243], v[120:123], v[208:211], v[240:243]\n"
  "buffer_load_dwordx4 v14, s[8:11], s66 offen offset:2048 lds\n"
  "v_mfma_f32_16x16x32_f16 v[244:247], v[124:127], v[208:211], v[244:247]\n"
  "ds_read_b128 v[208:211], v26 offset:3072\n"
  "v_mfma_f32_16x16x32_f16 v[244:247], v[120:123], v[212:215], v[244:247]\n"
  "ds_read_b128 v[212:215], v26 offset:3584\n"
  "v_mfma_f32_16x16x32_f16 v[216:219], v[152:155], v[184:187], v[216:219]\n"
  "ds_read_b64_tr_b16 a[34:35], v8 offset:49664\n"
  "v_mfma_f32_16x16x32_f16 v[220:223], v[156:159], v[184:187], v[220:223]\n"
  "buffer_load_dwordx4 v15, s[8:11], s66 offen offset:3072 lds\n"
  "v_mfma_f32_16x16x32_f16 v[220:223], v[152:155], v[188:191], v[220:223]\n"
  "ds_read_b64_tr_b16 a[36:37], v9 offset:49152\n"
  "s_mov_b32 m0, s22\n"
  "v_mfma_f32_16x16x32_f16 v[224:227], v[160:163], v[184:187], v[224:227]\n"
  "ds_read_b64_tr_b16 a[38:39], v9 offset:49664\n"
  "s_add_i32 s66, s66, 0x1c000\n"
  "v_mfma_f32_16x16x32_f16 v[224:227], v[156:159], v[188:191], v[224:227]\n"
  "s_waitcnt lgkmcnt(3)\n"
  "s_barrier\n"
  "v_mfma_f32_16x16x32_f16 v[224:227], v[152:155], v[192:195], v[224:227]\n"
  "ds_write_b128 v29, v[216:219] offset:0\n"
  "v_mfma_f32_16x16x32_f16 v[228:231], v[164:167], v[184:187], v[228:231]\n"
  "buffer_load_dwordx4 v12, s[8:11], s66 offen offset:0 lds\n"
  "v_mfma_f32_16x16x32_f16 v[228:231], v[160:163], v[188:191], v[228:231]\n"
  "ds_read_b64_tr_b16 a[40:41], v10 offset:49152\n"
  "v_mfma_f32_16x16x32_f16 v[228:231], v[156:159], v[192:195], v[228:231]\n"
  "ds_write_b128 v29, v[220:223] offset:1024\n"
  "v_mfma_f32_16x16x32_f16 v[228:231], v[152:155], v[196:199], v[228:231]\n"
  "ds_read_b64_tr_b16 a[42:43], v10 offset:49664\n"
  "v_mfma_f32_16x16x32_f16 v[232:235], v[168:171], v[184:187], v[232:235]\n"
  "buffer_load_dwordx4 v13, s[8:11], s66 offen offset:1024 lds\n"
  "v_mfma_f32_16x16x32_f16 v[232:235], v[164:167], v[188:191], v[232:235]\n"
  "ds_write_b128 v29, v[224:227] offset:2048\n"
  "v_mfma_f32_16x16x32_f16 v[232:235], v[160:163], v[192:195], v[232:235]\n"
  "ds_read_b64_tr_b16 a[44:45], v11 offset:49152\n"
  "v_mfma_f32_16x16x32_f16 v[232:235], v[156:159], v[196:199], v[232:235]\n"
  "ds_read_b64_tr_b16 a[46:47], v11 offset:49664\n"
  "v_mfma_f32_16x16x32_f16 v[232:235], v[152:155], v[200:203], v[232:235]\n"
  "buffer_load_dwordx4 v14, s[8:11], s66 offen offset:2048 lds\n"
  "v_mfma_f32_16x16x32_f16 v[236:239], v[172:175], v[184:187], v[236:239]\n"
  "ds_write_b128 v29, v[228:231] offset:3072\n"
  "v_mfma_f32_16x16x32_f16 v[236:239], v[168:171], v[188:191], v[236:239]\n"
  "s_waitcnt vmcnt(15)\n"
  "ds_read_b64_tr_b16 a[48:49], v23 offset:0\n"
  "v_mfma_f32_16x16x32_f16 v[236:239], v[164:167], v[192:195], v[236:239]\n"
  "ds_read_b64_tr_b16 a[50:51], v23 offset:512\n"
  "v_mfma_f32_16x16x32_f16 v[236:239], v[160:163], v[196:199], v[236:239]\n"
  "ds_write_b128 v29, v[232:235] offset:4096\n"
  "v_mfma_f32_16x16x32_f16 v[236:239], v[156:159], v[200:203], v[236:239]\n"
  "buffer_load_dwordx4 v15, s[8:11], s66 offen offset:3072 lds\n"
  "v_mfma_f32_16x16x32_f16 v[236:239], v[152:155], v[204:207], v[236:239]\n"
  "ds_read_b64_tr_b16 a[52:53], v53 offset:0\n"
  "s_mov_b32 m0, s21\n"
  "v_mfma_f32_16x16x32_f16 v[240:243], v[176:179], v[184:187], v[240:243]\n"
  "ds_read_b64_tr_b16 a[54:55], v53 offset:512\n"
  "s_add_i32 s66, s64, 0x380000\n"
  "v_mfma_f32_16x16x32_f16 v[240:243], v[172:175], v[188:191], v[240:243]\n"
  "ds_read_b64_tr_b16 a[56:57], v54 offset:0\n"
  "v_mfma_f32_16x16x32_f16 v[240:243], v[168:171], v[192:195], v[240:243]\n"
  "buffer_load_dwordx4 v12, s[8:11], s66 offen offset:0 lds\n"
  "v_mfma_f32_16x16x32_f16 v[240:243], v[164:167], v[196:199], v[240:243]\n"
  "ds_write_b128 v29, v[236:239] offset:5120\n"
  "v_mfma_f32_16x16x32_f16 v[240:243], v[160:163], v[200:203], v[240:243]\n"
  "ds_read_b64_tr_b16 a[58:59], v54 offset:512\n"
  "v_mfma_f32_16x16x32_f16 v[240:243], v[156:159], v[204:207], v[240:243]\n"
  "ds_read_b64_tr_b16 a[60:61], v55 offset:0\n"
  "v_mfma_f32_16x16x32_f16 v[240:243], v[152:155], v[208:211], v[240:243]\n"
  "ds_read_b64_tr_b16 a[62:63], v55 offset:512\n"
  "v_mfma_f32_16x16x32_f16 v[244:247], v[180:183], v[184:187], v[244:247]\n"
  "buffer_load_dwordx4 v13, s[8:11], s66 offen offset:1024 lds\n"
  "v_mfma_f32_16x16x32_f16 v[244:247], v[176:179], v[188:191], v[244:247]\n"
  "s_waitcnt vmcnt(14)\n"
  "ds_read_b64_tr_b16 a[64:65], v23 offset:16384\n"
  "v_mfma_f32_16x16x32_f16 v[244:247], v[172:175], v[192:195], v[244:247]\n"
  "ds_read_b64_tr_b16 a[66:67], v23 offset:16896\n"
  "v_mfma_f32_16x16x32_f16 v[244:247], v[168:171], v[196:199], v[244:247]\n"
  "ds_write_b128 v29, v[240:243] offset:6144\n"
  "v_mfma_f32_16x16x32_f16 v[244:247], v[164:167], v[200:203], v[244:247]\n"
  "buffer_load_dwordx4 v14, s[8:11], s66 offen offset:2048 lds\n"
  "v_mfma_f32_16x16x32_f16 v[244:247], v[160:163], v[204:207], v[244:247]\n"
  "ds_read_b64_tr_b16 a[68:69], v53 offset:16384\n"
  "v_mfma_f32_16x16x32_f16 v[244:247], v[156:159], v[208:211], v[244:247]\n"
  "ds_read_b64_tr_b16 a[70:71], v53 offset:16896\n"
  "v_mfma_f32_16x16x32_f16 v[244:247], v[152:155], v[212:215], v[244:247]\n"
  "buffer_load_dwordx4 v15, s[8:11], s66 offen offset:3072 lds\n"
  "s_mov_b32 m0, s20\n"
  "s_add_i32 s66, s66, 0x1c000\n"
  "buffer_load_dwordx4 v12, s[8:11], s66 offen offset:0 lds\n"
  "buffer_load_dwordx4 v13, s[8:11], s66 offen offset:1024 lds\n"
  "s_nop 2\n"
  "ds_write_b128 v29, v[244:247] offset:7168\n"
  "s_waitcnt lgkmcnt(0)\n"
  "s_barrier\n"
  "ds_read_b128 v[56:59], v52 offset:0\n"
  "ds_read_b128 v[60:63], v52 offset:256\n"
  "ds_read_b128 v[64:67], v52 offset:8192\n"
  "ds_read_b128 v[68:71], v52 offset:8448\n"
  "ds_read_b128 v[72:75], v52 offset:1024\n"
  "ds_read_b128 v[76:79], v52 offset:1280\n"
  "ds_read_b128 v[80:83], v52 offset:9216\n"
  "ds_read_b128 v[84:87], v52 offset:9472\n"
  "buffer_load_dwordx4 v14, s[8:11], s66 offen offset:2048 lds\n"
  "buffer_load_dwordx4 v15, s[8:11], s66 offen offset:3072 lds\n"
  "s_waitcnt lgkmcnt(4)\n"
  "v_pk_add_f32 v[62:63], v[62:63], v[70:71]\n"
  "v_pk_add_f32 v[60:61], v[60:61], v[68:69]\n"
  "v_pk_add_f32 v[58:59], v[58:59], v[66:67]\n"
  "v_pk_add_f32 v[56:57], v[56:57], v[64:65]\n"
  "v_pk_mul_f32 v[62:63], v[62:63], v[28:29] op_sel_hi:[1,0]\n"
  "v_pk_mul_f32 v[60:61], v[60:61], v[28:29] op_sel_hi:[1,0]\n"
  "v_pk_mul_f32 v[58:59], v[58:59], v[28:29] op_sel_hi:[1,0]\n"
  "v_pk_mul_f32 v[56:57], v[56:57], v[28:29] op_sel_hi:[1,0]\n"
  "v_cvt_pk_bf16_f32 v251, v62, v63\n"
  "v_cvt_pk_bf16_f32 v250, v60, v61\n"
  "v_cvt_pk_bf16_f32 v249, v58, v59\n"
  "v_cvt_pk_bf16_f32 v248, v56, v57\n"
  "v_or_b32 v248, v4, v248\n"
  "v_or_b32 v249, v5, v249\n"
  "v_or_b32 v250, v6, v250\n"
  "v_or_b32_e64 v251, v7, v251\n"
  "buffer_store_dwordx4 v[248:251], v22, s[4:7], 0 offen sc1\n"
  "s_waitcnt lgkmcnt(0)\n"
  "v_pk_add_f32 v[78:79], v[78:79], v[86:87]\n"
  "v_pk_add_f32 v[76:77], v[76:77], v[84:85]\n"
  "v_pk_add_f32 v[74:75], v[74:75], v[82:83]\n"
  "v_pk_add_f32 v[72:73], v[72:73], v[80:81]\n"
  "v_pk_mul_f32 v[78:79], v[78:79], v[28:29] op_sel_hi:[1,0]\n"
  "v_pk_mul_f32 v[76:77], v[76:77], v[28:29] op_sel_hi:[1,0]\n"
  "v_pk_mul_f32 v[74:75], v[74:75], v[28:29] op_sel_hi:[1,0]\n"
  "v_pk_mul_f32 v[72:73], v[72:73], v[28:29] op_sel_hi:[1,0]\n"
  "v_cvt_pk_bf16_f32 v255, v78, v79\n"
  "v_cvt_pk_bf16_f32 v254, v76, v77\n"
  "v_cvt_pk_bf16_f32 v253, v74, v75\n"
  "v_cvt_pk_bf16_f32 v252, v72, v73\n"
  "v_or_b32 v252, v4, v252\n"
  "v_or_b32 v253, v5, v253\n"
  "v_or_b32 v254, v6, v254\n"
  "v_or_b32 v255, v7, v255\n"
  "buffer_store_dwordx4 v[252:255], v22, s[4:7], s77 offen sc1\n"
  "s_mul_i32 s86, s52, 1984\n"
  "v_add_u32 v24, s86, v24\n"
  "s_mov_b32 s33, 0x60006000\n"
  "v_bfe_u32 v35, v0, 4, 2\n"
  "v_lshlrev_b32 v35, 3, v35\n"
  "s_lshl_b32 s88, s60, 7\n"
  "s_lshl_b32 s89, s56, 6\n"
  "s_add_i32 s89, s88, s89\n"
  "buffer_load_dwordx2 v[248:249], v35, s[36:39], s89 offen\n"
  "buffer_load_dwordx2 v[250:251], v35, s[36:39], s89 offen offset:32\n"
  "s_lshl_b32 s88, s53, 6\n"
  "v_or_b32 v34, s88, v25\n"
  "v_lshrrev_b32 v34, 2, v34\n"
  "global_load_dword v252, v34, s[34:35]\n"
  "v_and_b32 v52, 0xf8, v0\n"
  "v_bfe_u32 v34, v0, 1, 2\n"
  "v_or_b32 v52, v52, v34\n"
  "v_lshlrev_b32 v52, 7, v52\n"
  "v_and_b32 v34, 1, v0\n"
  "v_lshl_or_b32 v52, v34, 4, v52\n"
  "s_lshr_b32 s86, s53, 3\n"
  "s_and_b32 s87, s53, 1\n"
  "s_lshl1_add_u32 s86, s86, s87\n"
  "s_mul_i32 s86, s86, 32768\n"
  "s_and_b32 s87, s53, 6\n"
  "s_lshl4_add_u32 s86, s87, s86\n"
  "s_add_i32 s86, s86, 0x290000\n"
  "v_add_u32_e64 v52, s86, v52\n"
  "ds_read_b64_tr_b16 a[72:73], v54 offset:16384\n"
  "ds_read_b64_tr_b16 a[74:75], v54 offset:16896\n"
  "ds_read_b64_tr_b16 a[76:77], v55 offset:16384\n"
  "ds_read_b64_tr_b16 a[78:79], v55 offset:16896\n"
  "s_waitcnt vmcnt(21)\n"
  "ds_read_b64_tr_b16 a[80:81], v23 offset:32768\n"
  "ds_read_b64_tr_b16 a[82:83], v23 offset:33280\n"
  "ds_read_b64_tr_b16 a[84:85], v53 offset:32768\n"
  "ds_read_b64_tr_b16 a[86:87], v53 offset:33280\n"
  "s_mov_b32 m0, s23\n"
  "s_add_i32 s66, s64, 0x460000\n"
  "buffer_load_dwordx4 v12, s[8:11], s66 offen offset:0 lds\n"
  "ds_read_b64_tr_b16 a[88:89], v54 offset:32768\n"
  "ds_read_b64_tr_b16 a[90:91], v54 offset:33280\n"
  "ds_read_b64_tr_b16 a[92:93], v55 offset:32768\n"
  "ds_read_b64_tr_b16 a[94:95], v55 offset:33280\n"
  "s_waitcnt vmcnt(18)\n"
  "ds_read_b64_tr_b16 a[96:97], v23 offset:49152\n"
  "ds_read_b64_tr_b16 a[98:99], v23 offset:49664\n"
  "ds_read_b64_tr_b16 a[100:101], v53 offset:49152\n"
  "ds_read_b64_tr_b16 a[102:103], v53 offset:49664\n"
  "buffer_load_dwordx4 v13, s[8:11], s66 offen offset:1024 lds\n"
  "ds_read_b64_tr_b16 a[104:105], v54 offset:49152\n"
  "ds_read_b64_tr_b16 a[106:107], v54 offset:49664\n"
  "ds_read_b64_tr_b16 a[108:109], v55 offset:49152\n"
  "ds_read_b64_tr_b16 a[110:111], v55 offset:49664\n"
  "s_waitcnt vmcnt(15)\n"
  "ds_read_b64_tr_b16 a[112:113], v8 offset:32768\n"
  "ds_read_b64_tr_b16 a[114:115], v8 offset:33280\n"
  "ds_read_b64_tr_b16 a[116:117], v9 offset:32768\n"
  "ds_read_b64_tr_b16 a[118:119], v9 offset:33280\n"
  "buffer_load_dwordx4 v14, s[8:11], s66 offen offset:2048 lds\n"
  "ds_read_b64_tr_b16 a[120:121], v10 offset:32768\n"
  "ds_read_b64_tr_b16 a[122:123], v10 offset:33280\n"
  "ds_read_b64_tr_b16 a[124:125], v11 offset:32768\n"
  "ds_read_b64_tr_b16 a[126:127], v11 offset:33280\n"
  "s_waitcnt vmcnt(12)\n"
  "ds_read_b64_tr_b16 a[128:129], v8 offset:16384\n"
  "ds_read_b64_tr_b16 a[130:131], v8 offset:16896\n"
  "ds_read_b64_tr_b16 a[132:133], v9 offset:16384\n"
  "ds_read_b64_tr_b16 a[134:135], v9 offset:16896\n"
  "buffer_load_dwordx4 v15, s[8:11], s66 offen offset:3072 lds\n"
  "ds_read_b64_tr_b16 a[136:137], v10 offset:16384\n"
  "ds_read_b64_tr_b16 a[138:139], v10 offset:16896\n"
  "ds_read_b64_tr_b16 a[140:141], v11 offset:16384\n"
  "ds_read_b64_tr_b16 a[142:143], v11 offset:16896\n"
  "s_waitcnt vmcnt(9)\n"
  "ds_read_b64_tr_b16 a[144:145], v8 offset:0\n"
  "ds_read_b64_tr_b16 a[146:147], v8 offset:512\n"
  "ds_read_b64_tr_b16 a[148:149], v9 offset:0\n"
  "ds_read_b64_tr_b16 a[150:151], v9 offset:512\n"
  "s_mov_b32 m0, s24\n"
  "s_add_i32 s66, s66, 0x1c000\n"
  "buffer_load_dwordx4 v12, s[8:11], s66 offen offset:0 lds\n"
  "ds_read_b64_tr_b16 a[152:153], v10 offset:0\n"
  "ds_read_b64_tr_b16 a[154:155], v10 offset:512\n"
  "ds_read_b64_tr_b16 a[156:157], v11 offset:0\n"
  "ds_read_b64_tr_b16 a[158:159], v11 offset:512\n"
  "buffer_load_dwordx4 v13, s[8:11], s66 offen offset:1024 lds\n"
  "buffer_load_dwordx4 v14, s[8:11], s66 offen offset:2048 lds\n"
  "buffer_load_dwordx4 v15, s[8:11], s66 offen offset:3072 lds\n"
  "s_mov_b32 m0, s25\n"
  "s_add_i32 s66, s64, 0x540000\n"
  "s_waitcnt lgkmcnt(14)\n"
  "buffer_load_dwordx4 v12, s[8:11], s66 offen offset:0 lds\n"
  "buffer_load_dwordx4 v13, s[8:11], s66 offen offset:1024 lds\n"
  "buffer_load_dwordx4 v14, s[8:11], s66 offen offset:2048 lds\n"
  "buffer_load_dwordx4 v15, s[8:11], s66 offen offset:3072 lds\n"
  "s_waitcnt vmcnt(15)\n"
  "s_sleep 8\n"
  "buffer_load_dwordx4 v[64:67], v52, s[4:7], 0 offen sc1\n"
  "buffer_load_dwordx4 v[68:71], v52, s[4:7], 0 offen offset:512 sc1\n"
  "v_and_b32 v34, 0x1f0, v1\n"
  "s_mov_b32 m0, s26\n"
  "s_add_i32 s66, s66, 0x1c000\n"
  "buffer_load_dwordx4 v12, s[8:11], s66 offen offset:0 lds\n"
  "s_mov_b32 s88, 262144\n"
  "v_mad_u32_u24 v3, v3, s88, v34\n"
  "s_lshl_b32 s88, s53, 9\n"
  "v_add_u32 v3, s88, v3\n"
  "s_lshl2_add_u32 s71, s60, s52\n"
  "s_lshl_b32 s71, s71, 10\n"
  "s_mul_i32 s88, s59, 229376\n"
  "s_add_i32 s71, s71, s88\n"
  "s_add_i32 s71, s71, 0xa10000\n"
  "v_and_b32 v156, 15, v0\n"
  "buffer_load_dwordx4 v13, s[8:11], s66 offen offset:1024 lds\n"
  "s_lshl_b32 s84, s57, 10\n"
  "v_or_b32_e64 v172, s84, v1\n"
  "v_xor_b32 v173, 0x400, v172\n"
  "s_lshl_b32 s84, s57, 8\n"
  "v_lshl_or_b32 v168, v156, 4, s84\n"
  "v_lshl_or_b32 v168, v35, 6, v168\n"
  "v_xor_b32 v169, 0x100, v168\n"
  "v_add_u32 v170, 131072, v168\n"
  "v_add_u32 v171, 131072, v169\n"
  "v_add_u32 v174, 114688, v172\n"
  "buffer_load_dwordx4 v14, s[8:11], s66 offen offset:2048 lds\n"
  "v_add_u32 v175, 114688, v173\n"
  "v_sub_u32 v156, v156, v35\n"
  "v_mov_b32 v157, 0x3f80\n"
  "v_mov_b32 v158, 1.0\n"
  "v_and_b32 v159, 16, v0\n"
  "v_lshl_or_b32 v159, v33, 7, v159\n"
  "v_xor_b32 v159, v159, v1\n"
  "v_bfe_u32 v29, v0, 2, 4\n"
  "buffer_load_dwordx4 v15, s[8:11], s66 offen offset:3072 lds\n"
  "v_and_b32 v27, 2, v0\n"
  "v_lshlrev_b32 v27, 2, v27\n"
  "v_and_or_b32 v27, v0, 1, v27\n"
  "v_xor_b32 v27, v27, v29\n"
  "v_lshlrev_b32 v27, 4, v27\n"
  "v_lshl_or_b32 v27, v25, 4, v27\n"
  "v_lshl_or_b32 v29, v29, 10, v25\n"
  "s_mul_i32 s84, s52, 8192\n"
  "v_add_u32 v26, s84, v159\n"
  "v_add_u32 v27, s84, v27\n"
  "s_mul_i32 s72, s59, 262144\n"
  "s_lshl_b32 s84, s52, 12\n"
  "s_add_i32 s72, s72, s84\n"
  "s_add_i32 s72, s72, 0x510000\n"
  "s_mul_i32 s73, s61, 229376\n"
  "s_mul_i32 s84, s52, 4096\n"
  "s_add_i32 s73, s73, s84\n"
  "s_add_i32 s73, s73, 0xa10000\n"
  "s_lshl1_add_u32 s74, s62, s56\n"
  "s_lshl_b32 s50, s74, 9\n"
  "s_add_i32 s50, s50, 0x790000\n"
  "s_lshl_b32 s51, s53, 7\n"
  "s_add_i32 s51, s51, 0x790000\n"
  "s_cmp_eq_u32 s52, 0\n"
  "s_cselect_b32 s51, s51, 0x7fffffff\n"
  "s_lshl_b32 s74, s74, 11\n"
  "s_mul_i32 s84, s61, 262144\n"
  "s_add_i32 s74, s74, s84\n"
  "s_add_i32 s74, s74, 0x510000\n"
  "s_lshl1_add_u32 s75, s62, s56\n"
  "s_lshl_b32 s75, s75, 9\n"
  "s_lshl1_add_u32 s75, s61, s75\n"
  "s_add_i32 s75, s75, s57\n"
  "s_lshl_b32 s75, s75, 6\n"
  "s_add_i32 s58, s72, 0x4000\n"
  "s_add_i32 s67, s72, 0x8000\n"
  "s_add_i32 s68, s72, 0xc000\n"
  "s_add_i32 s78, s72, 0x10000\n"
  "s_add_i32 s79, s72, 0x14000\n"
  "s_add_i32 s69, s72, 0x18000\n"
  "s_add_i32 s77, s72, 0x1c000\n"
  "s_add_i32 s0, s73, 0x4000\n"
  "s_add_i32 s1, s73, 0x8000\n"
  "s_add_i32 s3, s73, 0xc000\n"
  "s_add_i32 s94, s73, 0x10000\n"
  "s_add_i32 s95, s73, 0x14000\n"
  "s_add_i32 s30, s73, 0x18000\n"
  "v_and_b32 v156, 15, v156\n"
  "v_cmp_eq_u32 vcc, 0, v156\n"
  "v_cmp_eq_u32 s[84:85], 1, v156\n"
  "s_nop 0\n"
  "v_cndmask_b32 v160, 0, v157, vcc\n"
  "v_cndmask_b32 v160, v160, v158, s[84:85]\n"
  "v_cmp_eq_u32 vcc, 2, v156\n"
  "v_cmp_eq_u32 s[84:85], 3, v156\n"
  "s_nop 0\n"
  "v_cndmask_b32 v161, 0, v157, vcc\n"
  "v_cndmask_b32 v161, v161, v158, s[84:85]\n"
  "v_cmp_eq_u32 vcc, 4, v156\n"
  "v_cmp_eq_u32 s[84:85], 5, v156\n"
  "s_nop 0\n"
  "v_cndmask_b32 v162, 0, v157, vcc\n"
  "v_cndmask_b32 v162, v162, v158, s[84:85]\n"
  "v_cmp_eq_u32 vcc, 6, v156\n"
  "v_cmp_eq_u32 s[84:85], 7, v156\n"
  "s_nop 0\n"
  "v_cndmask_b32 v163, 0, v157, vcc\n"
  "v_cndmask_b32 v163, v163, v158, s[84:85]\n"
  "v_cmp_eq_u32_e64 vcc, 0, v33\n"
  "s_nop 1\n"
  "v_cndmask_b32 v164, v160, 0, vcc\n"
  "v_cndmask_b32 v165, v161, 0, vcc\n"
  "v_cndmask_b32 v166, v162, 0, vcc\n"
  "v_cndmask_b32 v167, v163, 0, vcc\n"
  "v_cndmask_b32 v160, 0, v160, vcc\n"
  "v_cndmask_b32 v161, 0, v161, vcc\n"
  "v_cndmask_b32 v162, 0, v162, vcc\n"
  "v_cndmask_b32 v163, 0, v163, vcc\n"
  "v_and_b32 v156, 2, v0\n"
  "v_cmp_eq_u32 vcc, 0, v156\n"
  "s_nop 1\n"
  "v_cndmask_b32 v34, v5, v4, vcc\n"
  "v_cndmask_b32 v35, v7, v6, vcc\n"
  "s_xor_b32 s84, s52, 1\n"
  "s_mul_i32 s84, s84, 8192\n"
  "s_add_i32 s84, s84, 0\n"
  "v_add_u32 v20, s84, v159\n"
  "s_xor_b32 s84, s52, 2\n"
  "s_mul_i32 s84, s84, 8192\n"
  "s_add_i32 s84, s84, 2048\n"
  "v_add_u32 v21, s84, v159\n"
  "s_xor_b32 s84, s52, 3\n"
  "s_mul_i32 s84, s84, 8192\n"
  "s_add_i32 s84, s84, 4096\n"
  "v_add_u32 v22, s84, v159\n"
  "s_movk_i32 s80, 0x6b80\n"
  "s_mov_b32 s81, 0x6b800000\n"
  "v_mov_b32 v56, s83\n"
  "s_mov_b32 s32, 0x6b800000\n"
  "s_mov_b32 s34, 0x6b800000\n"
  "v_mov_b32 v28, 0xc0135761\n"
  "s_mov_b32 s86, 0x13800000\n"
  "s_mov_b32 s87, 0x13800000\n"
  "s_mov_b32 s70, 0x510000\n"
  "s_mov_b32 s84, 0x530000\n"
  "s_waitcnt vmcnt(4)\n"
  "v_xor_b32 v68, v4, v68\n"
  "v_xor_b32 v69, v5, v69\n"
  "v_xor_b32 v70, v6, v70\n"
  "v_xor_b32 v71, v7, v71\n"
  "v_xor_b32 v64, v4, v64\n"
  "v_xor_b32 v65, v5, v65\n"
  "v_xor_b32 v66, v6, v66\n"
  "v_xor_b32_e64 v67, v7, v67\n"
  "v_or3_b32 v58, v68, v69, v70\n"
  "v_or3_b32 v58, v58, v71, v64\n"
  "v_or3_b32 v58, v58, v65, v66\n"
  "v_bitop3_b32 v58, v58, s33, v67 bitop3:0xc8\n"
  "v_cmp_ne_u32 vcc, 0, v58\n"
  "s_cbranch_vccnz Lslow1\n"
  "LbackYa:\n"
  "v_dot2c_f32_bf16 v36, s80, v64\n"
  "s_mov_b32 m0, s27\n"
  "s_add_i32 s66, s64, 0x620000\n"
  "buffer_load_dwordx4 v12, s[8:11], s66 offen offset:0 lds\n"
  "v_dot2c_f32_bf16 v37, s81, v64\n"
  "v_dot2c_f32_bf16 v44, s80, v68\n"
  "v_dot2c_f32_bf16 v45, s81, v68\n"
  "v_dot2c_f32_bf16 v38, s80, v65\n"
  "v_dot2c_f32_bf16 v39, s81, v65\n"
  "v_dot2c_f32_bf16 v46, s80, v69\n"
  "v_dot2c_f32_bf16 v47, s81, v69\n"
  "v_dot2c_f32_bf16 v40, s80, v66\n"
  "buffer_load_dwordx4 v13, s[8:11], s66 offen offset:1024 lds\n"
  "v_dot2c_f32_bf16 v41, s81, v66\n"
  "v_dot2c_f32_bf16 v48, s80, v70\n"
  "v_dot2c_f32_bf16 v49, s81, v70\n"
  "v_dot2c_f32_bf16 v42, s80, v67\n"
  "v_dot2c_f32_bf16 v43, s81, v67\n"
  "v_dot2c_f32_bf16 v50, s80, v71\n"
  "v_dot2c_f32_bf16 v51, s81, v71\n"
  "v_pk_mul_f32 v[192:193], v[36:37], v[36:37]\n"
  "buffer_load_dwordx4 v14, s[8:11], s66 offen offset:2048 lds\n"
  "v_pk_mul_f32 v[194:195], v[38:39], v[38:39]\n"
  "v_pk_mul_f32 v[196:197], v[40:41], v[40:41]\n"
  "v_pk_mul_f32 v[198:199], v[42:43], v[42:43]\n"
  "v_pk_add_f32 v[184:185], v[36:37], v[44:45]\n"
  "v_pk_add_f32 v[186:187], v[38:39], v[46:47]\n"
  "v_pk_add_f32 v[188:189], v[40:41], v[48:49]\n"
  "v_pk_add_f32 v[190:191], v[42:43], v[50:51]\n"
  "v_pk_fma_f32 v[192:193], v[44:45], v[44:45], v[192:193]\n"
  "buffer_load_dwordx4 v15, s[8:11], s66 offen offset:3072 lds\n"
  "v_pk_fma_f32 v[194:195], v[46:47], v[46:47], v[194:195]\n"
  "v_pk_fma_f32 v[196:197], v[48:49], v[48:49], v[196:197]\n"
  "v_pk_fma_f32 v[198:199], v[50:51], v[50:51], v[198:199]\n"
  "v_permlane32_swap_b32 v184, v192\n"
  "v_permlane32_swap_b32 v185, v193\n"
  "v_permlane32_swap_b32 v186, v194\n"
  "v_permlane32_swap_b32 v187, v195\n"
  "v_permlane32_swap_b32 v188, v196\n"
  "s_mov_b32 m0, s22\n"
  "s_add_i32 s66, s66, 0x1c000\n"
  "buffer_load_dwordx4 v12, s[8:11], s66 offen offset:0 lds\n"
  "v_permlane32_swap_b32 v189, v197\n"
  "v_permlane32_swap_b32 v190, v198\n"
  "v_permlane32_swap_b32 v191, v199\n"
  "v_pk_add_f32 v[184:185], v[184:185], v[192:193]\n"
  "v_pk_add_f32 v[188:189], v[188:189], v[196:197]\n"
  "v_pk_add_f32 v[186:187], v[186:187], v[194:195]\n"
  "v_pk_add_f32 v[190:191], v[190:191], v[198:199]\n"
  "v_permlane16_swap_b32 v184, v188\n"
  "buffer_load_dwordx4 v13, s[8:11], s66 offen offset:1024 lds\n"
  "v_permlane16_swap_b32 v185, v189\n"
  "v_permlane16_swap_b32 v186, v190\n"
  "v_permlane16_swap_b32 v187, v191\n"
  "v_pk_add_f32 v[184:185], v[184:185], v[188:189]\n"
  "v_pk_add_f32 v[186:187], v[186:187], v[190:191]\n"
  "v_lshlrev_b32_e64 v140, 16, v252\n"
  "v_add_f32_dpp v192, v184, v184 row_ror:8 row_mask:0xf bank_mask:0x3\n"
  "v_add_f32_dpp v193, v185, v185 row_ror:8 row_mask:0xf bank_mask:0x3\n"
  "buffer_load_dwordx4 v14, s[8:11], s66 offen offset:2048 lds\n"
  "v_and_b32 v141, 0xffff0000, v252\n"
  "v_add_f32_dpp v192, v186, v186 row_ror:8 row_mask:0xf bank_mask:0xc\n"
  "v_add_f32_dpp v193, v187, v187 row_ror:8 row_mask:0xf bank_mask:0xc\n"
  "v_lshlrev_b32 v142, 1, v25\n"
  "v_add_f32_dpp v194, v192, v192 row_half_mirror row_mask:0xf bank_mask:0x5\n"
  "v_add_f32 v140, v32, v140\n"
  "v_add_f32 v141, v31, v141\n"
  "v_add_f32_dpp v194, v193, v193 row_half_mirror row_mask:0xf bank_mask:0xa\n"
  "buffer_load_dwordx4 v15, s[8:11], s66 offen offset:3072 lds\n"
  "v_mov_b32 v145, s48\n"
  "v_mov_b32 v147, s49\n"
  "v_add_f32_dpp v195, v194, v194 quad_perm:[1,0,3,2] row_mask:0xf bank_mask:0xf\n"
  "v_mov_b32 v149, s48\n"
  "v_mov_b32 v151, s49\n"
  "v_add_f32_dpp v194, v195, v195 quad_perm:[2,3,0,1] row_mask:0xf bank_mask:0xf\n"
  "ds_write_b32 v24, v194 offset:0\n"
  "v_lshlrev_b32 v72, 16, v248\n"
  "s_mov_b32 m0, s21\n"
  "s_add_i32 s66, s65, 0x0\n"
  "s_waitcnt lgkmcnt(1)\n"
  "buffer_load_dwordx4 v16, s[12:15], s66 offen offset:0 lds\n"
  "v_and_b32 v73, 0xffff0000, v248\n"
  "v_lshlrev_b32 v74, 16, v249\n"
  "v_and_b32 v75, 0xffff0000, v249\n"
  "v_lshlrev_b32 v76, 16, v250\n"
  "v_and_b32 v77, 0xffff0000, v250\n"
  "v_lshlrev_b32 v78, 16, v251\n"
  "s_waitcnt lgkmcnt(0)\n"
  "s_barrier\n"
  "buffer_load_dwordx4 v17, s[12:15], s66 offen offset:1024 lds\n"
  "ds_read_b128 v[200:203], v25 offset:0\n"
  "ds_read_b128 v[204:207], v25 offset:2048\n"
  "ds_read_b128 v[208:211], v25 offset:4096\n"
  "ds_read_b128 v[212:215], v25 offset:6144\n"
  "v_and_b32 v79, 0xffff0000, v251\n"
  "s_waitcnt lgkmcnt(0)\n"
  "v_pk_add_f32 v[200:201], v[200:201], v[204:205]\n"
  "v_pk_add_f32 v[202:203], v[202:203], v[206:207]\n"
  "buffer_load_dwordx4 v18, s[12:15], s66 offen offset:2048 lds\n"
  "v_pk_add_f32 v[208:209], v[208:209], v[212:213]\n"
  "v_pk_add_f32 v[210:211], v[210:211], v[214:215]\n"
  "s_nop 0\n"
  "v_pk_add_f32 v[202:203], v[202:203], v[210:211]\n"
  "v_pk_add_f32 v[200:201], v[200:201], v[208:209]\n"
  "v_pk_mul_f32 v[202:203], v[202:203], s[82:83] op_sel_hi:[1,0]\n"
  "v_pk_mul_f32 v[136:137], v[200:201], s[82:83] op_sel_hi:[1,0]\n"
  "s_nop 0\n"
  "v_pk_fma_f32 v[202:203], v[136:137], v[136:137], v[202:203] neg_lo:[1,0,0] neg_hi:[1,0,0]\n"
  "s_nop 0\n"
  "v_max_f32 v202, 0, v202\n"
  "buffer_load_dwordx4 v19, s[12:15], s66 offen offset:3072 lds\n"
  "v_max_f32 v203, 0, v203\n"
  "v_pk_add_f32 v[204:205], v[202:203], v[56:57] op_sel_hi:[1,0]\n"
  "s_nop 0\n"
  "v_pk_fma_f32 v[138:139], v[204:205], v[56:57], v[202:203] op_sel_hi:[1,0,1]\n"
  "s_nop 0\n"
  "v_rsq_f32 v202, v138\n"
  "v_rsq_f32 v203, v139\n"
  "s_nop 0\n"
  "v_pk_mul_f32 v[202:203], v[202:203], s[86:87] op_sel_hi:[1,0]\n"
  "s_nop 0\n"
  "v_pk_mul_f32 v[200:201], v[136:137], v[202:203] neg_lo:[1,0] neg_hi:[1,0]\n"
  "v_mov_b64_dpp v[216:217], v[202:203] row_newbcast:0 row_mask:0xf bank_mask:0xf\n"
  "v_mov_b64_dpp v[218:219], v[202:203] row_newbcast:1 row_mask:0xf bank_mask:0xf\n"
  "v_mov_b64_dpp v[220:221], v[202:203] row_newbcast:2 row_mask:0xf bank_mask:0xf\n"
  "v_mov_b64_dpp v[222:223], v[202:203] row_newbcast:3 row_mask:0xf bank_mask:0xf\n"
  "v_mov_b64_dpp v[224:225], v[200:201] row_newbcast:0 row_mask:0xf bank_mask:0xf\n"
  "v_mov_b64_dpp v[226:227], v[200:201] row_newbcast:1 row_mask:0xf bank_mask:0xf\n"
  "v_mov_b64_dpp v[228:229], v[200:201] row_newbcast:2 row_mask:0xf bank_mask:0xf\n"
  "v_mov_b64_dpp v[230:231], v[200:201] row_newbcast:3 row_mask:0xf bank_mask:0xf\n"
  "v_pk_fma_f32 v[200:201], v[36:37], v[216:217], v[224:225]\n"
  "v_pk_fma_f32 v[202:203], v[38:39], v[218:219], v[226:227]\n"
  "v_pk_fma_f32 v[204:205], v[40:41], v[220:221], v[228:229]\n"
  "v_pk_fma_f32 v[206:207], v[42:43], v[222:223], v[230:231]\n"
  "v_cvt_pk_bf16_f32 v184, v200, v201\n"
  "v_cvt_pk_bf16_f32 v185, v202, v203\n"
  "v_cvt_pk_bf16_f32 v186, v204, v205\n"
  "v_cvt_pk_bf16_f32 v187, v206, v207\n"
  "v_or_b32 v184, v34, v184\n"
  "v_or_b32 v186, v35, v186\n"
  "buffer_store_dwordx4 v[184:187], v3, s[4:7], s70 offen sc1\n"
  "v_pk_fma_f32 v[208:209], v[44:45], v[216:217], v[224:225]\n"
  "v_pk_fma_f32 v[210:211], v[46:47], v[218:219], v[226:227]\n"
  "v_pk_fma_f32 v[212:213], v[48:49], v[220:221], v[228:229]\n"
  "v_pk_fma_f32 v[214:215], v[50:51], v[222:223], v[230:231]\n"
  "v_cvt_pk_bf16_f32 v188, v208, v209\n"
  "v_cvt_pk_bf16_f32 v189, v210, v211\n"
  "v_cvt_pk_bf16_f32 v190, v212, v213\n"
  "v_cvt_pk_bf16_f32 v191, v214, v215\n"
  "v_or_b32 v188, v34, v188\n"
  "v_or_b32 v190, v35, v190\n"
  "buffer_store_dwordx4 v[188:191], v3, s[4:7], s84 offen sc1\n"
  "v_sqrt_f32 v144, v138\n"
  "v_sqrt_f32 v148, v139\n"
  "v_pk_add_f32 v[136:137], v[136:137], v[140:141]\n"
  "v_and_b32 v32, 0xf0, v1\n"
  "v_mul_f32 v146, s87, v136\n"
  "v_mul_f32 v150, s87, v137\n"
  "s_mov_b64 exec, 15\n"
  "buffer_store_dwordx4 v[144:147], v142, s[4:7], s51 offen sc1\n"
  "buffer_store_dwordx4 v[148:151], v142, s[4:7], s51 offen offset:16 sc1\n"
  "s_mov_b64 exec, -1\n"
  "s_mov_b32 s82, 0xbdd2d3e8\n"
  "v_mov_b64 v[44:45], v[72:73]\n"
  "v_mov_b64 v[46:47], v[74:75]\n"
  "v_mov_b64 v[48:49], v[76:77]\n"
  "v_mov_b64 v[50:51], v[78:79]\n"
  "ds_read_b64_tr_b16 a[160:161], v8 offset:49152\n"
  "ds_read_b64_tr_b16 a[162:163], v8 offset:49664\n"
  "ds_read_b64_tr_b16 a[164:165], v9 offset:49152\n"
  "ds_read_b64_tr_b16 a[166:167], v9 offset:49664\n"
  "ds_read_b64_tr_b16 a[168:169], v10 offset:49152\n"
  "ds_read_b64_tr_b16 a[170:171], v10 offset:49664\n"
  "ds_read_b64_tr_b16 a[172:173], v11 offset:49152\n"
  "ds_read_b64_tr_b16 a[174:175], v11 offset:49664\n"
  "s_mov_b32 m0, s20\n"
  "s_add_i32 s66, s66, 0x20000\n"
  "buffer_load_dwordx4 v16, s[12:15], s66 offen offset:0 lds\n"
  "ds_read_b64_tr_b16 a[176:177], v23 offset:0\n"
  "ds_read_b64_tr_b16 a[178:179], v23 offset:512\n"
  "ds_read_b64_tr_b16 a[180:181], v53 offset:0\n"
  "ds_read_b64_tr_b16 a[182:183], v53 offset:512\n"
  "ds_read_b64_tr_b16 a[184:185], v54 offset:0\n"
  "ds_read_b64_tr_b16 a[186:187], v54 offset:512\n"
  "ds_read_b64_tr_b16 a[188:189], v55 offset:0\n"
  "ds_read_b64_tr_b16 a[190:191], v55 offset:512\n"
  "buffer_load_dwordx4 v17, s[12:15], s66 offen offset:1024 lds\n"
  "ds_read_b64_tr_b16 a[192:193], v23 offset:16384\n"
  "ds_read_b64_tr_b16 a[194:195], v23 offset:16896\n"
  "ds_read_b64_tr_b16 a[196:197], v53 offset:16384\n"
  "ds_read_b64_tr_b16 a[198:199], v53 offset:16896\n"
  "ds_read_b64_tr_b16 a[200:201], v54 offset:16384\n"
  "ds_read_b64_tr_b16 a[202:203], v54 offset:16896\n"
  "ds_read_b64_tr_b16 a[204:205], v55 offset:16384\n"
  "ds_read_b64_tr_b16 a[206:207], v55 offset:16896\n"
  "buffer_load_dwordx4 v18, s[12:15], s66 offen offset:2048 lds\n"
  "s_waitcnt vmcnt(19)\n"
  "ds_read_b64_tr_b16 a[208:209], v23 offset:32768\n"
  "ds_read_b64_tr_b16 a[210:211], v23 offset:33280\n"
  "ds_read_b64_tr_b16 a[212:213], v53 offset:32768\n"
  "ds_read_b64_tr_b16 a[214:215], v53 offset:33280\n"
  "ds_read_b64_tr_b16 a[216:217], v54 offset:32768\n"
  "ds_read_b64_tr_b16 a[218:219], v54 offset:33280\n"
  "ds_read_b64_tr_b16 a[220:221], v55 offset:32768\n"
  "ds_read_b64_tr_b16 a[222:223], v55 offset:33280\n"
  "buffer_load_dwordx4 v19, s[12:15], s66 offen offset:3072 lds\n"
  "s_waitcnt vmcnt(16)\n"
  "ds_read_b64_tr_b16 a[224:225], v23 offset:49152\n"
  "ds_read_b64_tr_b16 a[226:227], v23 offset:49664\n"
  "ds_read_b64_tr_b16 a[228:229], v53 offset:49152\n"
  "ds_read_b64_tr_b16 a[230:231], v53 offset:49664\n"
  "ds_read_b64_tr_b16 a[232:233], v54 offset:49152\n"
  "ds_read_b64_tr_b16 a[234:235], v54 offset:49664\n"
  "ds_read_b64_tr_b16 a[236:237], v55 offset:49152\n"
  "ds_read_b64_tr_b16 a[238:239], v55 offset:49664\n"
  "s_mov_b32 m0, s23\n"
  "s_add_i32 s66, s65, 0x100000\n"
  "s_waitcnt lgkmcnt(14)\n"
  "buffer_load_dwordx4 v16, s[12:15], s66 offen offset:0 lds\n"
  "s_waitcnt vmcnt(13)\n"
  "ds_read_b64_tr_b16 a[240:241], v8 offset:32768\n"
  "ds_read_b64_tr_b16 a[242:243], v8 offset:33280\n"
  "ds_read_b64_tr_b16 a[244:245], v9 offset:32768\n"
  "ds_read_b64_tr_b16 a[246:247], v9 offset:33280\n"
  "ds_read_b64_tr_b16 a[248:249], v10 offset:32768\n"
  "ds_read_b64_tr_b16 a[250:251], v10 offset:33280\n"
  "ds_read_b64_tr_b16 a[252:253], v11 offset:32768\n"
  "ds_read_b64_tr_b16 a[254:255], v11 offset:33280\n"
  "buffer_load_dwordx4 v17, s[12:15], s66 offen offset:1024 lds\n"
  "buffer_load_dwordx4 v18, s[12:15], s66 offen offset:2048 lds\n"
  "buffer_load_dwordx4 v19, s[12:15], s66 offen offset:3072 lds\n"
  "s_mov_b32 m0, s24\n"
  "s_add_i32 s66, s66, 0x20000\n"
  "buffer_load_dwordx4 v16, s[12:15], s66 offen offset:0 lds\n"
  "buffer_load_dwordx4 v17, s[12:15], s66 offen offset:1024 lds\n"
  "buffer_load_dwordx4 v18, s[12:15], s66 offen offset:2048 lds\n"
  "buffer_load_dwordx4 v19, s[12:15], s66 offen offset:3072 lds\n"
  "s_mov_b32 m0, s25\n"
  "s_add_i32 s66, s65, 0x200000\n"
  "buffer_load_dwordx4 v16, s[12:15], s66 offen offset:0 lds\n"
  "buffer_load_dwordx4 v17, s[12:15], s66 offen offset:1024 lds\n"
  "buffer_load_dwordx4 v18, s[12:15], s66 offen offset:2048 lds\n"
  "buffer_load_dwordx4 v19, s[12:15], s66 offen offset:3072 lds\n"
  "s_mov_b32 m0, s26\n"
  "s_add_i32 s66, s66, 0x20000\n"
  "s_waitcnt lgkmcnt(14)\n"
  "buffer_load_dwordx4 v16, s[12:15], s66 offen offset:0 lds\n"
  "buffer_load_dwordx4 v17, s[12:15], s66 offen offset:1024 lds\n"
  "buffer_load_dwordx4 v18, s[12:15], s66 offen offset:2048 lds\n"
  "buffer_load_dwordx4 v19, s[12:15], s66 offen offset:3072 lds\n"
  "s_waitcnt vmcnt(23)\n"
  "s_sleep 8\n"
  "buffer_load_dwordx4 v[192:195], v168, s[4:7], s72 offen offset:0 sc1\n"
  "buffer_load_dwordx4 v[196:199], v169, s[4:7], s72 offen offset:0 sc1\n"
  "buffer_load_dwordx4 v[200:203], v168, s[4:7], s72 offen offset:2048 sc1\n"
  "buffer_load_dwordx4 v[204:207], v169, s[4:7], s72 offen offset:2048 sc1\n"
  "buffer_load_dwordx4 v[208:211], v168, s[4:7], s58 offen offset:0 sc1\n"
  "buffer_load_dwordx4 v[212:215], v169, s[4:7], s58 offen offset:0 sc1\n"
  "buffer_load_dwordx4 v[216:219], v168, s[4:7], s58 offen offset:2048 sc1\n"
  "buffer_load_dwordx4 v[220:223], v169, s[4:7], s58 offen offset:2048 sc1\n"
  "buffer_load_dwordx4 v[224:227], v168, s[4:7], s67 offen offset:0 sc1\n"
  "buffer_load_dwordx4 v[228:231], v169, s[4:7], s67 offen offset:0 sc1\n"
  "buffer_load_dwordx4 v[232:235], v168, s[4:7], s67 offen offset:2048 sc1\n"
  "buffer_load_dwordx4 v[236:239], v169, s[4:7], s67 offen offset:2048 sc1\n"
  "s_waitcnt vmcnt(11)\n"
  "v_xor_b32 v192, v34, v192\n"
  "v_xor_b32 v194, v35, v194\n"
  "s_waitcnt vmcnt(10)\n"
  "v_xor_b32 v196, v34, v196\n"
  "v_xor_b32 v198, v35, v198\n"
  "v_or3_b32 v52, v192, v194, v196\n"
  "v_bitop3_b32 v52, v52, s33, v198 bitop3:0xc8\n"
  "v_cmp_ne_u32 vcc, 0, v52\n"
  "s_cbranch_vccnz Lslow2\n"
  "Lb2:\n"
  "v_mfma_f32_16x16x32_bf16 v[76:79], a[12:15], v[192:195], 0\n"
  "s_waitcnt vmcnt(9)\n"
  "v_xor_b32 v200, v34, v200\n"
  "v_xor_b32 v202, v35, v202\n"
  "v_mfma_f32_16x16x32_bf16 v[84:87], a[12:15], v[196:199], 0\n"
  "buffer_load_dwordx4 v[240:243], v168, s[4:7], s68 offen offset:0 sc1\n"
  "v_mfma_f32_16x16x32_bf16 v[80:83], a[8:11], v[196:199], 0\n"
  "s_waitcnt vmcnt(9)\n"
  "v_xor_b32 v204, v34, v204\n"
  "v_xor_b32 v206, v35, v206\n"
  "v_mfma_f32_16x16x32_bf16 v[72:75], a[8:11], v[192:195], 0\n"
  "buffer_load_dwordx4 v[244:247], v169, s[4:7], s68 offen offset:0 sc1\n"
  "v_mfma_f32_16x16x32_bf16 v[60:63], a[4:7], v[192:195], 0\n"
  "v_or3_b32 v52, v200, v202, v204\n"
  "v_bitop3_b32 v52, v52, s33, v206 bitop3:0xc8\n"
  "v_mfma_f32_16x16x32_bf16 v[68:71], a[4:7], v[196:199], 0\n"
  "v_cmp_ne_u32 vcc, 0, v52\n"
  "buffer_load_dwordx4 v[248:251], v168, s[4:7], s68 offen offset:2048 sc1\n"
  "v_mfma_f32_16x16x32_bf16 v[64:67], a[0:3], v[196:199], 0\n"
  "v_mfma_f32_16x16x32_bf16 v[56:59], a[0:3], v[192:195], 0\n"
  "buffer_load_dwordx4 v[252:255], v169, s[4:7], s68 offen offset:2048 sc1\n"
  "buffer_load_dwordx4 v[192:195], v168, s[4:7], s78 offen offset:0 sc1\n"
  "s_cbranch_vccnz Lslow3\n"
  "Lb3:\n"
  "v_mfma_f32_16x16x32_bf16 v[76:79], a[28:31], v[200:203], v[76:79]\n"
  "s_waitcnt vmcnt(12)\n"
  "v_xor_b32 v208, v34, v208\n"
  "v_xor_b32 v210, v35, v210\n"
  "v_mfma_f32_16x16x32_bf16 v[84:87], a[28:31], v[204:207], v[84:87]\n"
  "s_mov_b32 m0, s27\n"
  "s_add_i32 s66, s65, 0x300000\n"
  "s_waitcnt lgkmcnt(0)\n"
  "buffer_load_dwordx4 v16, s[12:15], s66 offen offset:0 lds\n"
  "v_mfma_f32_16x16x32_bf16 v[80:83], a[24:27], v[204:207], v[80:83]\n"
  "s_waitcnt vmcnt(12)\n"
  "v_xor_b32 v212, v34, v212\n"
  "v_xor_b32 v214, v35, v214\n"
  "v_mfma_f32_16x16x32_bf16 v[72:75], a[24:27], v[200:203], v[72:75]\n"
  "buffer_load_dwordx4 v[196:199], v169, s[4:7], s78 offen offset:0 sc1\n"
  "v_mfma_f32_16x16x32_bf16 v[60:63], a[20:23], v[200:203], v[60:63]\n"
  "v_or3_b32 v52, v208, v210, v212\n"
  "v_bitop3_b32 v52, v52, s33, v214 bitop3:0xc8\n"
  "v_mfma_f32_16x16x32_bf16 v[68:71], a[20:23], v[204:207], v[68:71]\n"
  "v_cmp_ne_u32 vcc, 0, v52\n"
  "buffer_load_dwordx4 v17, s[12:15], s66 offen offset:1024 lds\n"
  "v_mfma_f32_16x16x32_bf16 v[64:67], a[16:19], v[204:207], v[64:67]\n"
  "v_mfma_f32_16x16x32_bf16 v[56:59], a[16:19], v[200:203], v[56:59]\n"
  "buffer_load_dwordx4 v[200:203], v168, s[4:7], s78 offen offset:2048 sc1\n"
  "s_cbranch_vccnz Lslow4\n"
  "Lb4:\n"
  "v_mfma_f32_16x16x32_bf16 v[76:79], a[44:47], v[208:211], v[76:79]\n"
  "s_waitcnt vmcnt(14)\n"
  "v_xor_b32 v216, v34, v216\n"
  "v_xor_b32 v218, v35, v218\n"
  "v_mfma_f32_16x16x32_bf16 v[84:87], a[44:47], v[212:215], v[84:87]\n"
  "buffer_load_dwordx4 v[204:207], v169, s[4:7], s78 offen offset:2048 sc1\n"
  "v_mfma_f32_16x16x32_bf16 v[80:83], a[40:43], v[212:215], v[80:83]\n"
  "s_waitcnt vmcnt(14)\n"
  "v_xor_b32 v220, v34, v220\n"
  "v_xor_b32 v222, v35, v222\n"
  "v_mfma_f32_16x16x32_bf16 v[72:75], a[40:43], v[208:211], v[72:75]\n"
  "buffer_load_dwordx4 v18, s[12:15], s66 offen offset:2048 lds\n"
  "v_mfma_f32_16x16x32_bf16 v[60:63], a[36:39], v[208:211], v[60:63]\n"
  "v_or3_b32 v52, v216, v218, v220\n"
  "v_bitop3_b32 v52, v52, s33, v222 bitop3:0xc8\n"
  "v_mfma_f32_16x16x32_bf16 v[68:71], a[36:39], v[212:215], v[68:71]\n"
  "v_cmp_ne_u32 vcc, 0, v52\n"
  "v_mfma_f32_16x16x32_bf16 v[64:67], a[32:35], v[212:215], v[64:67]\n"
  "v_mfma_f32_16x16x32_bf16 v[56:59], a[32:35], v[208:211], v[56:59]\n"
  "s_cbranch_vccnz Lslow5\n"
  "Lb5:\n"
  "v_mfma_f32_16x16x32_bf16 v[76:79], a[60:63], v[216:219], v[76:79]\n"
  "s_waitcnt vmcnt(14)\n"
  "v_xor_b32 v224, v34, v224\n"
  "v_xor_b32 v226, v35, v226\n"
  "v_mfma_f32_16x16x32_bf16 v[84:87], a[60:63], v[220:223], v[84:87]\n"
  "buffer_load_dwordx4 v[208:211], v168, s[4:7], s79 offen offset:0 sc1\n"
  "v_mfma_f32_16x16x32_bf16 v[80:83], a[56:59], v[220:223], v[80:83]\n"
  "s_waitcnt vmcnt(14)\n"
  "v_xor_b32 v228, v34, v228\n"
  "v_xor_b32 v230, v35, v230\n"
  "v_mfma_f32_16x16x32_bf16 v[72:75], a[56:59], v[216:219], v[72:75]\n"
  "buffer_load_dwordx4 v[212:215], v169, s[4:7], s79 offen offset:0 sc1\n"
  "v_mfma_f32_16x16x32_bf16 v[60:63], a[52:55], v[216:219], v[60:63]\n"
  "v_or3_b32 v52, v224, v226, v228\n"
  "v_bitop3_b32 v52, v52, s33, v230 bitop3:0xc8\n"
  "v_mfma_f32_16x16x32_bf16 v[68:71], a[52:55], v[220:223], v[68:71]\n"
  "v_cmp_ne_u32 vcc, 0, v52\n"
  "v_mfma_f32_16x16x32_bf16 v[64:67], a[48:51], v[220:223], v[64:67]\n"
  "v_mfma_f32_16x16x32_bf16 v[56:59], a[48:51], v[216:219], v[56:59]\n"
  "s_cbranch_vccnz Lslow6\n"
  "Lb6:\n"
  "v_mfma_f32_16x16x32_bf16 v[76:79], a[76:79], v[224:227], v[76:79]\n"
  "s_waitcnt vmcnt(14)\n"
  "v_xor_b32 v232, v34, v232\n"
  "v_xor_b32 v234, v35, v234\n"
  "v_mfma_f32_16x16x32_bf16 v[84:87], a[76:79], v[228:231], v[84:87]\n"
  "buffer_load_dwordx4 v[216:219], v168, s[4:7], s79 offen offset:2048 sc1\n"
  "v_mfma_f32_16x16x32_bf16 v[80:83], a[72:75], v[228:231], v[80:83]\n"
  "s_waitcnt vmcnt(14)\n"
  "v_xor_b32 v236, v34, v236\n"
  "v_xor_b32 v238, v35, v238\n"
  "v_mfma_f32_16x16x32_bf16 v[72:75], a[72:75], v[224:227], v[72:75]\n"
  "buffer_load_dwordx4 v[220:223], v169, s[4:7], s79 offen offset:2048 sc1\n"
  "v_mfma_f32_16x16x32_bf16 v[60:63], a[68:71], v[224:227], v[60:63]\n"
  "v_or3_b32 v52, v232, v234, v236\n"
  "v_bitop3_b32 v52, v52, s33, v238 bitop3:0xc8\n"
  "v_mfma_f32_16x16x32_bf16 v[68:71], a[68:71], v[228:231], v[68:71]\n"
  "v_cmp_ne_u32 vcc, 0, v52\n"
  "v_mfma_f32_16x16x32_bf16 v[64:67], a[64:67], v[228:231], v[64:67]\n"
  "v_mfma_f32_16x16x32_bf16 v[56:59], a[64:67], v[224:227], v[56:59]\n"
  "s_cbranch_vccnz Lslow7\n"
  "Lb7:\n"
  "v_mfma_f32_16x16x32_bf16 v[76:79], a[92:95], v[232:235], v[76:79]\n"
  "s_waitcnt vmcnt(14)\n"
  "v_xor_b32 v240, v34, v240\n"
  "v_xor_b32 v242, v35, v242\n"
  "v_mfma_f32_16x16x32_bf16 v[84:87], a[92:95], v[236:239], v[84:87]\n"
  "buffer_load_dwordx4 v[224:227], v168, s[4:7], s69 offen offset:0 sc1\n"
  "v_mfma_f32_16x16x32_bf16 v[80:83], a[88:91], v[236:239], v[80:83]\n"
  "s_waitcnt vmcnt(14)\n"
  "v_xor_b32 v244, v34, v244\n"
  "v_xor_b32 v246, v35, v246\n"
  "v_mfma_f32_16x16x32_bf16 v[72:75], a[88:91], v[232:235], v[72:75]\n"
  "buffer_load_dwordx4 v19, s[12:15], s66 offen offset:3072 lds\n"
  "v_mfma_f32_16x16x32_bf16 v[60:63], a[84:87], v[232:235], v[60:63]\n"
  "v_or3_b32 v52, v240, v242, v244\n"
  "v_bitop3_b32 v52, v52, s33, v246 bitop3:0xc8\n"
  "v_mfma_f32_16x16x32_bf16 v[68:71], a[84:87], v[236:239], v[68:71]\n"
  "v_cmp_ne_u32 vcc, 0, v52\n"
  "v_mfma_f32_16x16x32_bf16 v[64:67], a[80:83], v[236:239], v[64:67]\n"
  "v_mfma_f32_16x16x32_bf16 v[56:59], a[80:83], v[232:235], v[56:59]\n"
  "s_cbranch_vccnz Lslow8\n"
  "Lb8:\n"
  "v_mfma_f32_16x16x32_bf16 v[76:79], a[108:111], v[240:243], v[76:79]\n"
  "s_waitcnt vmcnt(14)\n"
  "v_xor_b32 v248, v34, v248\n"
  "v_xor_b32 v250, v35, v250\n"
  "v_mfma_f32_16x16x32_bf16 v[84:87], a[108:111], v[244:247], v[84:87]\n"
  "buffer_load_dwordx4 v[228:231], v169, s[4:7], s69 offen offset:0 sc1\n"
  "s_barrier\n"
  "v_mfma_f32_16x16x32_bf16 v[80:83], a[104:107], v[244:247], v[80:83]\n"
  "s_waitcnt vmcnt(14)\n"
  "v_xor_b32 v252, v34, v252\n"
  "v_xor_b32 v254, v35, v254\n"
  "v_mfma_f32_16x16x32_bf16 v[72:75], a[104:107], v[240:243], v[72:75]\n"
  "buffer_load_dwordx4 v[232:235], v168, s[4:7], s69 offen offset:2048 sc1\n"
  "v_mfma_f32_16x16x32_bf16 v[60:63], a[100:103], v[240:243], v[60:63]\n"
  "v_or3_b32 v52, v248, v250, v252\n"
  "v_bitop3_b32 v52, v52, s33, v254 bitop3:0xc8\n"
  "v_mfma_f32_16x16x32_bf16 v[68:71], a[100:103], v[244:247], v[68:71]\n"
  "v_cmp_ne_u32 vcc, 0, v52\n"
  "v_mfma_f32_16x16x32_bf16 v[64:67], a[96:99], v[244:247], v[64:67]\n"
  "v_mfma_f32_16x16x32_bf16 v[56:59], a[96:99], v[240:243], v[56:59]\n"
  "s_cbranch_vccnz Lslow9\n"
  "Lb9:\n"
  "v_mfma_f32_16x16x32_bf16 v[76:79], a[124:127], v[248:251], v[76:79]\n"
  "s_waitcnt vmcnt(14)\n"
  "v_xor_b32 v192, v34, v192\n"
  "v_xor_b32 v194, v35, v194\n"
  "v_mfma_f32_16x16x32_bf16 v[84:87], a[124:127], v[252:255], v[84:87]\n"
  "buffer_load_dwordx4 v[236:239], v169, s[4:7], s69 offen offset:2048 sc1\n"
  "v_mfma_f32_16x16x32_bf16 v[80:83], a[120:123], v[252:255], v[80:83]\n"
  "s_waitcnt vmcnt(13)\n"
  "v_xor_b32 v196, v34, v196\n"
  "v_xor_b32 v198, v35, v198\n"
  "v_mfma_f32_16x16x32_bf16 v[72:75], a[120:123], v[248:251], v[72:75]\n"
  "buffer_load_dwordx4 v[240:243], v168, s[4:7], s77 offen offset:0 sc1\n"
  "v_mfma_f32_16x16x32_bf16 v[60:63], a[116:119], v[248:251], v[60:63]\n"
  "v_or3_b32 v52, v192, v194, v196\n"
  "v_bitop3_b32 v52, v52, s33, v198 bitop3:0xc8\n"
  "v_mfma_f32_16x16x32_bf16 v[68:71], a[116:119], v[252:255], v[68:71]\n"
  "v_cmp_ne_u32 vcc, 0, v52\n"
  "buffer_load_dwordx4 v[244:247], v169, s[4:7], s77 offen offset:0 sc1\n"
  "v_mfma_f32_16x16x32_bf16 v[64:67], a[112:115], v[252:255], v[64:67]\n"
  "v_mfma_f32_16x16x32_bf16 v[56:59], a[112:115], v[248:251], v[56:59]\n"
  "s_mov_b32 m0, s22\n"
  "s_add_i32 s66, s66, 0x20000\n"
  "buffer_load_dwordx4 v16, s[12:15], s66 offen offset:0 lds\n"
  "s_cbranch_vccnz Lslow10\n"
  "Lb10:\n"
  "v_mfma_f32_16x16x32_bf16 v[76:79], a[140:143], v[192:195], v[76:79]\n"
  "s_waitcnt vmcnt(14)\n"
  "v_xor_b32 v200, v34, v200\n"
  "v_xor_b32 v202, v35, v202\n"
  "v_mfma_f32_16x16x32_bf16 v[84:87], a[140:143], v[196:199], v[84:87]\n"
  "buffer_load_dwordx4 v[248:251], v168, s[4:7], s77 offen offset:2048 sc1\n"
  "v_mfma_f32_16x16x32_bf16 v[80:83], a[136:139], v[196:199], v[80:83]\n"
  "s_waitcnt vmcnt(14)\n"
  "v_xor_b32 v204, v34, v204\n"
  "v_xor_b32 v206, v35, v206\n"
  "v_mfma_f32_16x16x32_bf16 v[72:75], a[136:139], v[192:195], v[72:75]\n"
  "buffer_load_dwordx4 v[252:255], v169, s[4:7], s77 offen offset:2048 sc1\n"
  "v_mfma_f32_16x16x32_bf16 v[60:63], a[132:135], v[192:195], v[60:63]\n"
  "v_or3_b32 v52, v200, v202, v204\n"
  "v_bitop3_b32 v52, v52, s33, v206 bitop3:0xc8\n"
  "v_mfma_f32_16x16x32_bf16 v[68:71], a[132:135], v[196:199], v[68:71]\n"
  "v_cmp_ne_u32 vcc, 0, v52\n"
  "buffer_load_dwordx4 v17, s[12:15], s66 offen offset:1024 lds\n"
  "v_mfma_f32_16x16x32_bf16 v[64:67], a[128:131], v[196:199], v[64:67]\n"
  "v_mfma_f32_16x16x32_bf16 v[56:59], a[128:131], v[192:195], v[56:59]\n"
  "s_cbranch_vccnz Lslow11\n"
  "Lb11:\n"
  "v_mfma_f32_16x16x32_bf16 v[76:79], a[156:159], v[200:203], v[76:79]\n"
  "s_waitcnt vmcnt(14)\n"
  "v_xor_b32 v208, v34, v208\n"
  "v_xor_b32 v210, v35, v210\n"
  "v_mfma_f32_16x16x32_bf16 v[84:87], a[156:159], v[204:207], v[84:87]\n"
  "buffer_load_dwordx4 v[192:195], v170, s[4:7], s72 offen offset:0 sc1\n"
  "v_mfma_f32_16x16x32_bf16 v[80:83], a[152:155], v[204:207], v[80:83]\n"
  "s_waitcnt vmcnt(14)\n"
  "v_xor_b32 v212, v34, v212\n"
  "v_xor_b32 v214, v35, v214\n"
  "v_mfma_f32_16x16x32_bf16 v[72:75], a[152:155], v[200:203], v[72:75]\n"
  "buffer_load_dwordx4 v[196:199], v171, s[4:7], s72 offen offset:0 sc1\n"
  "v_mfma_f32_16x16x32_bf16 v[60:63], a[148:151], v[200:203], v[60:63]\n"
  "v_or3_b32 v52, v208, v210, v212\n"
  "v_bitop3_b32 v52, v52, s33, v214 bitop3:0xc8\n"
  "v_mfma_f32_16x16x32_bf16 v[68:71], a[148:151], v[204:207], v[68:71]\n"
  "v_cmp_ne_u32 vcc, 0, v52\n"
  "v_mfma_f32_16x16x32_bf16 v[64:67], a[144:147], v[204:207], v[64:67]\n"
  "v_mfma_f32_16x16x32_bf16 v[56:59], a[144:147], v[200:203], v[56:59]\n"
  "s_cbranch_vccnz Lslow12\n"
  "Lb12:\n"
  "v_mfma_f32_16x16x32_bf16 v[76:79], a[172:175], v[208:211], v[76:79]\n"
  "s_waitcnt vmcnt(14)\n"
  "v_xor_b32 v216, v34, v216\n"
  "v_xor_b32 v218, v35, v218\n"
  "v_mfma_f32_16x16x32_bf16 v[84:87], a[172:175], v[212:215], v[84:87]\n"
  "buffer_load_dwordx4 v[200:203], v170, s[4:7], s72 offen offset:2048 sc1\n"
  "v_mfma_f32_16x16x32_bf16 v[80:83], a[168:171], v[212:215], v[80:83]\n"
  "s_waitcnt vmcnt(14)\n"
  "v_xor_b32 v220, v34, v220\n"
  "v_xor_b32 v222, v35, v222\n"
  "v_mfma_f32_16x16x32_bf16 v[72:75], a[168:171], v[208:211], v[72:75]\n"
  "buffer_load_dwordx4 v[204:207], v171, s[4:7], s72 offen offset:2048 sc1\n"
  "v_mfma_f32_16x16x32_bf16 v[60:63], a[164:167], v[208:211], v[60:63]\n"
  "v_or3_b32 v52, v216, v218, v220\n"
  "v_bitop3_b32 v52, v52, s33, v222 bitop3:0xc8\n"
  "v_mfma_f32_16x16x32_bf16 v[68:71], a[164:167], v[212:215], v[68:71]\n"
  "v_cmp_ne_u32 vcc, 0, v52\n"
  "v_mfma_f32_16x16x32_bf16 v[64:67], a[160:163], v[212:215], v[64:67]\n"
  "v_mfma_f32_16x16x32_bf16 v[56:59], a[160:163], v[208:211], v[56:59]\n"
  "s_cbranch_vccnz Lslow13\n"
  "Lb13:\n"
  "v_mfma_f32_16x16x32_bf16 v[76:79], a[188:191], v[216:219], v[76:79]\n"
  "s_waitcnt vmcnt(14)\n"
  "v_xor_b32 v224, v34, v224\n"
  "v_xor_b32 v226, v35, v226\n"
  "v_mfma_f32_16x16x32_bf16 v[84:87], a[188:191], v[220:223], v[84:87]\n"
  "buffer_load_dwordx4 v[208:211], v170, s[4:7], s58 offen offset:0 sc1\n"
  "v_mfma_f32_16x16x32_bf16 v[80:83], a[184:187], v[220:223], v[80:83]\n"
  "s_waitcnt vmcnt(13)\n"
  "v_xor_b32 v228, v34, v228\n"
  "v_xor_b32 v230, v35, v230\n"
  "v_mfma_f32_16x16x32_bf16 v[72:75], a[184:187], v[216:219], v[72:75]\n"
  "buffer_load_dwordx4 v18, s[12:15], s66 offen offset:2048 lds\n"
  "v_mfma_f32_16x16x32_bf16 v[60:63], a[180:183], v[216:219], v[60:63]\n"
  "v_or3_b32 v52, v224, v226, v228\n"
  "v_bitop3_b32 v52, v52, s33, v230 bitop3:0xc8\n"
  "v_mfma_f32_16x16x32_bf16 v[68:71], a[180:183], v[220:223], v[68:71]\n"
  "v_cmp_ne_u32 vcc, 0, v52\n"
  "buffer_load_dwordx4 v[212:215], v171, s[4:7], s58 offen offset:0 sc1\n"
  "v_mfma_f32_16x16x32_bf16 v[64:67], a[176:179], v[220:223], v[64:67]\n"
  "v_mfma_f32_16x16x32_bf16 v[56:59], a[176:179], v[216:219], v[56:59]\n"
  "s_cbranch_vccnz Lslow14\n"
  "Lb14:\n"
  "v_mfma_f32_16x16x32_bf16 v[76:79], a[204:207], v[224:227], v[76:79]\n"
  "s_waitcnt vmcnt(14)\n"
  "v_xor_b32 v232, v34, v232\n"
  "v_xor_b32 v234, v35, v234\n"
  "v_mfma_f32_16x16x32_bf16 v[84:87], a[204:207], v[228:231], v[84:87]\n"
  "buffer_load_dwordx4 v[216:219], v170, s[4:7], s58 offen offset:2048 sc1\n"
  "v_mfma_f32_16x16x32_bf16 v[80:83], a[200:203], v[228:231], v[80:83]\n"
  "s_waitcnt vmcnt(14)\n"
  "v_xor_b32 v236, v34, v236\n"
  "v_xor_b32 v238, v35, v238\n"
  "v_mfma_f32_16x16x32_bf16 v[72:75], a[200:203], v[224:227], v[72:75]\n"
  "buffer_load_dwordx4 v[220:223], v171, s[4:7], s58 offen offset:2048 sc1\n"
  "v_mfma_f32_16x16x32_bf16 v[60:63], a[196:199], v[224:227], v[60:63]\n"
  "v_or3_b32 v52, v232, v234, v236\n"
  "v_bitop3_b32 v52, v52, s33, v238 bitop3:0xc8\n"
  "v_mfma_f32_16x16x32_bf16 v[68:71], a[196:199], v[228:231], v[68:71]\n"
  "v_cmp_ne_u32 vcc, 0, v52\n"
  "v_mfma_f32_16x16x32_bf16 v[64:67], a[192:195], v[228:231], v[64:67]\n"
  "v_mfma_f32_16x16x32_bf16 v[56:59], a[192:195], v[224:227], v[56:59]\n"
  "s_cbranch_vccnz Lslow15\n"
  "Lb15:\n"
  "v_mfma_f32_16x16x32_bf16 v[76:79], a[220:223], v[232:235], v[76:79]\n"
  "s_waitcnt vmcnt(14)\n"
  "v_xor_b32 v240, v34, v240\n"
  "v_xor_b32 v242, v35, v242\n"
  "v_mfma_f32_16x16x32_bf16 v[84:87], a[220:223], v[236:239], v[84:87]\n"
  "buffer_load_dwordx4 v[224:227], v170, s[4:7], s67 offen offset:0 sc1\n"
  "v_mfma_f32_16x16x32_bf16 v[80:83], a[216:219], v[236:239], v[80:83]\n"
  "s_waitcnt vmcnt(14)\n"
  "v_xor_b32 v244, v34, v244\n"
  "v_xor_b32 v246, v35, v246\n"
  "v_mfma_f32_16x16x32_bf16 v[72:75], a[216:219], v[232:235], v[72:75]\n"
  "buffer_load_dwordx4 v[228:231], v171, s[4:7], s67 offen offset:0 sc1\n"
  "v_mfma_f32_16x16x32_bf16 v[60:63], a[212:215], v[232:235], v[60:63]\n"
  "v_or3_b32 v52, v240, v242, v244\n"
  "v_bitop3_b32 v52, v52, s33, v246 bitop3:0xc8\n"
  "v_mfma_f32_16x16x32_bf16 v[68:71], a[212:215], v[236:239], v[68:71]\n"
  "v_cmp_ne_u32 vcc, 0, v52\n"
  "buffer_load_dwordx4 v19, s[12:15], s66 offen offset:3072 lds\n"
  "v_mfma_f32_16x16x32_bf16 v[64:67], a[208:211], v[236:239], v[64:67]\n"
  "v_mfma_f32_16x16x32_bf16 v[56:59], a[208:211], v[232:235], v[56:59]\n"
  "s_cbranch_vccnz Lslow16\n"
  "Lb16:\n"
  "v_mfma_f32_16x16x32_bf16 v[76:79], a[236:239], v[240:243], v[76:79]\n"
  "s_waitcnt vmcnt(14)\n"
  "v_xor_b32 v248, v34, v248\n"
  "v_xor_b32 v250, v35, v250\n"
  "v_mfma_f32_16x16x32_bf16 v[84:87], a[236:239], v[244:247], v[84:87]\n"
  "buffer_load_dwordx4 v[232:235], v170, s[4:7], s67 offen offset:2048 sc1\n"
  "v_mfma_f32_16x16x32_bf16 v[80:83], a[232:235], v[244:247], v[80:83]\n"
  "s_waitcnt vmcnt(14)\n"
  "v_xor_b32 v252, v34, v252\n"
  "v_xor_b32 v254, v35, v254\n"
  "v_mfma_f32_16x16x32_bf16 v[72:75], a[232:235], v[240:243], v[72:75]\n"
  "buffer_load_dwordx4 v[236:239], v171, s[4:7], s67 offen offset:2048 sc1\n"
  "v_mfma_f32_16x16x32_bf16 v[60:63], a[228:231], v[240:243], v[60:63]\n"
  "v_or3_b32 v52, v248, v250, v252\n"
  "v_bitop3_b32 v52, v52, s33, v254 bitop3:0xc8\n"
  "v_mfma_f32_16x16x32_bf16 v[68:71], a[228:231], v[244:247], v[68:71]\n"
  "v_cmp_ne_u32 vcc, 0, v52\n"
  "v_mfma_f32_16x16x32_bf16 v[64:67], a[224:227], v[244:247], v[64:67]\n"
  "v_mfma_f32_16x16x32_bf16 v[56:59], a[224:227], v[240:243], v[56:59]\n"
  "buffer_load_dwordx4 v[240:243], v170, s[4:7], s68 offen offset:0 sc1\n"
  "s_cbranch_vccnz Lslow17\n"
  "Lb17:\n"
  "v_mfma_f32_16x16x32_bf16 v[76:79], a[252:255], v[248:251], v[76:79]\n"
  "s_waitcnt vmcnt(14)\n"
  "v_xor_b32 v192, v34, v192\n"
  "v_xor_b32 v194, v35, v194\n"
  "v_mfma_f32_16x16x32_bf16 v[84:87], a[252:255], v[252:255], v[84:87]\n"
  "buffer_load_dwordx4 v[244:247], v171, s[4:7], s68 offen offset:0 sc1\n"
  "v_mfma_f32_16x16x32_bf16 v[80:83], a[248:251], v[252:255], v[80:83]\n"
  "s_waitcnt vmcnt(14)\n"
  "v_xor_b32 v196, v34, v196\n"
  "v_xor_b32 v198, v35, v198\n"
  "v_mfma_f32_16x16x32_bf16 v[72:75], a[248:251], v[248:251], v[72:75]\n"
  "v_mfma_f32_16x16x32_bf16 v[60:63], a[244:247], v[248:251], v[60:63]\n"
  "v_or3_b32 v52, v192, v194, v196\n"
  "v_bitop3_b32 v52, v52, s33, v198 bitop3:0xc8\n"
  "v_mfma_f32_16x16x32_bf16 v[68:71], a[244:247], v[252:255], v[68:71]\n"
  "v_cmp_ne_u32 vcc, 0, v52\n"
  "v_mfma_f32_16x16x32_bf16 v[64:67], a[240:243], v[252:255], v[64:67]\n"
  "v_mfma_f32_16x16x32_bf16 v[56:59], a[240:243], v[248:251], v[56:59]\n"
  "buffer_load_dwordx4 v[248:251], v170, s[4:7], s68 offen offset:2048 sc1\n"
  "s_cbranch_vccnz Lslow18\n"
  "Lb18:\n"
  "v_mfma_f32_16x16x32_bf16 v[108:111], a[12:15], v[192:195], 0\n"
  "s_waitcnt vmcnt(14)\n"
  "v_xor_b32 v200, v34, v200\n"
  "v_xor_b32 v202, v35, v202\n"
  "v_mfma_f32_16x16x32_bf16 v[116:119], a[12:15], v[196:199], 0\n"
  "v_mfma_f32_16x16x32_bf16 v[112:115], a[8:11], v[196:199], 0\n"
  "s_waitcnt vmcnt(13)\n"
  "v_xor_b32 v204, v34, v204\n"
  "v_xor_b32 v206, v35, v206\n"
  "v_mfma_f32_16x16x32_bf16 v[104:107], a[8:11], v[192:195], 0\n"
  "v_mfma_f32_16x16x32_bf16 v[92:95], a[4:7], v[192:195], 0\n"
  "v_or3_b32 v52, v200, v202, v204\n"
  "v_bitop3_b32 v52, v52, s33, v206 bitop3:0xc8\n"
  "v_mfma_f32_16x16x32_bf16 v[100:103], a[4:7], v[196:199], 0\n"
  "v_cmp_ne_u32 vcc, 0, v52\n"
  "v_mfma_f32_16x16x32_bf16 v[96:99], a[0:3], v[196:199], 0\n"
  "v_mfma_f32_16x16x32_bf16 v[88:91], a[0:3], v[192:195], 0\n"
  "s_cbranch_vccnz Lslow19\n"
  "Lb19:\n"
  "ds_write_b128 v21, v[76:79] offset:1024\n"
  "ds_write_b128 v21, v[72:75] offset:0\n"
  "v_mfma_f32_16x16x32_bf16 v[108:111], a[28:31], v[200:203], v[108:111]\n"
  "s_waitcnt vmcnt(12)\n"
  "v_xor_b32 v208, v34, v208\n"
  "v_xor_b32 v210, v35, v210\n"
  "v_mfma_f32_16x16x32_bf16 v[116:119], a[28:31], v[204:207], v[116:119]\n"
  "buffer_load_dwordx4 v[252:255], v171, s[4:7], s68 offen offset:2048 sc1\n"
  "v_mfma_f32_16x16x32_bf16 v[112:115], a[24:27], v[204:207], v[112:115]\n"
  "s_waitcnt vmcnt(11)\n"
  "v_xor_b32 v212, v34, v212\n"
  "v_xor_b32 v214, v35, v214\n"
  "v_mfma_f32_16x16x32_bf16 v[104:107], a[24:27], v[200:203], v[104:107]\n"
  "buffer_load_dwordx4 v[192:195], v170, s[4:7], s78 offen offset:0 sc1\n"
  "ds_read_b64_tr_b16 a[0:1], v8 offset:16384\n"
  "ds_read_b64_tr_b16 a[2:3], v8 offset:16896\n"
  "ds_read_b64_tr_b16 a[4:5], v9 offset:16384\n"
  "ds_read_b64_tr_b16 a[6:7], v9 offset:16896\n"
  "v_mfma_f32_16x16x32_bf16 v[92:95], a[20:23], v[200:203], v[92:95]\n"
  "v_or3_b32 v52, v208, v210, v212\n"
  "v_bitop3_b32 v52, v52, s33, v214 bitop3:0xc8\n"
  "v_mfma_f32_16x16x32_bf16 v[100:103], a[20:23], v[204:207], v[100:103]\n"
  "v_cmp_ne_u32 vcc, 0, v52\n"
  "buffer_load_dwordx4 v[196:199], v171, s[4:7], s78 offen offset:0 sc1\n"
  "v_mfma_f32_16x16x32_bf16 v[96:99], a[16:19], v[204:207], v[96:99]\n"
  "v_mfma_f32_16x16x32_bf16 v[88:91], a[16:19], v[200:203], v[88:91]\n"
  "s_cbranch_vccnz Lslow20\n"
  "Lb20:\n"
  "ds_write_b128 v22, v[84:87] offset:1024\n"
  "ds_write_b128 v22, v[80:83] offset:0\n"
  "v_mfma_f32_16x16x32_bf16 v[108:111], a[44:47], v[208:211], v[108:111]\n"
  "s_waitcnt vmcnt(12)\n"
  "v_xor_b32 v216, v34, v216\n"
  "v_xor_b32 v218, v35, v218\n"
  "v_mfma_f32_16x16x32_bf16 v[116:119], a[44:47], v[212:215], v[116:119]\n"
  "buffer_load_dwordx4 v[200:203], v170, s[4:7], s78 offen offset:2048 sc1\n"
  "v_mfma_f32_16x16x32_bf16 v[112:115], a[40:43], v[212:215], v[112:115]\n"
  "s_waitcnt vmcnt(12)\n"
  "v_xor_b32 v220, v34, v220\n"
  "v_xor_b32 v222, v35, v222\n"
  "v_mfma_f32_16x16x32_bf16 v[104:107], a[40:43], v[208:211], v[104:107]\n"
  "buffer_load_dwordx4 v[204:207], v171, s[4:7], s78 offen offset:2048 sc1\n"
  "ds_read_b64_tr_b16 a[8:9], v10 offset:16384\n"
  "ds_read_b64_tr_b16 a[10:11], v10 offset:16896\n"
  "ds_read_b64_tr_b16 a[12:13], v11 offset:16384\n"
  "ds_read_b64_tr_b16 a[14:15], v11 offset:16896\n"
  "v_mfma_f32_16x16x32_bf16 v[92:95], a[36:39], v[208:211], v[92:95]\n"
  "v_or3_b32 v52, v216, v218, v220\n"
  "v_bitop3_b32 v52, v52, s33, v222 bitop3:0xc8\n"
  "v_mfma_f32_16x16x32_bf16 v[100:103], a[36:39], v[212:215], v[100:103]\n"
  "v_cmp_ne_u32 vcc, 0, v52\n"
  "v_mfma_f32_16x16x32_bf16 v[96:99], a[32:35], v[212:215], v[96:99]\n"
  "v_mfma_f32_16x16x32_bf16 v[88:91], a[32:35], v[208:211], v[88:91]\n"
  "s_cbranch_vccnz Lslow21\n"
  "Lb21:\n"
  "ds_write_b128 v20, v[68:71] offset:1024\n"
  "ds_write_b128 v20, v[64:67] offset:0\n"
  "v_mfma_f32_16x16x32_bf16 v[108:111], a[60:63], v[216:219], v[108:111]\n"
  "s_waitcnt vmcnt(12)\n"
  "v_xor_b32 v224, v34, v224\n"
  "v_xor_b32 v226, v35, v226\n"
  "v_mfma_f32_16x16x32_bf16 v[116:119], a[60:63], v[220:223], v[116:119]\n"
  "s_mov_b32 m0, s21\n"
  "s_add_i32 s66, s65, 0x400000\n"
  "s_waitcnt lgkmcnt(2)\n"
  "buffer_load_dwordx4 v16, s[12:15], s66 offen offset:0 lds\n"
  "v_mfma_f32_16x16x32_bf16 v[112:115], a[56:59], v[220:223], v[112:115]\n"
  "s_waitcnt vmcnt(12)\n"
  "v_xor_b32 v228, v34, v228\n"
  "v_xor_b32 v230, v35, v230\n"
  "v_mfma_f32_16x16x32_bf16 v[104:107], a[56:59], v[216:219], v[104:107]\n"
  "buffer_load_dwordx4 v[208:211], v170, s[4:7], s79 offen offset:0 sc1\n"
  "ds_read_b64_tr_b16 a[16:17], v8 offset:0\n"
  "ds_read_b64_tr_b16 a[18:19], v8 offset:512\n"
  "ds_read_b64_tr_b16 a[20:21], v9 offset:0\n"
  "ds_read_b64_tr_b16 a[22:23], v9 offset:512\n"
  "v_mfma_f32_16x16x32_bf16 v[92:95], a[52:55], v[216:219], v[92:95]\n"
  "v_or3_b32 v52, v224, v226, v228\n"
  "v_bitop3_b32 v52, v52, s33, v230 bitop3:0xc8\n"
  "v_mfma_f32_16x16x32_bf16 v[100:103], a[52:55], v[220:223], v[100:103]\n"
  "v_cmp_ne_u32 vcc, 0, v52\n"
  "buffer_load_dwordx4 v[212:215], v171, s[4:7], s79 offen offset:0 sc1\n"
  "v_mfma_f32_16x16x32_bf16 v[96:99], a[48:51], v[220:223], v[96:99]\n"
  "v_mfma_f32_16x16x32_bf16 v[88:91], a[48:51], v[216:219], v[88:91]\n"
  "s_cbranch_vccnz Lslow22\n"
  "Lb22:\n"
  "s_waitcnt lgkmcnt(0)\n"
  "s_barrier\n"
  "ds_read_b128 v[120:123], v26 offset:0\n"
  "ds_read_b128 v[128:131], v26 offset:2048\n"
  "ds_read_b128 v[136:139], v26 offset:4096\n"
  "ds_read_b128 v[124:127], v26 offset:1024\n"
  "ds_read_b128 v[132:135], v26 offset:3072\n"
  "ds_read_b128 v[140:143], v26 offset:5120\n"
  "v_mfma_f32_16x16x32_bf16 v[108:111], a[76:79], v[224:227], v[108:111]\n"
  "s_waitcnt vmcnt(12)\n"
  "v_xor_b32 v232, v34, v232\n"
  "v_xor_b32 v234, v35, v234\n"
  "v_mfma_f32_16x16x32_bf16 v[116:119], a[76:79], v[228:231], v[116:119]\n"
  "buffer_load_dwordx4 v17, s[12:15], s66 offen offset:1024 lds\n"
  "v_mfma_f32_16x16x32_bf16 v[112:115], a[72:75], v[228:231], v[112:115]\n"
  "s_waitcnt vmcnt(12)\n"
  "v_xor_b32 v236, v34, v236\n"
  "v_xor_b32 v238, v35, v238\n"
  "v_mfma_f32_16x16x32_bf16 v[104:107], a[72:75], v[224:227], v[104:107]\n"
  "buffer_load_dwordx4 v[216:219], v170, s[4:7], s79 offen offset:2048 sc1\n"
  "ds_read_b64_tr_b16 a[24:25], v10 offset:0\n"
  "ds_read_b64_tr_b16 a[26:27], v10 offset:512\n"
  "ds_read_b64_tr_b16 a[28:29], v11 offset:0\n"
  "ds_read_b64_tr_b16 a[30:31], v11 offset:512\n"
  "v_mfma_f32_16x16x32_bf16 v[92:95], a[68:71], v[224:227], v[92:95]\n"
  "v_or3_b32 v52, v232, v234, v236\n"
  "v_bitop3_b32 v52, v52, s33, v238 bitop3:0xc8\n"
  "v_mfma_f32_16x16x32_bf16 v[100:103], a[68:71], v[228:231], v[100:103]\n"
  "v_cmp_ne_u32_e64 vcc, 0, v52\n"
  "v_mfma_f32_16x16x32_bf16 v[96:99], a[64:67], v[228:231], v[96:99]\n"
  "v_mfma_f32_16x16x32_bf16 v[88:91], a[64:67], v[224:227], v[88:91]\n"
  "s_cbranch_vccnz Lslow23\n"
  "Lb23:\n"
  "s_waitcnt lgkmcnt(7)\n"
  "v_pk_add_f32 v[136:137], v[128:129], v[136:137]\n"
  "v_pk_add_f32 v[138:139], v[130:131], v[138:139]\n"
  "v_pk_add_f32 v[120:121], v[120:121], v[56:57]\n"
  "v_pk_add_f32 v[122:123], v[122:123], v[58:59]\n"
  "v_pk_add_f32 v[120:121], v[120:121], v[136:137]\n"
  "v_pk_add_f32 v[122:123], v[122:123], v[138:139]\n"
  "v_pk_fma_f32 v[144:145], v[120:121], s[34:35], v[44:45] op_sel_hi:[1,0,1]\n"
  "v_pk_fma_f32 v[146:147], v[122:123], s[34:35], v[46:47] op_sel_hi:[1,0,1]\n"
  "s_waitcnt lgkmcnt(4)\n"
  "v_pk_add_f32 v[140:141], v[132:133], v[140:141]\n"
  "v_pk_add_f32 v[142:143], v[134:135], v[142:143]\n"
  "v_pk_add_f32 v[124:125], v[124:125], v[60:61]\n"
  "v_pk_add_f32 v[126:127], v[126:127], v[62:63]\n"
  "v_pk_add_f32 v[124:125], v[124:125], v[140:141]\n"
  "v_pk_add_f32 v[126:127], v[126:127], v[142:143]\n"
  "v_pk_fma_f32 v[148:149], v[124:125], s[34:35], v[48:49] op_sel_hi:[1,0,1]\n"
  "v_pk_fma_f32 v[150:151], v[126:127], s[34:35], v[50:51] op_sel_hi:[1,0,1]\n"
  "v_pk_mul_f32 v[152:153], v[144:145], v[144:145]\n"
  "v_pk_mul_f32 v[154:155], v[146:147], v[146:147]\n"
  "v_pk_mul_f32 v[156:157], v[148:149], v[148:149]\n"
  "v_pk_mul_f32 v[158:159], v[150:151], v[150:151]\n"
  "v_pk_fma_f32 v[152:153], v[152:153], s[82:83], v[28:29] op_sel_hi:[1,0,0]\n"
  "v_pk_fma_f32 v[154:155], v[154:155], s[82:83], v[28:29] op_sel_hi:[1,0,0]\n"
  "v_pk_fma_f32 v[156:157], v[156:157], s[82:83], v[28:29] op_sel_hi:[1,0,0]\n"
  "v_pk_fma_f32 v[158:159], v[158:159], s[82:83], v[28:29] op_sel_hi:[1,0,0]\n"
  "v_pk_mul_f32 v[152:153], v[152:153], v[144:145]\n"
  "v_pk_mul_f32 v[154:155], v[154:155], v[146:147]\n"
  "v_pk_mul_f32 v[156:157], v[156:157], v[148:149]\n"
  "v_pk_mul_f32 v[158:159], v[158:159], v[150:151]\n"
  "v_exp_f32 v152, v152\n"
  "v_exp_f32 v153, v153\n"
  "v_exp_f32 v154, v154\n"
  "v_exp_f32 v155, v155\n"
  "v_exp_f32 v156, v156\n"
  "v_exp_f32 v157, v157\n"
  "v_exp_f32 v158, v158\n"
  "v_exp_f32_e64 v159, v159\n"
  "v_pk_fma_f32 v[152:153], v[152:153], s[32:33], s[32:33] op_sel_hi:[1,0,0]\n"
  "v_pk_fma_f32 v[154:155], v[154:155], s[32:33], s[32:33] op_sel_hi:[1,0,0]\n"
  "v_pk_fma_f32 v[156:157], v[156:157], s[32:33], s[32:33] op_sel_hi:[1,0,0]\n"
  "v_pk_fma_f32 v[158:159], v[158:159], s[32:33], s[32:33] op_sel_hi:[1,0,0]\n"
  "v_rcp_f32 v152, v152\n"
  "v_rcp_f32 v153, v153\n"
  "v_rcp_f32 v154, v154\n"
  "v_rcp_f32 v155, v155\n"
  "v_rcp_f32 v156, v156\n"
  "v_rcp_f32 v157, v157\n"
  "v_rcp_f32 v158, v158\n"
  "v_rcp_f32 v159, v159\n"
  "v_pk_mul_f32 v[152:153], v[152:153], v[144:145]\n"
  "v_pk_mul_f32 v[154:155], v[154:155], v[146:147]\n"
  "v_pk_mul_f32 v[156:157], v[156:157], v[148:149]\n"
  "v_pk_mul_f32 v[158:159], v[158:159], v[150:151]\n"
  "v_cvt_pk_bf16_f32 v144, v152, v153\n"
  "v_cvt_pk_bf16_f32 v145, v154, v155\n"
  "v_cvt_pk_bf16_f32 v146, v156, v157\n"
  "v_cvt_pk_bf16_f32 v147, v158, v159\n"
  "v_or_b32 v144, v34, v144\n"
  "v_or_b32 v146, v35, v146\n"
  "buffer_store_dwordx4 v[144:147], v1, s[4:7], s71 offen sc1\n"
  "s_add_i32 s71, s71, 114688\n"
  "v_mfma_f32_16x16x32_bf16 v[108:111], a[92:95], v[232:235], v[108:111]\n"
  "s_waitcnt vmcnt(13)\n"
  "v_xor_b32 v240, v34, v240\n"
  "v_xor_b32 v242, v35, v242\n"
  "v_mfma_f32_16x16x32_bf16 v[116:119], a[92:95], v[236:239], v[116:119]\n"
  "v_mfma_f32_16x16x32_bf16 v[112:115], a[88:91], v[236:239], v[112:115]\n"
  "s_waitcnt vmcnt(12)\n"
  "v_xor_b32 v244, v34, v244\n"
  "v_xor_b32 v246, v35, v246\n"
  "v_mfma_f32_16x16x32_bf16 v[104:107], a[88:91], v[232:235], v[104:107]\n"
  "buffer_load_dwordx4 v[220:223], v171, s[4:7], s79 offen offset:2048 sc1\n"
  "ds_read_b64_tr_b16 a[32:33], v8 offset:49152\n"
  "ds_read_b64_tr_b16 a[34:35], v8 offset:49664\n"
  "ds_read_b64_tr_b16 a[36:37], v9 offset:49152\n"
  "ds_read_b64_tr_b16 a[38:39], v9 offset:49664\n"
  "v_mfma_f32_16x16x32_bf16 v[92:95], a[84:87], v[232:235], v[92:95]\n"
  "v_or3_b32 v52, v240, v242, v244\n"
  "v_bitop3_b32 v52, v52, s33, v246 bitop3:0xc8\n"
  "v_mfma_f32_16x16x32_bf16 v[100:103], a[84:87], v[236:239], v[100:103]\n"
  "v_cmp_ne_u32 vcc, 0, v52\n"
  "v_mfma_f32_16x16x32_bf16 v[96:99], a[80:83], v[236:239], v[96:99]\n"
  "v_mfma_f32_16x16x32_bf16 v[88:91], a[80:83], v[232:235], v[88:91]\n"
  "s_cbranch_vccnz Lslow24\n"
  "Lb24:\n"
  "s_barrier\n"
  "v_mfma_f32_16x16x32_bf16 v[108:111], a[108:111], v[240:243], v[108:111]\n"
  "s_waitcnt vmcnt(12)\n"
  "v_xor_b32 v248, v34, v248\n"
  "v_xor_b32 v250, v35, v250\n"
  "v_mfma_f32_16x16x32_bf16 v[116:119], a[108:111], v[244:247], v[116:119]\n"
  "buffer_load_dwordx4 v[224:227], v170, s[4:7], s69 offen offset:0 sc1\n"
  "v_mfma_f32_16x16x32_bf16 v[112:115], a[104:107], v[244:247], v[112:115]\n"
  "s_waitcnt vmcnt(12)\n"
  "v_xor_b32 v252, v34, v252\n"
  "v_xor_b32_e64 v254, v35, v254\n"
  "v_mfma_f32_16x16x32_bf16 v[104:107], a[104:107], v[240:243], v[104:107]\n"
  "buffer_load_dwordx4 v18, s[12:15], s66 offen offset:2048 lds\n"
  "ds_read_b64_tr_b16 a[40:41], v10 offset:49152\n"
  "ds_read_b64_tr_b16 a[42:43], v10 offset:49664\n"
  "ds_read_b64_tr_b16 a[44:45], v11 offset:49152\n"
  "ds_read_b64_tr_b16 a[46:47], v11 offset:49664\n"
  "v_mfma_f32_16x16x32_bf16 v[92:95], a[100:103], v[240:243], v[92:95]\n"
  "v_or3_b32 v52, v248, v250, v252\n"
  "v_bitop3_b32 v52, v52, s33, v254 bitop3:0xc8\n"
  "v_mfma_f32_16x16x32_bf16 v[100:103], a[100:103], v[244:247], v[100:103]\n"
  "v_cmp_ne_u32 vcc, 0, v52\n"
  "v_mfma_f32_16x16x32_bf16 v[96:99], a[96:99], v[244:247], v[96:99]\n"
  "v_mfma_f32_16x16x32_bf16 v[88:91], a[96:99], v[240:243], v[88:91]\n"
  "s_cbranch_vccnz Lslow25\n"
  "Lb25:\n"
  "v_mfma_f32_16x16x32_bf16 v[108:111], a[124:127], v[248:251], v[108:111]\n"
  "s_waitcnt vmcnt(12)\n"
  "v_xor_b32 v192, v34, v192\n"
  "v_xor_b32 v194, v35, v194\n"
  "v_mfma_f32_16x16x32_bf16 v[116:119], a[124:127], v[252:255], v[116:119]\n"
  "buffer_load_dwordx4 v[228:231], v171, s[4:7], s69 offen offset:0 sc1\n"
  "v_mfma_f32_16x16x32_bf16 v[112:115], a[120:123], v[252:255], v[112:115]\n"
  "s_waitcnt vmcnt(12)\n"
  "v_xor_b32 v196, v34, v196\n"
  "v_xor_b32 v198, v35, v198\n"
  "v_mfma_f32_16x16x32_bf16 v[104:107], a[120:123], v[248:251], v[104:107]\n"
  "buffer_load_dwordx4 v[232:235], v170, s[4:7], s69 offen offset:2048 sc1\n"
  "ds_read_b64_tr_b16 a[48:49], v23 offset:0\n"
  "ds_read_b64_tr_b16 a[50:51], v23 offset:512\n"
  "ds_read_b64_tr_b16 a[52:53], v53 offset:0\n"
  "ds_read_b64_tr_b16 a[54:55], v53 offset:512\n"
  "v_mfma_f32_16x16x32_bf16 v[92:95], a[116:119], v[248:251], v[92:95]\n"
  "v_or3_b32 v52, v192, v194, v196\n"
  "v_bitop3_b32 v52, v52, s33, v198 bitop3:0xc8\n"
  "v_mfma_f32_16x16x32_bf16 v[100:103], a[116:119], v[252:255], v[100:103]\n"
  "v_cmp_ne_u32 vcc, 0, v52\n"
  "v_mfma_f32_16x16x32_bf16 v[96:99], a[112:115], v[252:255], v[96:99]\n"
  "v_mfma_f32_16x16x32_bf16 v[88:91], a[112:115], v[248:251], v[88:91]\n"
  "s_cbranch_vccnz Lslow26\n"
  "Lb26:\n"
  "v_mfma_f32_16x16x32_bf16 v[108:111], a[140:143], v[192:195], v[108:111]\n"
  "s_waitcnt vmcnt(12)\n"
  "v_xor_b32 v200, v34, v200\n"
  "v_xor_b32 v202, v35, v202\n"
  "v_mfma_f32_16x16x32_bf16 v[116:119], a[140:143], v[196:199], v[116:119]\n"
  "buffer_load_dwordx4 v[236:239], v171, s[4:7], s69 offen offset:2048 sc1\n"
  "v_mfma_f32_16x16x32_bf16 v[112:115], a[136:139], v[196:199], v[112:115]\n"
  "s_waitcnt vmcnt(12)\n"
  "v_xor_b32 v204, v34, v204\n"
  "v_xor_b32 v206, v35, v206\n"
  "v_mfma_f32_16x16x32_bf16 v[104:107], a[136:139], v[192:195], v[104:107]\n"
  "buffer_load_dwordx4 v[240:243], v170, s[4:7], s77 offen offset:0 sc1\n"
  "ds_read_b64_tr_b16 a[56:57], v54 offset:0\n"
  "ds_read_b64_tr_b16 a[58:59], v54 offset:512\n"
  "ds_read_b64_tr_b16 a[60:61], v55 offset:0\n"
  "ds_read_b64_tr_b16 a[62:63], v55 offset:512\n"
  "v_mfma_f32_16x16x32_bf16 v[92:95], a[132:135], v[192:195], v[92:95]\n"
  "v_or3_b32 v52, v200, v202, v204\n"
  "v_bitop3_b32 v52, v52, s33, v206 bitop3:0xc8\n"
  "v_mfma_f32_16x16x32_bf16 v[100:103], a[132:135], v[196:199], v[100:103]\n"
  "v_cmp_ne_u32 vcc, 0, v52\n"
  "buffer_load_dwordx4 v19, s[12:15], s66 offen offset:3072 lds\n"
  "v_mfma_f32_16x16x32_bf16 v[96:99], a[128:131], v[196:199], v[96:99]\n"
  "v_mfma_f32_16x16x32_bf16 v[88:91], a[128:131], v[192:195], v[88:91]\n"
  "s_cbranch_vccnz Lslow27\n"
  "Lb27:\n"
  "buffer_load_dwordx4 v[36:39], v168, s[4:7], s74 offen sc1\n"
  "buffer_load_dwordx4 v[40:43], v170, s[4:7], s74 offen sc1\n"
  "buffer_load_dwordx4 v[176:179], v32, s[4:7], s50 offen offset:0 sc1\n"
  "buffer_load_dwordx4 v[180:183], v32, s[4:7], s50 offen offset:256 sc1\n"
  "v_mfma_f32_16x16x32_bf16 v[108:111], a[156:159], v[200:203], v[108:111]\n"
  "s_waitcnt vmcnt(16)\n"
  "v_xor_b32 v208, v34, v208\n"
  "v_xor_b32 v210, v35, v210\n"
  "v_mfma_f32_16x16x32_bf16 v[116:119], a[156:159], v[204:207], v[116:119]\n"
  "v_mfma_f32_16x16x32_bf16 v[112:115], a[152:155], v[204:207], v[112:115]\n"
  "s_waitcnt vmcnt(15)\n"
  "v_xor_b32 v212, v34, v212\n"
  "v_xor_b32 v214, v35, v214\n"
  "v_mfma_f32_16x16x32_bf16 v[104:107], a[152:155], v[200:203], v[104:107]\n"
  "ds_read_b64_tr_b16 a[64:65], v23 offset:16384\n"
  "ds_read_b64_tr_b16 a[66:67], v23 offset:16896\n"
  "ds_read_b64_tr_b16 a[68:69], v53 offset:16384\n"
  "ds_read_b64_tr_b16 a[70:71], v53 offset:16896\n"
  "v_mfma_f32_16x16x32_bf16 v[92:95], a[148:151], v[200:203], v[92:95]\n"
  "v_or3_b32 v52, v208, v210, v212\n"
  "v_bitop3_b32 v52, v52, s33, v214 bitop3:0xc8\n"
  "v_mfma_f32_16x16x32_bf16 v[100:103], a[148:151], v[204:207], v[100:103]\n"
  "v_cmp_ne_u32 vcc, 0, v52\n"
  "v_mfma_f32_16x16x32_bf16 v[96:99], a[144:147], v[204:207], v[96:99]\n"
  "v_mfma_f32_16x16x32_bf16 v[88:91], a[144:147], v[200:203], v[88:91]\n"
  "s_cbranch_vccnz Lslow28\n"
  "Lb28:\n"
  "v_mfma_f32_16x16x32_bf16 v[108:111], a[172:175], v[208:211], v[108:111]\n"
  "s_waitcnt vmcnt(13)\n"
  "v_xor_b32 v216, v34, v216\n"
  "v_xor_b32 v218, v35, v218\n"
  "v_mfma_f32_16x16x32_bf16 v[116:119], a[172:175], v[212:215], v[116:119]\n"
  "buffer_load_dwordx4 v[244:247], v171, s[4:7], s77 offen offset:0 sc1\n"
  "v_mfma_f32_16x16x32_bf16 v[112:115], a[168:171], v[212:215], v[112:115]\n"
  "s_waitcnt vmcnt(12)\n"
  "v_xor_b32 v220, v34, v220\n"
  "v_xor_b32 v222, v35, v222\n"
  "v_mfma_f32_16x16x32_bf16 v[104:107], a[168:171], v[208:211], v[104:107]\n"
  "buffer_load_dwordx4 v[248:251], v170, s[4:7], s77 offen offset:2048 sc1\n"
  "ds_read_b64_tr_b16 a[72:73], v54 offset:16384\n"
  "ds_read_b64_tr_b16 a[74:75], v54 offset:16896\n"
  "ds_read_b64_tr_b16 a[76:77], v55 offset:16384\n"
  "ds_read_b64_tr_b16 a[78:79], v55 offset:16896\n"
  "v_mfma_f32_16x16x32_bf16 v[92:95], a[164:167], v[208:211], v[92:95]\n"
  "v_or3_b32 v52, v216, v218, v220\n"
  "v_bitop3_b32 v52, v52, s33, v222 bitop3:0xc8\n"
  "v_mfma_f32_16x16x32_bf16 v[100:103], a[164:167], v[212:215], v[100:103]\n"
  "v_cmp_ne_u32 vcc, 0, v52\n"
  "v_mfma_f32_16x16x32_bf16 v[96:99], a[160:163], v[212:215], v[96:99]\n"
  "v_mfma_f32_16x16x32_bf16 v[88:91], a[160:163], v[208:211], v[88:91]\n"
  "s_cbranch_vccnz Lslow29\n"
  "Lb29:\n"
  "v_mfma_f32_16x16x32_bf16 v[108:111], a[188:191], v[216:219], v[108:111]\n"
  "s_waitcnt vmcnt(12)\n"
  "v_xor_b32 v224, v34, v224\n"
  "v_xor_b32 v226, v35, v226\n"
  "v_mfma_f32_16x16x32_bf16 v[116:119], a[188:191], v[220:223], v[116:119]\n"
  "buffer_load_dwordx4 v[252:255], v171, s[4:7], s77 offen offset:2048 sc1\n"
  "v_mfma_f32_16x16x32_bf16 v[112:115], a[184:187], v[220:223], v[112:115]\n"
  "s_waitcnt vmcnt(11)\n"
  "v_xor_b32 v228, v34, v228\n"
  "v_xor_b32 v230, v35, v230\n"
  "v_mfma_f32_16x16x32_bf16 v[104:107], a[184:187], v[216:219], v[104:107]\n"
  "s_mov_b32 m0, s20\n"
  "s_add_i32 s66, s66, 0x20000\n"
  "s_waitcnt lgkmcnt(8)\n"
  "buffer_load_dwordx4 v16, s[12:15], s66 offen offset:0 lds\n"
  "ds_read_b64_tr_b16 a[80:81], v23 offset:32768\n"
  "ds_read_b64_tr_b16 a[82:83], v23 offset:33280\n"
  "ds_read_b64_tr_b16 a[84:85], v53 offset:32768\n"
  "ds_read_b64_tr_b16 a[86:87], v53 offset:33280\n"
  "v_mfma_f32_16x16x32_bf16 v[92:95], a[180:183], v[216:219], v[92:95]\n"
  "v_or3_b32 v52, v224, v226, v228\n"
  "v_bitop3_b32 v52, v52, s33, v230 bitop3:0xc8\n"
  "v_mfma_f32_16x16x32_bf16 v[100:103], a[180:183], v[220:223], v[100:103]\n"
  "v_cmp_ne_u32 vcc, 0, v52\n"
  "buffer_load_dwordx4 v17, s[12:15], s66 offen offset:1024 lds\n"
  "v_mfma_f32_16x16x32_bf16 v[96:99], a[176:179], v[220:223], v[96:99]\n"
  "v_mfma_f32_16x16x32_bf16 v[88:91], a[176:179], v[216:219], v[88:91]\n"
  "s_cbranch_vccnz Lslow30\n"
  "Lb30:\n"
  "v_mfma_f32_16x16x32_bf16 v[108:111], a[204:207], v[224:227], v[108:111]\n"
  "s_waitcnt vmcnt(12)\n"
  "v_xor_b32 v232, v34, v232\n"
  "v_xor_b32 v234, v35, v234\n"
  "v_mfma_f32_16x16x32_bf16 v[116:119], a[204:207], v[228:231], v[116:119]\n"
  "buffer_load_dwordx4 v18, s[12:15], s66 offen offset:2048 lds\n"
  "v_mfma_f32_16x16x32_bf16 v[112:115], a[200:203], v[228:231], v[112:115]\n"
  "s_waitcnt vmcnt(12)\n"
  "v_xor_b32 v236, v34, v236\n"
  "v_xor_b32 v238, v35, v238\n"
  "v_mfma_f32_16x16x32_bf16 v[104:107], a[200:203], v[224:227], v[104:107]\n"
  "buffer_load_dwordx4 v19, s[12:15], s66 offen offset:3072 lds\n"
  "ds_read_b64_tr_b16 a[88:89], v54 offset:32768\n"
  "ds_read_b64_tr_b16 a[90:91], v54 offset:33280\n"
  "ds_read_b64_tr_b16 a[92:93], v55 offset:32768\n"
  "ds_read_b64_tr_b16 a[94:95], v55 offset:33280\n"
  "v_mfma_f32_16x16x32_bf16 v[92:95], a[196:199], v[224:227], v[92:95]\n"
  "v_or3_b32 v52, v232, v234, v236\n"
  "v_bitop3_b32 v52, v52, s33, v238 bitop3:0xc8\n"
  "v_mfma_f32_16x16x32_bf16 v[100:103], a[196:199], v[228:231], v[100:103]\n"
  "v_cmp_ne_u32 vcc, 0, v52\n"
  "v_mfma_f32_16x16x32_bf16 v[96:99], a[192:195], v[228:231], v[96:99]\n"
  "v_mfma_f32_16x16x32_bf16 v[88:91], a[192:195], v[224:227], v[88:91]\n"
  "s_cbranch_vccnz Lslow31\n"
  "Lb31:\n"
  "v_mfma_f32_16x16x32_bf16 v[108:111], a[220:223], v[232:235], v[108:111]\n"
  "s_waitcnt vmcnt(12)\n"
  "v_xor_b32 v240, v34, v240\n"
  "v_xor_b32 v242, v35, v242\n"
  "v_mfma_f32_16x16x32_bf16 v[116:119], a[220:223], v[236:239], v[116:119]\n"
  "s_mov_b32 m0, s23\n"
  "s_add_i32 s66, s65, 0x500000\n"
  "buffer_load_dwordx4 v16, s[12:15], s66 offen offset:0 lds\n"
  "v_mfma_f32_16x16x32_bf16 v[112:115], a[216:219], v[236:239], v[112:115]\n"
  "s_waitcnt vmcnt(7)\n"
  "v_xor_b32 v244, v34, v244\n"
  "v_xor_b32_e64 v246, v35, v246\n"
  "v_mfma_f32_16x16x32_bf16 v[104:107], a[216:219], v[232:235], v[104:107]\n"
  "buffer_load_dwordx4 v17, s[12:15], s66 offen offset:1024 lds\n"
  "ds_read_b64_tr_b16 a[96:97], v23 offset:49152\n"
  "ds_read_b64_tr_b16 a[98:99], v23 offset:49664\n"
  "ds_read_b64_tr_b16 a[100:101], v53 offset:49152\n"
  "ds_read_b64_tr_b16 a[102:103], v53 offset:49664\n"
  "v_mfma_f32_16x16x32_bf16 v[92:95], a[212:215], v[232:235], v[92:95]\n"
  "v_or3_b32 v52, v240, v242, v244\n"
  "v_bitop3_b32 v52, v52, s33, v246 bitop3:0xc8\n"
  "v_mfma_f32_16x16x32_bf16 v[100:103], a[212:215], v[236:239], v[100:103]\n"
  "v_cmp_ne_u32 vcc, 0, v52\n"
  "buffer_load_dwordx4 v18, s[12:15], s66 offen offset:2048 lds\n"
  "v_mfma_f32_16x16x32_bf16 v[96:99], a[208:211], v[236:239], v[96:99]\n"
  "v_mfma_f32_16x16x32_bf16 v[88:91], a[208:211], v[232:235], v[88:91]\n"
  "buffer_load_dwordx4 v19, s[12:15], s66 offen offset:3072 lds\n"
  "s_mov_b32 m0, s24\n"
  "s_add_i32 s66, s66, 0x20000\n"
  "buffer_load_dwordx4 v16, s[12:15], s66 offen offset:0 lds\n"
  "s_cbranch_vccnz Lslow32\n"
  "Lb32:\n"
  "v_mfma_f32_16x16x32_bf16 v[108:111], a[236:239], v[240:243], v[108:111]\n"
  "s_waitcnt vmcnt(10)\n"
  "v_xor_b32 v248, v34, v248\n"
  "v_xor_b32 v250, v35, v250\n"
  "v_mfma_f32_16x16x32_bf16 v[116:119], a[236:239], v[244:247], v[116:119]\n"
  "buffer_load_dwordx4 v[192:195], v172, s[4:7], s73 offen offset:0 sc1\n"
  "v_mfma_f32_16x16x32_bf16 v[112:115], a[232:235], v[244:247], v[112:115]\n"
  "s_waitcnt vmcnt(10)\n"
  "v_xor_b32 v252, v34, v252\n"
  "v_xor_b32_e64 v254, v35, v254\n"
  "v_mfma_f32_16x16x32_bf16 v[104:107], a[232:235], v[240:243], v[104:107]\n"
  "buffer_load_dwordx4 v[196:199], v173, s[4:7], s73 offen offset:0 sc1\n"
  "ds_read_b64_tr_b16 a[104:105], v54 offset:49152\n"
  "ds_read_b64_tr_b16 a[106:107], v54 offset:49664\n"
  "ds_read_b64_tr_b16 a[108:109], v55 offset:49152\n"
  "ds_read_b64_tr_b16 a[110:111], v55 offset:49664\n"
  "v_mfma_f32_16x16x32_bf16 v[92:95], a[228:231], v[240:243], v[92:95]\n"
  "v_or3_b32 v52, v248, v250, v252\n"
  "v_bitop3_b32 v52, v52, s33, v254 bitop3:0xc8\n"
  "v_mfma_f32_16x16x32_bf16 v[100:103], a[228:231], v[244:247], v[100:103]\n"
  "v_cmp_ne_u32 vcc, 0, v52\n"
  "buffer_load_dwordx4 v[200:203], v172, s[4:7], s73 offen offset:2048 sc1\n"
  "v_mfma_f32_16x16x32_bf16 v[96:99], a[224:227], v[244:247], v[96:99]\n"
  "v_mfma_f32_16x16x32_bf16 v[88:91], a[224:227], v[240:243], v[88:91]\n"
  "buffer_load_dwordx4 v[204:207], v173, s[4:7], s73 offen offset:2048 sc1\n"
  "buffer_load_dwordx4 v17, s[12:15], s66 offen offset:1024 lds\n"
  "s_cbranch_vccnz Lslow33\n"
  "Lb33:\n"
  "v_xor_b32 v183, s49, v183\n"
  "v_xor_b32 v181, s48, v181\n"
  "v_xor_b32 v179, s49, v179\n"
  "v_xor_b32 v177, s48, v177\n"
  "v_xor_b32 v40, v34, v40\n"
  "v_xor_b32 v42, v35, v42\n"
  "v_xor_b32 v36, v34, v36\n"
  "v_xor_b32 v38, v35, v38\n"
  "v_or3_b32 v52, v40, v42, v36\n"
  "v_bitop3_b32 v52, v52, s33, v38 bitop3:0xc8\n"
  "v_or3_b32 v52, v52, v177, v179\n"
  "v_or3_b32 v52, v52, v181, v183\n"
  "v_cmp_ne_u32 vcc, 0, v52\n"
  "s_cbranch_vccnz Lslow34\n"
  "LbackHa:\n"
  "v_mfma_f32_16x16x32_bf16 v[56:59], v[36:39], v[160:163], 0\n"
  "v_mfma_f32_16x16x32_bf16 v[60:63], v[36:39], v[164:167], 0\n"
  "v_mfma_f32_16x16x32_bf16 v[108:111], a[252:255], v[248:251], v[108:111]\n"
  "v_mfma_f32_16x16x32_bf16 v[116:119], a[252:255], v[252:255], v[116:119]\n"
  "buffer_load_dwordx4 v[208:211], v172, s[4:7], s0 offen offset:0 sc1\n"
  "v_mfma_f32_16x16x32_bf16 v[112:115], a[248:251], v[252:255], v[112:115]\n"
  "v_mfma_f32_16x16x32_bf16 v[104:107], a[248:251], v[248:251], v[104:107]\n"
  "buffer_load_dwordx4 v[212:215], v173, s[4:7], s0 offen offset:0 sc1\n"
  "ds_read_b64_tr_b16 a[112:113], v8 offset:32768\n"
  "ds_read_b64_tr_b16 a[114:115], v8 offset:33280\n"
  "ds_read_b64_tr_b16 a[116:117], v9 offset:32768\n"
  "ds_read_b64_tr_b16 a[118:119], v9 offset:33280\n"
  "v_mfma_f32_16x16x32_bf16 v[92:95], a[244:247], v[248:251], v[92:95]\n"
  "v_mfma_f32_16x16x32_bf16 v[100:103], a[244:247], v[252:255], v[100:103]\n"
  "buffer_load_dwordx4 v[216:219], v172, s[4:7], s0 offen offset:2048 sc1\n"
  "v_mfma_f32_16x16x32_bf16 v[96:99], a[240:243], v[252:255], v[96:99]\n"
  "v_mfma_f32_16x16x32_bf16 v[88:91], a[240:243], v[248:251], v[88:91]\n"
  "buffer_load_dwordx4 v18, s[12:15], s66 offen offset:2048 lds\n"
  "buffer_load_dwordx4 v[220:223], v173, s[4:7], s0 offen offset:2048 sc1\n"
  "v_fma_f32 v56, v56, v176, v178\n"
  "v_fma_f32 v57, v57, v176, v178\n"
  "v_fma_f32 v58, v58, v176, v178\n"
  "v_fma_f32 v59, v59, v176, v178\n"
  "v_fma_f32 v60, v60, v180, v182\n"
  "v_fma_f32 v61, v61, v180, v182\n"
  "v_fma_f32 v62, v62, v180, v182\n"
  "v_fma_f32 v63, v63, v180, v182\n"
  "ds_write_b128 v21, v[108:111] offset:1024\n"
  "buffer_load_dwordx4 v[224:227], v172, s[4:7], s1 offen offset:0 sc1\n"
  "ds_write_b128 v21, v[104:107] offset:0\n"
  "ds_write_b128 v22, v[116:119] offset:1024\n"
  "ds_write_b128 v22, v[112:115] offset:0\n"
  "ds_write_b128 v20, v[100:103] offset:1024\n"
  "ds_write_b128 v20, v[96:99] offset:0\n"
  "s_waitcnt lgkmcnt(0)\n"
  "s_barrier\n"
  "ds_read_b128 v[120:123], v26 offset:0\n"
  "buffer_load_dwordx4 v[228:231], v173, s[4:7], s1 offen offset:0 sc1\n"
  "ds_read_b128 v[128:131], v26 offset:2048\n"
  "ds_read_b128 v[136:139], v26 offset:4096\n"
  "ds_read_b128 v[124:127], v26 offset:1024\n"
  "ds_read_b128 v[132:135], v26 offset:3072\n"
  "ds_read_b128 v[140:143], v26 offset:5120\n"
  "s_waitcnt lgkmcnt(3)\n"
  "v_pk_add_f32 v[136:137], v[128:129], v[136:137]\n"
  "v_pk_add_f32 v[138:139], v[130:131], v[138:139]\n"
  "v_pk_add_f32 v[120:121], v[120:121], v[88:89]\n"
  "buffer_load_dwordx4 v[232:235], v172, s[4:7], s1 offen offset:2048 sc1\n"
  "v_pk_add_f32 v[122:123], v[122:123], v[90:91]\n"
  "v_pk_add_f32 v[120:121], v[120:121], v[136:137]\n"
  "v_pk_add_f32 v[122:123], v[122:123], v[138:139]\n"
  "v_pk_fma_f32 v[144:145], v[120:121], s[34:35], v[44:45] op_sel_hi:[1,0,1]\n"
  "v_pk_fma_f32 v[146:147], v[122:123], s[34:35], v[46:47] op_sel_hi:[1,0,1]\n"
  "s_waitcnt lgkmcnt(0)\n"
  "v_pk_add_f32 v[140:141], v[132:133], v[140:141]\n"
  "v_pk_add_f32 v[142:143], v[134:135], v[142:143]\n"
  "v_pk_add_f32 v[124:125], v[124:125], v[92:93]\n"
  "buffer_load_dwordx4 v[236:239], v173, s[4:7], s1 offen offset:2048 sc1\n"
  "v_pk_add_f32 v[126:127], v[126:127], v[94:95]\n"
  "v_pk_add_f32 v[124:125], v[124:125], v[140:141]\n"
  "v_pk_add_f32 v[126:127], v[126:127], v[142:143]\n"
  "v_pk_fma_f32 v[148:149], v[124:125], s[34:35], v[48:49] op_sel_hi:[1,0,1]\n"
  "v_pk_fma_f32 v[150:151], v[126:127], s[34:35], v[50:51] op_sel_hi:[1,0,1]\n"
  "v_pk_mul_f32 v[152:153], v[144:145], v[144:145]\n"
  "v_pk_mul_f32 v[154:155], v[146:147], v[146:147]\n"
  "v_pk_mul_f32 v[156:157], v[148:149], v[148:149]\n"
  "buffer_load_dwordx4 v[240:243], v172, s[4:7], s3 offen offset:0 sc1\n"
  "v_pk_mul_f32 v[158:159], v[150:151], v[150:151]\n"
  "v_pk_fma_f32 v[152:153], v[152:153], s[82:83], v[28:29] op_sel_hi:[1,0,0]\n"
  "v_pk_fma_f32 v[154:155], v[154:155], s[82:83], v[28:29] op_sel_hi:[1,0,0]\n"
  "v_pk_fma_f32 v[156:157], v[156:157], s[82:83], v[28:29] op_sel_hi:[1,0,0]\n"
  "v_pk_fma_f32 v[158:159], v[158:159], s[82:83], v[28:29] op_sel_hi:[1,0,0]\n"
  "v_pk_mul_f32 v[152:153], v[152:153], v[144:145]\n"
  "v_pk_mul_f32 v[154:155], v[154:155], v[146:147]\n"
  "v_pk_mul_f32 v[156:157], v[156:157], v[148:149]\n"
  "buffer_load_dwordx4 v[244:247], v173, s[4:7], s3 offen offset:0 sc1\n"
  "v_pk_mul_f32 v[158:159], v[158:159], v[150:151]\n"
  "v_exp_f32 v152, v152\n"
  "v_exp_f32 v153, v153\n"
  "v_exp_f32 v154, v154\n"
  "v_exp_f32 v155, v155\n"
  "v_exp_f32 v156, v156\n"
  "v_exp_f32 v157, v157\n"
  "v_exp_f32 v158, v158\n"
  "buffer_load_dwordx4 v[248:251], v172, s[4:7], s3 offen offset:2048 sc1\n"
  "v_exp_f32 v159, v159\n"
  "v_pk_fma_f32 v[152:153], v[152:153], s[32:33], s[32:33] op_sel_hi:[1,0,0]\n"
  "v_pk_fma_f32 v[154:155], v[154:155], s[32:33], s[32:33] op_sel_hi:[1,0,0]\n"
  "v_pk_fma_f32 v[156:157], v[156:157], s[32:33], s[32:33] op_sel_hi:[1,0,0]\n"
  "v_pk_fma_f32 v[158:159], v[158:159], s[32:33], s[32:33] op_sel_hi:[1,0,0]\n"
  "v_rcp_f32 v152, v152\n"
  "v_rcp_f32 v153, v153\n"
  "v_rcp_f32 v154, v154\n"
  "buffer_load_dwordx4 v[252:255], v173, s[4:7], s3 offen offset:2048 sc1\n"
  "v_rcp_f32 v155, v155\n"
  "v_rcp_f32 v156, v156\n"
  "v_rcp_f32 v157, v157\n"
  "v_rcp_f32 v158, v158\n"
  "v_rcp_f32 v159, v159\n"
  "v_pk_mul_f32 v[152:153], v[152:153], v[144:145]\n"
  "v_pk_mul_f32 v[154:155], v[154:155], v[146:147]\n"
  "v_pk_mul_f32 v[156:157], v[156:157], v[148:149]\n"
  "v_pk_mul_f32 v[158:159], v[158:159], v[150:151]\n"
  "v_cvt_pk_bf16_f32 v144, v152, v153\n"
  "v_cvt_pk_bf16_f32 v145, v154, v155\n"
  "v_cvt_pk_bf16_f32 v146, v156, v157\n"
  "v_cvt_pk_bf16_f32 v147, v158, v159\n"
  "v_or_b32 v144, v34, v144\n"
  "v_or_b32 v146, v35, v146\n"
  "buffer_store_dwordx4 v[144:147], v1, s[4:7], s71 offen sc1\n"
  "s_waitcnt vmcnt(18)\n"
  "v_xor_b32 v192, v34, v192\n"
  "v_xor_b32 v194, v35, v194\n"
  "s_waitcnt vmcnt(17)\n"
  "v_xor_b32 v196, v34, v196\n"
  "v_xor_b32 v198, v35, v198\n"
  "v_or3_b32 v52, v192, v194, v196\n"
  "v_bitop3_b32 v52, v52, s33, v198 bitop3:0xc8\n"
  "v_cmp_ne_u32 vcc, 0, v52\n"
  "s_cbranch_vccnz Lslow35\n"
  "Lb35:\n"
  "v_mfma_f32_16x16x32_bf16 v[76:79], v[192:195], a[12:15], 0\n"
  "s_waitcnt vmcnt(16)\n"
  "v_xor_b32 v200, v34, v200\n"
  "v_xor_b32 v202, v35, v202\n"
  "v_mfma_f32_16x16x32_bf16 v[84:87], v[196:199], a[12:15], 0\n"
  "v_mfma_f32_16x16x32_bf16 v[80:83], v[196:199], a[8:11], 0\n"
  "s_waitcnt vmcnt(15)\n"
  "v_xor_b32 v204, v34, v204\n"
  "v_xor_b32 v206, v35, v206\n"
  "v_mfma_f32_16x16x32_bf16 v[72:75], v[192:195], a[8:11], 0\n"
  "ds_read_b64_tr_b16 a[120:121], v10 offset:32768\n"
  "ds_read_b64_tr_b16 a[122:123], v10 offset:33280\n"
  "ds_read_b64_tr_b16 a[124:125], v11 offset:32768\n"
  "ds_read_b64_tr_b16 a[126:127], v11 offset:33280\n"
  "v_mfma_f32_16x16x32_bf16 v[60:63], v[192:195], a[4:7], v[60:63]\n"
  "v_or3_b32 v52, v200, v202, v204\n"
  "v_bitop3_b32 v52, v52, s33, v206 bitop3:0xc8\n"
  "v_mfma_f32_16x16x32_bf16 v[68:71], v[196:199], a[4:7], 0\n"
  "v_cmp_ne_u32_e64 vcc, 0, v52\n"
  "v_mfma_f32_16x16x32_bf16 v[64:67], v[196:199], a[0:3], 0\n"
  "ds_read_b64_tr_b16 a[128:129], v8 offset:16384\n"
  "ds_read_b64_tr_b16 a[130:131], v8 offset:16896\n"
  "ds_read_b64_tr_b16 a[132:133], v9 offset:16384\n"
  "ds_read_b64_tr_b16 a[134:135], v9 offset:16896\n"
  "v_mfma_f32_16x16x32_bf16 v[56:59], v[192:195], a[0:3], v[56:59]\n"
  "s_cbranch_vccnz Lslow36\n"
  "Lb36:\n"
  "s_barrier\n"
  "v_mfma_f32_16x16x32_bf16 v[76:79], v[200:203], a[28:31], v[76:79]\n"
  "s_waitcnt vmcnt(13)\n"
  "v_xor_b32 v208, v34, v208\n"
  "v_xor_b32 v210, v35, v210\n"
  "v_mfma_f32_16x16x32_bf16 v[84:87], v[204:207], a[28:31], v[84:87]\n"
  "v_mfma_f32_16x16x32_bf16 v[80:83], v[204:207], a[24:27], v[80:83]\n"
  "s_waitcnt vmcnt(12)\n"
  "v_xor_b32 v212, v34, v212\n"
  "v_xor_b32 v214, v35, v214\n"
  "v_mfma_f32_16x16x32_bf16 v[72:75], v[200:203], a[24:27], v[72:75]\n"
  "ds_read_b64_tr_b16 a[136:137], v10 offset:16384\n"
  "ds_read_b64_tr_b16 a[138:139], v10 offset:16896\n"
  "ds_read_b64_tr_b16 a[140:141], v11 offset:16384\n"
  "ds_read_b64_tr_b16 a[142:143], v11 offset:16896\n"
  "v_mfma_f32_16x16x32_bf16 v[60:63], v[200:203], a[20:23], v[60:63]\n"
  "v_or3_b32 v52, v208, v210, v212\n"
  "v_bitop3_b32 v52, v52, s33, v214 bitop3:0xc8\n"
  "v_mfma_f32_16x16x32_bf16 v[68:71], v[204:207], a[20:23], v[68:71]\n"
  "v_cmp_ne_u32_e64 vcc, 0, v52\n"
  "v_mfma_f32_16x16x32_bf16 v[64:67], v[204:207], a[16:19], v[64:67]\n"
  "ds_read_b64_tr_b16 a[144:145], v8 offset:0\n"
  "ds_read_b64_tr_b16 a[146:147], v8 offset:512\n"
  "ds_read_b64_tr_b16 a[148:149], v9 offset:0\n"
  "ds_read_b64_tr_b16 a[150:151], v9 offset:512\n"
  "v_mfma_f32_16x16x32_bf16 v[56:59], v[200:203], a[16:19], v[56:59]\n"
  "s_cbranch_vccnz Lslow37\n"
  "Lb37:\n"
  "v_mfma_f32_16x16x32_bf16 v[76:79], v[208:211], a[44:47], v[76:79]\n"
  "s_waitcnt vmcnt(11)\n"
  "v_xor_b32 v216, v34, v216\n"
  "v_xor_b32 v218, v35, v218\n"
  "v_mfma_f32_16x16x32_bf16 v[84:87], v[212:215], a[44:47], v[84:87]\n"
  "buffer_load_dwordx4 v[192:195], v172, s[4:7], s94 offen offset:0 sc1\n"
  "v_mfma_f32_16x16x32_bf16 v[80:83], v[212:215], a[40:43], v[80:83]\n"
  "s_waitcnt vmcnt(10)\n"
  "v_xor_b32 v220, v34, v220\n"
  "v_xor_b32_e64 v222, v35, v222\n"
  "v_mfma_f32_16x16x32_bf16 v[72:75], v[208:211], a[40:43], v[72:75]\n"
  "buffer_load_dwordx4 v[196:199], v173, s[4:7], s94 offen offset:0 sc1\n"
  "ds_read_b64_tr_b16 a[152:153], v10 offset:0\n"
  "ds_read_b64_tr_b16 a[154:155], v10 offset:512\n"
  "ds_read_b64_tr_b16 a[156:157], v11 offset:0\n"
  "ds_read_b64_tr_b16 a[158:159], v11 offset:512\n"
  "v_mfma_f32_16x16x32_bf16 v[60:63], v[208:211], a[36:39], v[60:63]\n"
  "v_or3_b32 v52, v216, v218, v220\n"
  "v_bitop3_b32 v52, v52, s33, v222 bitop3:0xc8\n"
  "v_mfma_f32_16x16x32_bf16 v[68:71], v[212:215], a[36:39], v[68:71]\n"
  "v_cmp_ne_u32_e64 vcc, 0, v52\n"
  "buffer_load_dwordx4 v19, s[12:15], s66 offen offset:3072 lds\n"
  "v_mfma_f32_16x16x32_bf16 v[64:67], v[212:215], a[32:35], v[64:67]\n"
  "ds_read_b64_tr_b16 a[160:161], v8 offset:49152\n"
  "ds_read_b64_tr_b16 a[162:163], v8 offset:49664\n"
  "ds_read_b64_tr_b16 a[164:165], v9 offset:49152\n"
  "ds_read_b64_tr_b16 a[166:167], v9 offset:49664\n"
  "v_mfma_f32_16x16x32_bf16 v[56:59], v[208:211], a[32:35], v[56:59]\n"
  "s_cbranch_vccnz Lslow38\n"
  "Lb38:\n"
  "v_mfma_f32_16x16x32_bf16 v[88:91], v[40:43], v[160:163], 0\n"
  "v_mfma_f32_16x16x32_bf16 v[92:95], v[40:43], v[164:167], 0\n"
  "v_mfma_f32_16x16x32_bf16 v[76:79], v[216:219], a[60:63], v[76:79]\n"
  "s_waitcnt vmcnt(11)\n"
  "v_xor_b32 v224, v34, v224\n"
  "v_xor_b32 v226, v35, v226\n"
  "v_mfma_f32_16x16x32_bf16 v[84:87], v[220:223], a[60:63], v[84:87]\n"
  "buffer_load_dwordx4 v[200:203], v172, s[4:7], s94 offen offset:2048 sc1\n"
  "v_mfma_f32_16x16x32_bf16 v[80:83], v[220:223], a[56:59], v[80:83]\n"
  "s_waitcnt vmcnt(11)\n"
  "v_xor_b32 v228, v34, v228\n"
  "v_xor_b32_e64 v230, v35, v230\n"
  "v_mfma_f32_16x16x32_bf16 v[72:75], v[216:219], a[56:59], v[72:75]\n"
  "buffer_load_dwordx4 v[204:207], v173, s[4:7], s94 offen offset:2048 sc1\n"
  "ds_read_b64_tr_b16 a[168:169], v10 offset:49152\n"
  "ds_read_b64_tr_b16 a[170:171], v10 offset:49664\n"
  "ds_read_b64_tr_b16 a[172:173], v11 offset:49152\n"
  "ds_read_b64_tr_b16 a[174:175], v11 offset:49664\n"
  "v_mfma_f32_16x16x32_bf16 v[60:63], v[216:219], a[52:55], v[60:63]\n"
  "v_or3_b32 v52, v224, v226, v228\n"
  "v_bitop3_b32 v52, v52, s33, v230 bitop3:0xc8\n"
  "v_mfma_f32_16x16x32_bf16 v[68:71], v[220:223], a[52:55], v[68:71]\n"
  "v_cmp_ne_u32 vcc, 0, v52\n"
  "v_mfma_f32_16x16x32_bf16 v[64:67], v[220:223], a[48:51], v[64:67]\n"
  "v_mfma_f32_16x16x32_bf16 v[56:59], v[216:219], a[48:51], v[56:59]\n"
  "s_cbranch_vccnz Lslow39\n"
  "Lb39:\n"
  "v_fma_f32 v88, v88, v176, v178\n"
  "v_fma_f32 v89, v89, v176, v178\n"
  "v_fma_f32 v90, v90, v176, v178\n"
  "v_fma_f32 v91, v91, v176, v178\n"
  "v_fma_f32 v92, v92, v180, v182\n"
  "v_fma_f32 v93, v93, v180, v182\n"
  "v_fma_f32 v94, v94, v180, v182\n"
  "v_fma_f32 v95, v95, v180, v182\n"
  "v_mfma_f32_16x16x32_bf16 v[76:79], v[224:227], a[76:79], v[76:79]\n"
  "s_waitcnt vmcnt(11)\n"
  "v_xor_b32 v232, v34, v232\n"
  "v_xor_b32 v234, v35, v234\n"
  "v_mfma_f32_16x16x32_bf16 v[84:87], v[228:231], a[76:79], v[84:87]\n"
  "s_mov_b32 m0, s25\n"
  "s_add_i32 s66, s65, 0x600000\n"
  "buffer_load_dwordx4 v16, s[12:15], s66 offen offset:0 lds\n"
  "v_mfma_f32_16x16x32_bf16 v[80:83], v[228:231], a[72:75], v[80:83]\n"
  "s_waitcnt vmcnt(11)\n"
  "v_xor_b32 v236, v34, v236\n"
  "v_xor_b32 v238, v35, v238\n"
  "v_mfma_f32_16x16x32_bf16 v[72:75], v[224:227], a[72:75], v[72:75]\n"
  "buffer_load_dwordx4 v[208:211], v172, s[4:7], s95 offen offset:0 sc1\n"
  "v_mfma_f32_16x16x32_bf16 v[60:63], v[224:227], a[68:71], v[60:63]\n"
  "v_or3_b32 v52, v232, v234, v236\n"
  "v_bitop3_b32 v52, v52, s33, v238 bitop3:0xc8\n"
  "v_mfma_f32_16x16x32_bf16 v[68:71], v[228:231], a[68:71], v[68:71]\n"
  "v_cmp_ne_u32 vcc, 0, v52\n"
  "v_mfma_f32_16x16x32_bf16 v[64:67], v[228:231], a[64:67], v[64:67]\n"
  "v_mfma_f32_16x16x32_bf16 v[56:59], v[224:227], a[64:67], v[56:59]\n"
  "s_cbranch_vccnz Lslow40\n"
  "Lb40:\n"
  "v_mfma_f32_16x16x32_bf16 v[76:79], v[232:235], a[92:95], v[76:79]\n"
  "s_waitcnt vmcnt(11)\n"
  "v_xor_b32 v240, v34, v240\n"
  "v_xor_b32 v242, v35, v242\n"
  "v_mfma_f32_16x16x32_bf16 v[84:87], v[236:239], a[92:95], v[84:87]\n"
  "buffer_load_dwordx4 v[212:215], v173, s[4:7], s95 offen offset:0 sc1\n"
  "v_mfma_f32_16x16x32_bf16 v[80:83], v[236:239], a[88:91], v[80:83]\n"
  "s_waitcnt vmcnt(11)\n"
  "v_xor_b32 v244, v34, v244\n"
  "v_xor_b32 v246, v35, v246\n"
  "v_mfma_f32_16x16x32_bf16 v[72:75], v[232:235], a[88:91], v[72:75]\n"
  "buffer_load_dwordx4 v17, s[12:15], s66 offen offset:1024 lds\n"
  "v_mfma_f32_16x16x32_bf16 v[60:63], v[232:235], a[84:87], v[60:63]\n"
  "v_or3_b32 v52, v240, v242, v244\n"
  "v_bitop3_b32 v52, v52, s33, v246 bitop3:0xc8\n"
  "v_mfma_f32_16x16x32_bf16 v[68:71], v[236:239], a[84:87], v[68:71]\n"
  "v_cmp_ne_u32 vcc, 0, v52\n"
  "v_mfma_f32_16x16x32_bf16 v[64:67], v[236:239], a[80:83], v[64:67]\n"
  "v_mfma_f32_16x16x32_bf16 v[56:59], v[232:235], a[80:83], v[56:59]\n"
  "s_cbranch_vccnz Lslow41\n"
  "Lb41:\n"
  "v_mfma_f32_16x16x32_bf16 v[76:79], v[240:243], a[108:111], v[76:79]\n"
  "s_waitcnt vmcnt(11)\n"
  "v_xor_b32 v248, v34, v248\n"
  "v_xor_b32 v250, v35, v250\n"
  "v_mfma_f32_16x16x32_bf16 v[84:87], v[244:247], a[108:111], v[84:87]\n"
  "buffer_load_dwordx4 v[216:219], v172, s[4:7], s95 offen offset:2048 sc1\n"
  "v_mfma_f32_16x16x32_bf16 v[80:83], v[244:247], a[104:107], v[80:83]\n"
  "s_waitcnt vmcnt(11)\n"
  "v_xor_b32 v252, v34, v252\n"
  "v_xor_b32 v254, v35, v254\n"
  "v_mfma_f32_16x16x32_bf16 v[72:75], v[240:243], a[104:107], v[72:75]\n"
  "buffer_load_dwordx4 v[220:223], v173, s[4:7], s95 offen offset:2048 sc1\n"
  "v_mfma_f32_16x16x32_bf16 v[60:63], v[240:243], a[100:103], v[60:63]\n"
  "v_or3_b32 v52, v248, v250, v252\n"
  "v_bitop3_b32 v52, v52, s33, v254 bitop3:0xc8\n"
  "v_mfma_f32_16x16x32_bf16 v[68:71], v[244:247], a[100:103], v[68:71]\n"
  "v_cmp_ne_u32 vcc, 0, v52\n"
  "buffer_load_dwordx4 v18, s[12:15], s66 offen offset:2048 lds\n"
  "v_mfma_f32_16x16x32_bf16 v[64:67], v[244:247], a[96:99], v[64:67]\n"
  "v_mfma_f32_16x16x32_bf16 v[56:59], v[240:243], a[96:99], v[56:59]\n"
  "s_cbranch_vccnz Lslow42\n"
  "Lb42:\n"
  "s_waitcnt lgkmcnt(0)\n"
  "v_mfma_f32_16x16x32_bf16 v[76:79], v[248:251], a[124:127], v[76:79]\n"
  "s_waitcnt vmcnt(11)\n"
  "v_xor_b32 v192, v34, v192\n"
  "v_xor_b32 v194, v35, v194\n"
  "v_mfma_f32_16x16x32_bf16 v[84:87], v[252:255], a[124:127], v[84:87]\n"
  "buffer_load_dwordx4 v19, s[12:15], s66 offen offset:3072 lds\n"
  "v_mfma_f32_16x16x32_bf16 v[80:83], v[252:255], a[120:123], v[80:83]\n"
  "s_waitcnt vmcnt(11)\n"
  "v_xor_b32 v196, v34, v196\n"
  "v_xor_b32 v198, v35, v198\n"
  "v_mfma_f32_16x16x32_bf16 v[72:75], v[248:251], a[120:123], v[72:75]\n"
  "buffer_load_dwordx4 v[224:227], v172, s[4:7], s30 offen offset:0 sc1\n"
  "v_mfma_f32_16x16x32_bf16 v[60:63], v[248:251], a[116:119], v[60:63]\n"
  "v_or3_b32 v52, v192, v194, v196\n"
  "v_bitop3_b32 v52, v52, s33, v198 bitop3:0xc8\n"
  "v_mfma_f32_16x16x32_bf16 v[68:71], v[252:255], a[116:119], v[68:71]\n"
  "v_cmp_ne_u32 vcc, 0, v52\n"
  "buffer_load_dwordx4 v[228:231], v173, s[4:7], s30 offen offset:0 sc1\n"
  "v_mfma_f32_16x16x32_bf16 v[64:67], v[252:255], a[112:115], v[64:67]\n"
  "v_mfma_f32_16x16x32_bf16 v[56:59], v[248:251], a[112:115], v[56:59]\n"
  "s_cbranch_vccnz Lslow43\n"
  "Lb43:\n"
  "v_mfma_f32_16x16x32_bf16 v[76:79], v[192:195], a[140:143], v[76:79]\n"
  "s_waitcnt vmcnt(11)\n"
  "v_xor_b32 v200, v34, v200\n"
  "v_xor_b32 v202, v35, v202\n"
  "v_mfma_f32_16x16x32_bf16 v[84:87], v[196:199], a[140:143], v[84:87]\n"
  "s_mov_b32 m0, s26\n"
  "s_add_i32 s66, s66, 0x20000\n"
  "buffer_load_dwordx4 v16, s[12:15], s66 offen offset:0 lds\n"
  "v_mfma_f32_16x16x32_bf16 v[80:83], v[196:199], a[136:139], v[80:83]\n"
  "s_waitcnt vmcnt(11)\n"
  "v_xor_b32 v204, v34, v204\n"
  "v_xor_b32 v206, v35, v206\n"
  "v_mfma_f32_16x16x32_bf16 v[72:75], v[192:195], a[136:139], v[72:75]\n"
  "buffer_load_dwordx4 v17, s[12:15], s66 offen offset:1024 lds\n"
  "v_mfma_f32_16x16x32_bf16 v[60:63], v[192:195], a[132:135], v[60:63]\n"
  "v_or3_b32 v52, v200, v202, v204\n"
  "v_bitop3_b32 v52, v52, s33, v206 bitop3:0xc8\n"
  "v_mfma_f32_16x16x32_bf16 v[68:71], v[196:199], a[132:135], v[68:71]\n"
  "v_cmp_ne_u32 vcc, 0, v52\n"
  "buffer_load_dwordx4 v18, s[12:15], s66 offen offset:2048 lds\n"
  "v_mfma_f32_16x16x32_bf16 v[64:67], v[196:199], a[128:131], v[64:67]\n"
  "v_mfma_f32_16x16x32_bf16 v[56:59], v[192:195], a[128:131], v[56:59]\n"
  "s_cbranch_vccnz Lslow44\n"
  "Lb44:\n"
  "v_mfma_f32_16x16x32_bf16 v[76:79], v[200:203], a[156:159], v[76:79]\n"
  "s_waitcnt vmcnt(11)\n"
  "v_xor_b32 v208, v34, v208\n"
  "v_xor_b32 v210, v35, v210\n"
  "v_mfma_f32_16x16x32_bf16 v[84:87], v[204:207], a[156:159], v[84:87]\n"
  "buffer_load_dwordx4 v19, s[12:15], s66 offen offset:3072 lds\n"
  "v_mfma_f32_16x16x32_bf16 v[80:83], v[204:207], a[152:155], v[80:83]\n"
  "s_waitcnt vmcnt(11)\n"
  "v_xor_b32 v212, v34, v212\n"
  "v_xor_b32_e64 v214, v35, v214\n"
  "v_mfma_f32_16x16x32_bf16 v[72:75], v[200:203], a[152:155], v[72:75]\n"
  "buffer_load_dwordx4 v[232:235], v172, s[4:7], s30 offen offset:2048 sc1\n"
  "ds_read_b64_tr_b16 a[176:177], v23 offset:0\n"
  "ds_read_b64_tr_b16 a[178:179], v23 offset:512\n"
  "ds_read_b64_tr_b16 a[180:181], v53 offset:0\n"
  "ds_read_b64_tr_b16 a[182:183], v53 offset:512\n"
  "v_mfma_f32_16x16x32_bf16 v[60:63], v[200:203], a[148:151], v[60:63]\n"
  "v_or3_b32 v52, v208, v210, v212\n"
  "v_bitop3_b32 v52, v52, s33, v214 bitop3:0xc8\n"
  "v_mfma_f32_16x16x32_bf16 v[68:71], v[204:207], a[148:151], v[68:71]\n"
  "v_cmp_ne_u32_e64 vcc, 0, v52\n"
  "buffer_load_dwordx4 v[236:239], v173, s[4:7], s30 offen offset:2048 sc1\n"
  "v_mfma_f32_16x16x32_bf16 v[64:67], v[204:207], a[144:147], v[64:67]\n"
  "ds_read_b64_tr_b16 a[184:185], v54 offset:0\n"
  "ds_read_b64_tr_b16 a[186:187], v54 offset:512\n"
  "ds_read_b64_tr_b16 a[188:189], v55 offset:0\n"
  "ds_read_b64_tr_b16 a[190:191], v55 offset:512\n"
  "v_mfma_f32_16x16x32_bf16 v[56:59], v[200:203], a[144:147], v[56:59]\n"
  "s_cbranch_vccnz Lslow45\n"
  "Lb45:\n"
  "v_mfma_f32_16x16x32_bf16 v[76:79], v[208:211], a[172:175], v[76:79]\n"
  "s_waitcnt vmcnt(11)\n"
  "v_xor_b32 v216, v34, v216\n"
  "v_xor_b32 v218, v35, v218\n"
  "v_mfma_f32_16x16x32_bf16 v[84:87], v[212:215], a[172:175], v[84:87]\n"
  "buffer_load_dwordx4 v[240:243], v174, s[4:7], s73 offen offset:0 sc1\n"
  "v_mfma_f32_16x16x32_bf16 v[80:83], v[212:215], a[168:171], v[80:83]\n"
  "s_waitcnt vmcnt(11)\n"
  "v_xor_b32 v220, v34, v220\n"
  "v_xor_b32 v222, v35, v222\n"
  "v_mfma_f32_16x16x32_bf16 v[72:75], v[208:211], a[168:171], v[72:75]\n"
  "buffer_load_dwordx4 v[244:247], v175, s[4:7], s73 offen offset:0 sc1\n"
  "s_waitcnt vmcnt(10)\n"
  "ds_read_b64_tr_b16 a[192:193], v23 offset:16384\n"
  "ds_read_b64_tr_b16 a[194:195], v23 offset:16896\n"
  "ds_read_b64_tr_b16 a[196:197], v53 offset:16384\n"
  "ds_read_b64_tr_b16 a[198:199], v53 offset:16896\n"
  "v_mfma_f32_16x16x32_bf16 v[60:63], v[208:211], a[164:167], v[60:63]\n"
  "v_or3_b32 v52, v216, v218, v220\n"
  "v_bitop3_b32 v52, v52, s33, v222 bitop3:0xc8\n"
  "v_mfma_f32_16x16x32_bf16 v[68:71], v[212:215], a[164:167], v[68:71]\n"
  "v_cmp_ne_u32_e64 vcc, 0, v52\n"
  "buffer_load_dwordx4 v[248:251], v174, s[4:7], s73 offen offset:2048 sc1\n"
  "v_mfma_f32_16x16x32_bf16 v[64:67], v[212:215], a[160:163], v[64:67]\n"
  "ds_read_b64_tr_b16 a[200:201], v54 offset:16384\n"
  "ds_read_b64_tr_b16 a[202:203], v54 offset:16896\n"
  "ds_read_b64_tr_b16 a[204:205], v55 offset:16384\n"
  "ds_read_b64_tr_b16 a[206:207], v55 offset:16896\n"
  "v_mfma_f32_16x16x32_bf16 v[56:59], v[208:211], a[160:163], v[56:59]\n"
  "buffer_load_dwordx4 v[252:255], v175, s[4:7], s73 offen offset:2048 sc1\n"
  "s_cbranch_vccnz Lslow46\n"
  "Lb46:\n"
  "s_waitcnt lgkmcnt(8)\n"
  "v_mfma_f32_16x16x32_bf16 v[76:79], v[216:219], a[188:191], v[76:79]\n"
  "s_waitcnt vmcnt(11)\n"
  "v_xor_b32 v224, v34, v224\n"
  "v_xor_b32 v226, v35, v226\n"
  "v_mfma_f32_16x16x32_bf16 v[84:87], v[220:223], a[188:191], v[84:87]\n"
  "buffer_load_dwordx4 v[192:195], v174, s[4:7], s0 offen offset:0 sc1\n"
  "v_mfma_f32_16x16x32_bf16 v[80:83], v[220:223], a[184:187], v[80:83]\n"
  "s_waitcnt vmcnt(11)\n"
  "v_xor_b32 v228, v34, v228\n"
  "v_xor_b32_e64 v230, v35, v230\n"
  "v_mfma_f32_16x16x32_bf16 v[72:75], v[216:219], a[184:187], v[72:75]\n"
  "buffer_load_dwordx4 v[196:199], v175, s[4:7], s0 offen offset:0 sc1\n"
  "s_waitcnt vmcnt(8)\n"
  "ds_read_b64_tr_b16 a[208:209], v23 offset:32768\n"
  "ds_read_b64_tr_b16 a[210:211], v23 offset:33280\n"
  "ds_read_b64_tr_b16 a[212:213], v53 offset:32768\n"
  "ds_read_b64_tr_b16 a[214:215], v53 offset:33280\n"
  "v_mfma_f32_16x16x32_bf16 v[60:63], v[216:219], a[180:183], v[60:63]\n"
  "v_or3_b32 v52, v224, v226, v228\n"
  "v_bitop3_b32 v52, v52, s33, v230 bitop3:0xc8\n"
  "v_mfma_f32_16x16x32_bf16 v[68:71], v[220:223], a[180:183], v[68:71]\n"
  "v_cmp_ne_u32_e64 vcc, 0, v52\n"
  "buffer_load_dwordx4 v[200:203], v174, s[4:7], s0 offen offset:2048 sc1\n"
  "v_mfma_f32_16x16x32_bf16 v[64:67], v[220:223], a[176:179], v[64:67]\n"
  "ds_read_b64_tr_b16 a[216:217], v54 offset:32768\n"
  "ds_read_b64_tr_b16 a[218:219], v54 offset:33280\n"
  "ds_read_b64_tr_b16 a[220:221], v55 offset:32768\n"
  "ds_read_b64_tr_b16 a[222:223], v55 offset:33280\n"
  "v_mfma_f32_16x16x32_bf16 v[56:59], v[216:219], a[176:179], v[56:59]\n"
  "buffer_load_dwordx4 v[204:207], v175, s[4:7], s0 offen offset:2048 sc1\n"
  "buffer_load_dwordx4 v[208:211], v174, s[4:7], s1 offen offset:0 sc1\n"
  "s_cbranch_vccnz Lslow47\n"
  "Lb47:\n"
  "s_waitcnt lgkmcnt(8)\n"
  "v_mfma_f32_16x16x32_bf16 v[76:79], v[224:227], a[204:207], v[76:79]\n"
  "s_waitcnt vmcnt(10)\n"
  "v_xor_b32 v232, v34, v232\n"
  "v_xor_b32 v234, v35, v234\n"
  "v_mfma_f32_16x16x32_bf16 v[84:87], v[228:231], a[204:207], v[84:87]\n"
  "buffer_load_dwordx4 v[212:215], v175, s[4:7], s1 offen offset:0 sc1\n"
  "v_mfma_f32_16x16x32_bf16 v[80:83], v[228:231], a[200:203], v[80:83]\n"
  "s_waitcnt vmcnt(10)\n"
  "v_xor_b32 v236, v34, v236\n"
  "v_xor_b32 v238, v35, v238\n"
  "v_mfma_f32_16x16x32_bf16 v[72:75], v[224:227], a[200:203], v[72:75]\n"
  "buffer_load_dwordx4 v[216:219], v174, s[4:7], s1 offen offset:2048 sc1\n"
  "v_mfma_f32_16x16x32_bf16 v[60:63], v[224:227], a[196:199], v[60:63]\n"
  "v_or3_b32 v52, v232, v234, v236\n"
  "v_bitop3_b32 v52, v52, s33, v238 bitop3:0xc8\n"
  "v_mfma_f32_16x16x32_bf16 v[68:71], v[228:231], a[196:199], v[68:71]\n"
  "v_cmp_ne_u32 vcc, 0, v52\n"
  "buffer_load_dwordx4 v[220:223], v175, s[4:7], s1 offen offset:2048 sc1\n"
  "v_mfma_f32_16x16x32_bf16 v[64:67], v[228:231], a[192:195], v[64:67]\n"
  "v_mfma_f32_16x16x32_bf16 v[56:59], v[224:227], a[192:195], v[56:59]\n"
  "s_cbranch_vccnz Lslow48\n"
  "Lb48:\n"
  "s_waitcnt lgkmcnt(0)\n"
  "v_mfma_f32_16x16x32_bf16 v[76:79], v[232:235], a[220:223], v[76:79]\n"
  "s_waitcnt vmcnt(11)\n"
  "v_xor_b32 v240, v34, v240\n"
  "v_xor_b32 v242, v35, v242\n"
  "v_mfma_f32_16x16x32_bf16 v[84:87], v[236:239], a[220:223], v[84:87]\n"
  "buffer_load_dwordx4 v[224:227], v175, s[4:7], s3 offen offset:0 sc1\n"
  "v_mfma_f32_16x16x32_bf16 v[80:83], v[236:239], a[216:219], v[80:83]\n"
  "s_waitcnt vmcnt(11)\n"
  "v_xor_b32 v244, v34, v244\n"
  "v_xor_b32 v246, v35, v246\n"
  "v_mfma_f32_16x16x32_bf16 v[72:75], v[232:235], a[216:219], v[72:75]\n"
  "buffer_load_dwordx4 v[228:231], v175, s[4:7], s3 offen offset:2048 sc1\n"
  "v_mfma_f32_16x16x32_bf16 v[60:63], v[232:235], a[212:215], v[60:63]\n"
  "v_or3_b32 v52, v240, v242, v244\n"
  "v_bitop3_b32 v52, v52, s33, v246 bitop3:0xc8\n"
  "v_mfma_f32_16x16x32_bf16 v[68:71], v[236:239], a[212:215], v[68:71]\n"
  "v_cmp_ne_u32 vcc, 0, v52\n"
  "v_mfma_f32_16x16x32_bf16 v[64:67], v[236:239], a[208:211], v[64:67]\n"
  "v_mfma_f32_16x16x32_bf16 v[56:59], v[232:235], a[208:211], v[56:59]\n"
  "s_cbranch_vccnz Lslow49\n"
  "Lb49:\n"
  "Ljoin:\n"
  "v_mfma_f32_16x16x32_bf16 v[108:111], v[240:243], a[12:15], 0\n"
  "s_waitcnt vmcnt(11)\n"
  "v_xor_b32 v248, v34, v248\n"
  "v_xor_b32 v250, v35, v250\n"
  "v_mfma_f32_16x16x32_bf16 v[116:119], v[244:247], a[12:15], 0\n"
  "v_mfma_f32_16x16x32_bf16 v[112:115], v[244:247], a[8:11], 0\n"
  "s_waitcnt vmcnt(10)\n"
  "v_xor_b32 v252, v34, v252\n"
  "v_xor_b32 v254, v35, v254\n"
  "v_mfma_f32_16x16x32_bf16 v[104:107], v[240:243], a[8:11], 0\n"
  "ds_write_b128 v21, v[76:79] offset:1024\n"
  "v_mfma_f32_16x16x32_bf16 v[92:95], v[240:243], a[4:7], v[92:95]\n"
  "v_or3_b32 v52, v248, v250, v252\n"
  "v_bitop3_b32 v52, v52, s33, v254 bitop3:0xc8\n"
  "v_mfma_f32_16x16x32_bf16 v[100:103], v[244:247], a[4:7], 0\n"
  "v_cmp_ne_u32 vcc, 0, v52\n"
  "v_mfma_f32_16x16x32_bf16 v[96:99], v[244:247], a[0:3], 0\n"
  "ds_write_b128 v21, v[72:75] offset:0\n"
  "v_mfma_f32_16x16x32_bf16 v[88:91], v[240:243], a[0:3], v[88:91]\n"
  "s_cbranch_vccnz Lslow50\n"
  "Lb50:\n"
  "v_mfma_f32_16x16x32_bf16 v[108:111], v[248:251], a[28:31], v[108:111]\n"
  "s_waitcnt vmcnt(9)\n"
  "v_xor_b32 v192, v34, v192\n"
  "v_xor_b32 v194, v35, v194\n"
  "v_mfma_f32_16x16x32_bf16 v[116:119], v[252:255], a[28:31], v[116:119]\n"
  "ds_write_b128 v22, v[84:87] offset:1024\n"
  "v_mfma_f32_16x16x32_bf16 v[112:115], v[252:255], a[24:27], v[112:115]\n"
  "s_waitcnt vmcnt(8)\n"
  "v_xor_b32 v196, v34, v196\n"
  "v_xor_b32 v198, v35, v198\n"
  "v_mfma_f32_16x16x32_bf16 v[104:107], v[248:251], a[24:27], v[104:107]\n"
  "buffer_load_dwordx4 v[232:235], v175, s[4:7], s94 offen offset:0 sc1\n"
  "v_mfma_f32_16x16x32_bf16 v[92:95], v[248:251], a[20:23], v[92:95]\n"
  "v_or3_b32 v52, v192, v194, v196\n"
  "v_bitop3_b32 v52, v52, s33, v198 bitop3:0xc8\n"
  "v_mfma_f32_16x16x32_bf16 v[100:103], v[252:255], a[20:23], v[100:103]\n"
  "v_cmp_ne_u32 vcc, 0, v52\n"
  "ds_write_b128 v22, v[80:83] offset:0\n"
  "v_mfma_f32_16x16x32_bf16 v[96:99], v[252:255], a[16:19], v[96:99]\n"
  "v_mfma_f32_16x16x32_bf16 v[88:91], v[248:251], a[16:19], v[88:91]\n"
  "s_cbranch_vccnz Lslow51\n"
  "Lb51:\n"
  "v_mfma_f32_16x16x32_bf16 v[108:111], v[192:195], a[44:47], v[108:111]\n"
  "s_waitcnt vmcnt(8)\n"
  "v_xor_b32 v200, v34, v200\n"
  "v_xor_b32 v202, v35, v202\n"
  "v_mfma_f32_16x16x32_bf16 v[116:119], v[196:199], a[44:47], v[116:119]\n"
  "buffer_load_dwordx4 v[236:239], v175, s[4:7], s94 offen offset:2048 sc1\n"
  "ds_write_b128 v20, v[68:71] offset:1024\n"
  "v_mfma_f32_16x16x32_bf16 v[112:115], v[196:199], a[40:43], v[112:115]\n"
  "s_waitcnt vmcnt(8)\n"
  "v_xor_b32 v204, v34, v204\n"
  "v_xor_b32 v206, v35, v206\n"
  "v_mfma_f32_16x16x32_bf16 v[104:107], v[192:195], a[40:43], v[104:107]\n"
  "buffer_load_dwordx4 v[240:243], v175, s[4:7], s95 offen offset:0 sc1\n"
  "v_mfma_f32_16x16x32_bf16 v[92:95], v[192:195], a[36:39], v[92:95]\n"
  "v_or3_b32 v52, v200, v202, v204\n"
  "v_bitop3_b32 v52, v52, s33, v206 bitop3:0xc8\n"
  "v_mfma_f32_16x16x32_bf16 v[100:103], v[196:199], a[36:39], v[100:103]\n"
  "v_cmp_ne_u32 vcc, 0, v52\n"
  "ds_write_b128 v20, v[64:67] offset:0\n"
  "v_mfma_f32_16x16x32_bf16 v[96:99], v[196:199], a[32:35], v[96:99]\n"
  "v_mfma_f32_16x16x32_bf16 v[88:91], v[192:195], a[32:35], v[88:91]\n"
  "s_cbranch_vccnz Lslow52\n"
  "Lb52:\n"
  "v_mfma_f32_16x16x32_bf16 v[108:111], v[200:203], a[60:63], v[108:111]\n"
  "s_waitcnt vmcnt(8)\n"
  "v_xor_b32 v208, v34, v208\n"
  "v_xor_b32 v210, v35, v210\n"
  "v_mfma_f32_16x16x32_bf16 v[116:119], v[204:207], a[60:63], v[116:119]\n"
  "buffer_load_dwordx4 v[244:247], v175, s[4:7], s95 offen offset:2048 sc1\n"
  "ds_write_b128 v26, v[60:63] offset:7168\n"
  "v_mfma_f32_16x16x32_bf16 v[112:115], v[204:207], a[56:59], v[112:115]\n"
  "s_waitcnt vmcnt(8)\n"
  "v_xor_b32 v212, v34, v212\n"
  "v_xor_b32 v214, v35, v214\n"
  "v_mfma_f32_16x16x32_bf16 v[104:107], v[200:203], a[56:59], v[104:107]\n"
  "buffer_load_dwordx4 v[248:251], v175, s[4:7], s30 offen offset:0 sc1\n"
  "v_mfma_f32_16x16x32_bf16 v[92:95], v[200:203], a[52:55], v[92:95]\n"
  "v_or3_b32 v52, v208, v210, v212\n"
  "v_bitop3_b32 v52, v52, s33, v214 bitop3:0xc8\n"
  "v_mfma_f32_16x16x32_bf16 v[100:103], v[204:207], a[52:55], v[100:103]\n"
  "v_cmp_ne_u32 vcc, 0, v52\n"
  "ds_write_b128 v26, v[56:59] offset:6144\n"
  "v_mfma_f32_16x16x32_bf16 v[96:99], v[204:207], a[48:51], v[96:99]\n"
  "v_mfma_f32_16x16x32_bf16 v[88:91], v[200:203], a[48:51], v[88:91]\n"
  "s_cbranch_vccnz Lslow53\n"
  "Lb53:\n"
  "v_mfma_f32_16x16x32_bf16 v[108:111], v[208:211], a[76:79], v[108:111]\n"
  "s_waitcnt vmcnt(8)\n"
  "v_xor_b32 v216, v34, v216\n"
  "v_xor_b32 v218, v35, v218\n"
  "v_mfma_f32_16x16x32_bf16 v[116:119], v[212:215], a[76:79], v[116:119]\n"
  "buffer_load_dwordx4 v[252:255], v175, s[4:7], s30 offen offset:2048 sc1\n"
  "v_mfma_f32_16x16x32_bf16 v[112:115], v[212:215], a[72:75], v[112:115]\n"
  "s_waitcnt vmcnt(8)\n"
  "v_xor_b32 v220, v34, v220\n"
  "v_xor_b32 v222, v35, v222\n"
  "v_mfma_f32_16x16x32_bf16 v[104:107], v[208:211], a[72:75], v[104:107]\n"
  "buffer_load_dwordx4 v[192:195], v174, s[4:7], s3 offen offset:0 sc1\n"
  "s_waitcnt lgkmcnt(0)\n"
  "s_barrier\n"
  "v_mfma_f32_16x16x32_bf16 v[92:95], v[208:211], a[68:71], v[92:95]\n"
  "v_or3_b32 v52, v216, v218, v220\n"
  "v_bitop3_b32 v52, v52, s33, v222 bitop3:0xc8\n"
  "v_mfma_f32_16x16x32_bf16 v[100:103], v[212:215], a[68:71], v[100:103]\n"
  "v_cmp_ne_u32 vcc, 0, v52\n"
  "ds_read_b128 v[56:59], v27 offset:6144\n"
  "v_mfma_f32_16x16x32_bf16 v[96:99], v[212:215], a[64:67], v[96:99]\n"
  "ds_read_b128 v[120:123], v27 offset:0\n"
  "v_mfma_f32_16x16x32_bf16 v[88:91], v[208:211], a[64:67], v[88:91]\n"
  "ds_read_b128 v[128:131], v27 offset:2048\n"
  "s_cbranch_vccnz Lslow54\n"
  "Lb54:\n"
  "v_mfma_f32_16x16x32_bf16 v[108:111], v[216:219], a[92:95], v[108:111]\n"
  "s_waitcnt vmcnt(8)\n"
  "v_xor_b32 v224, v34, v224\n"
  "v_xor_b32 v226, v35, v226\n"
  "v_mfma_f32_16x16x32_bf16 v[116:119], v[220:223], a[92:95], v[116:119]\n"
  "buffer_load_dwordx4 v[196:199], v174, s[4:7], s3 offen offset:2048 sc1\n"
  "ds_read_b128 v[136:139], v27 offset:4096\n"
  "v_mfma_f32_16x16x32_bf16 v[112:115], v[220:223], a[88:91], v[112:115]\n"
  "s_waitcnt vmcnt(8)\n"
  "v_xor_b32 v228, v34, v228\n"
  "v_xor_b32 v230, v35, v230\n"
  "v_mfma_f32_16x16x32_bf16 v[104:107], v[216:219], a[88:91], v[104:107]\n"
  "buffer_load_dwordx4 v[200:203], v174, s[4:7], s94 offen offset:0 sc1\n"
  "ds_read_b128 v[60:63], v27 offset:7168\n"
  "v_mfma_f32_16x16x32_bf16 v[92:95], v[216:219], a[84:87], v[92:95]\n"
  "v_or3_b32 v52, v224, v226, v228\n"
  "v_bitop3_b32 v52, v52, s33, v230 bitop3:0xc8\n"
  "v_mfma_f32_16x16x32_bf16 v[100:103], v[220:223], a[84:87], v[100:103]\n"
  "v_cmp_ne_u32 vcc, 0, v52\n"
  "ds_read_b128 v[124:127], v27 offset:1024\n"
  "v_mfma_f32_16x16x32_bf16 v[96:99], v[220:223], a[80:83], v[96:99]\n"
  "ds_read_b128 v[132:135], v27 offset:3072\n"
  "v_mfma_f32_16x16x32_bf16 v[88:91], v[216:219], a[80:83], v[88:91]\n"
  "ds_read_b128 v[140:143], v27 offset:5120\n"
  "s_cbranch_vccnz Lslow55\n"
  "Lb55:\n"
  "v_mfma_f32_16x16x32_bf16 v[116:119], v[224:227], a[108:111], v[116:119]\n"
  "s_waitcnt vmcnt(8)\n"
  "v_xor_b32 v232, v34, v232\n"
  "v_xor_b32 v234, v35, v234\n"
  "v_mfma_f32_16x16x32_bf16 v[116:119], v[228:231], a[124:127], v[116:119]\n"
  "buffer_load_dwordx4 v[204:207], v174, s[4:7], s94 offen offset:2048 sc1\n"
  "v_mfma_f32_16x16x32_bf16 v[112:115], v[228:231], a[120:123], v[112:115]\n"
  "s_waitcnt vmcnt(8)\n"
  "v_xor_b32 v236, v34, v236\n"
  "v_xor_b32 v238, v35, v238\n"
  "v_mfma_f32_16x16x32_bf16 v[112:115], v[224:227], a[104:107], v[112:115]\n"
  "buffer_load_dwordx4 v[208:211], v174, s[4:7], s95 offen offset:0 sc1\n"
  "s_waitcnt lgkmcnt(4)\n"
  "v_add_f32 v136, v128, v136\n"
  "v_add_f32 v137, v129, v137\n"
  "v_mfma_f32_16x16x32_bf16 v[100:103], v[224:227], a[100:103], v[100:103]\n"
  "v_or3_b32 v52, v232, v234, v236\n"
  "v_bitop3_b32 v52, v52, s33, v238 bitop3:0xc8\n"
  "v_mfma_f32_16x16x32_bf16 v[100:103], v[228:231], a[116:119], v[100:103]\n"
  "v_cmp_ne_u32 vcc, 0, v52\n"
  "v_add_f32 v138, v130, v138\n"
  "v_add_f32 v139, v131, v139\n"
  "v_mfma_f32_16x16x32_bf16 v[96:99], v[228:231], a[112:115], v[96:99]\n"
  "v_add_f32 v120, v120, v56\n"
  "v_add_f32 v121, v121, v57\n"
  "v_mfma_f32_16x16x32_bf16 v[96:99], v[224:227], a[96:99], v[96:99]\n"
  "v_add_f32 v122, v122, v58\n"
  "v_add_f32 v123, v123, v59\n"
  "s_cbranch_vccnz Lslow56\n"
  "Lb56:\n"
  "v_mfma_f32_16x16x32_bf16 v[116:119], v[232:235], a[140:143], v[116:119]\n"
  "s_waitcnt vmcnt(8)\n"
  "v_xor_b32 v240, v34, v240\n"
  "v_xor_b32 v242, v35, v242\n"
  "v_mfma_f32_16x16x32_bf16 v[116:119], v[236:239], a[156:159], v[116:119]\n"
  "buffer_load_dwordx4 v[212:215], v174, s[4:7], s95 offen offset:2048 sc1\n"
  "v_add_f32 v120, v120, v136\n"
  "v_add_f32 v121, v121, v137\n"
  "v_mfma_f32_16x16x32_bf16 v[112:115], v[236:239], a[152:155], v[112:115]\n"
  "s_waitcnt vmcnt(8)\n"
  "v_xor_b32 v244, v34, v244\n"
  "v_xor_b32 v246, v35, v246\n"
  "v_mfma_f32_16x16x32_bf16 v[112:115], v[232:235], a[136:139], v[112:115]\n"
  "buffer_load_dwordx4 v[216:219], v174, s[4:7], s30 offen offset:0 sc1\n"
  "v_add_f32 v122, v122, v138\n"
  "v_add_f32 v123, v123, v139\n"
  "v_mfma_f32_16x16x32_bf16 v[100:103], v[232:235], a[132:135], v[100:103]\n"
  "v_or3_b32 v52, v240, v242, v244\n"
  "v_bitop3_b32 v52, v52, s33, v246 bitop3:0xc8\n"
  "v_mfma_f32_16x16x32_bf16 v[100:103], v[236:239], a[148:151], v[100:103]\n"
  "v_cmp_ne_u32 vcc, 0, v52\n"
  "v_mul_f32 v44, s32, v120\n"
  "v_mul_f32 v45, s32, v121\n"
  "v_mfma_f32_16x16x32_bf16 v[96:99], v[236:239], a[144:147], v[96:99]\n"
  "v_mul_f32 v46, s32, v122\n"
  "v_mul_f32 v47, s32, v123\n"
  "v_mfma_f32_16x16x32_bf16 v[96:99], v[232:235], a[128:131], v[96:99]\n"
  "s_waitcnt lgkmcnt(0)\n"
  "v_add_f32 v140, v132, v140\n"
  "v_add_f32 v141, v133, v141\n"
  "s_cbranch_vccnz Lslow57\n"
  "Lb57:\n"
  "v_mfma_f32_16x16x32_bf16 v[116:119], v[240:243], a[172:175], v[116:119]\n"
  "s_waitcnt vmcnt(8)\n"
  "v_xor_b32 v248, v34, v248\n"
  "v_xor_b32 v250, v35, v250\n"
  "v_mfma_f32_16x16x32_bf16 v[116:119], v[244:247], a[188:191], v[116:119]\n"
  "buffer_load_dwordx4 v[220:223], v174, s[4:7], s30 offen offset:2048 sc1\n"
  "v_add_f32 v142, v134, v142\n"
  "v_add_f32 v143, v135, v143\n"
  "v_mfma_f32_16x16x32_bf16 v[112:115], v[244:247], a[184:187], v[112:115]\n"
  "s_waitcnt vmcnt(8)\n"
  "v_xor_b32 v252, v34, v252\n"
  "v_xor_b32 v254, v35, v254\n"
  "v_mfma_f32_16x16x32_bf16 v[112:115], v[240:243], a[168:171], v[112:115]\n"
  "v_add_f32 v124, v124, v60\n"
  "v_add_f32 v125, v125, v61\n"
  "v_mfma_f32_16x16x32_bf16 v[100:103], v[240:243], a[164:167], v[100:103]\n"
  "v_or3_b32 v52, v248, v250, v252\n"
  "v_bitop3_b32 v52, v52, s33, v254 bitop3:0xc8\n"
  "v_mfma_f32_16x16x32_bf16 v[100:103], v[244:247], a[180:183], v[100:103]\n"
  "v_cmp_ne_u32 vcc, 0, v52\n"
  "v_add_f32 v126, v126, v62\n"
  "v_add_f32 v127, v127, v63\n"
  "v_mfma_f32_16x16x32_bf16 v[96:99], v[244:247], a[176:179], v[96:99]\n"
  "v_add_f32 v124, v124, v140\n"
  "v_add_f32 v125, v125, v141\n"
  "v_mfma_f32_16x16x32_bf16 v[96:99], v[240:243], a[160:163], v[96:99]\n"
  "v_add_f32 v126, v126, v142\n"
  "v_add_f32 v127, v127, v143\n"
  "s_cbranch_vccnz Lslow58\n"
  "Lb58:\n"
  "v_mfma_f32_16x16x32_bf16 v[116:119], v[248:251], a[204:207], v[116:119]\n"
  "s_waitcnt vmcnt(7)\n"
  "v_xor_b32 v192, v34, v192\n"
  "v_xor_b32 v194, v35, v194\n"
  "v_mfma_f32_16x16x32_bf16 v[116:119], v[252:255], a[220:223], v[116:119]\n"
  "v_mul_f32 v48, s32, v124\n"
  "v_mul_f32 v49, s32, v125\n"
  "v_mfma_f32_16x16x32_bf16 v[112:115], v[252:255], a[216:219], v[112:115]\n"
  "s_waitcnt vmcnt(6)\n"
  "v_xor_b32 v196, v34, v196\n"
  "v_xor_b32 v198, v35, v198\n"
  "v_mfma_f32_16x16x32_bf16 v[112:115], v[248:251], a[200:203], v[112:115]\n"
  "v_mul_f32 v50, s32, v126\n"
  "v_mul_f32 v51, s32, v127\n"
  "v_mfma_f32_16x16x32_bf16 v[100:103], v[248:251], a[196:199], v[100:103]\n"
  "v_or3_b32 v52, v192, v194, v196\n"
  "v_bitop3_b32 v52, v52, s33, v198 bitop3:0xc8\n"
  "v_mfma_f32_16x16x32_bf16 v[100:103], v[252:255], a[212:215], v[100:103]\n"
  "v_cmp_ne_u32 vcc, 0, v52\n"
  "s_barrier\n"
  "v_mfma_f32_16x16x32_bf16 v[96:99], v[252:255], a[208:211], v[96:99]\n"
  "v_mfma_f32_16x16x32_bf16 v[96:99], v[248:251], a[192:195], v[96:99]\n"
  "s_cbranch_vccnz Lslow59\n"
  "Lb59:\n"
  "v_mfma_f32_16x16x32_bf16 v[108:111], v[192:195], a[108:111], v[108:111]\n"
  "s_waitcnt vmcnt(5)\n"
  "v_xor_b32 v200, v34, v200\n"
  "v_xor_b32 v202, v35, v202\n"
  "v_mfma_f32_16x16x32_bf16 v[108:111], v[196:199], a[124:127], v[108:111]\n"
  "v_mfma_f32_16x16x32_bf16 v[104:107], v[196:199], a[120:123], v[104:107]\n"
  "s_waitcnt vmcnt(4)\n"
  "v_xor_b32 v204, v34, v204\n"
  "v_xor_b32 v206, v35, v206\n"
  "v_mfma_f32_16x16x32_bf16 v[104:107], v[192:195], a[104:107], v[104:107]\n"
  "ds_write_b128 v22, v[116:119] offset:1024\n"
  "v_mfma_f32_16x16x32_bf16 v[92:95], v[192:195], a[100:103], v[92:95]\n"
  "v_or3_b32 v52, v200, v202, v204\n"
  "v_bitop3_b32 v52, v52, s33, v206 bitop3:0xc8\n"
  "v_mfma_f32_16x16x32_bf16 v[92:95], v[196:199], a[116:119], v[92:95]\n"
  "v_cmp_ne_u32 vcc, 0, v52\n"
  "v_mfma_f32_16x16x32_bf16 v[88:91], v[196:199], a[112:115], v[88:91]\n"
  "ds_write_b128 v22, v[112:115] offset:0\n"
  "v_mfma_f32_16x16x32_bf16 v[88:91], v[192:195], a[96:99], v[88:91]\n"
  "s_cbranch_vccnz Lslow60\n"
  "Lb60:\n"
  "v_mfma_f32_16x16x32_bf16 v[108:111], v[200:203], a[140:143], v[108:111]\n"
  "s_waitcnt vmcnt(3)\n"
  "v_xor_b32 v208, v34, v208\n"
  "v_xor_b32 v210, v35, v210\n"
  "v_mfma_f32_16x16x32_bf16 v[108:111], v[204:207], a[156:159], v[108:111]\n"
  "ds_write_b128 v20, v[100:103] offset:1024\n"
  "v_mfma_f32_16x16x32_bf16 v[104:107], v[204:207], a[152:155], v[104:107]\n"
  "s_waitcnt vmcnt(2)\n"
  "v_xor_b32 v212, v34, v212\n"
  "v_xor_b32 v214, v35, v214\n"
  "v_mfma_f32_16x16x32_bf16 v[104:107], v[200:203], a[136:139], v[104:107]\n"
  "v_mfma_f32_16x16x32_bf16 v[92:95], v[200:203], a[132:135], v[92:95]\n"
  "v_or3_b32 v52, v208, v210, v212\n"
  "v_bitop3_b32 v52, v52, s33, v214 bitop3:0xc8\n"
  "v_mfma_f32_16x16x32_bf16 v[92:95], v[204:207], a[148:151], v[92:95]\n"
  "v_cmp_ne_u32 vcc, 0, v52\n"
  "ds_write_b128 v20, v[96:99] offset:0\n"
  "v_mfma_f32_16x16x32_bf16 v[88:91], v[204:207], a[144:147], v[88:91]\n"
  "v_mfma_f32_16x16x32_bf16 v[88:91], v[200:203], a[128:131], v[88:91]\n"
  "s_cbranch_vccnz Lslow61\n"
  "Lb61:\n"
  "v_mfma_f32_16x16x32_bf16 v[108:111], v[208:211], a[172:175], v[108:111]\n"
  "s_waitcnt vmcnt(1)\n"
  "v_xor_b32 v216, v34, v216\n"
  "v_xor_b32 v218, v35, v218\n"
  "v_mfma_f32_16x16x32_bf16 v[108:111], v[212:215], a[188:191], v[108:111]\n"
  "v_mfma_f32_16x16x32_bf16 v[104:107], v[212:215], a[184:187], v[104:107]\n"
  "s_waitcnt vmcnt(0)\n"
  "v_xor_b32 v220, v34, v220\n"
  "v_xor_b32 v222, v35, v222\n"
  "v_mfma_f32_16x16x32_bf16 v[104:107], v[208:211], a[168:171], v[104:107]\n"
  "s_waitcnt lgkmcnt(0)\n"
  "s_barrier\n"
  "v_mfma_f32_16x16x32_bf16 v[92:95], v[208:211], a[164:167], v[92:95]\n"
  "v_or3_b32 v52, v216, v218, v220\n"
  "v_bitop3_b32 v52, v52, s33, v222 bitop3:0xc8\n"
  "v_mfma_f32_16x16x32_bf16 v[92:95], v[212:215], a[180:183], v[92:95]\n"
  "v_cmp_ne_u32 vcc, 0, v52\n"
  "ds_read_b128 v[120:123], v27 offset:0\n"
  "v_mfma_f32_16x16x32_bf16 v[88:91], v[212:215], a[176:179], v[88:91]\n"
  "ds_read_b128 v[136:139], v27 offset:4096\n"
  "v_mfma_f32_16x16x32_bf16 v[88:91], v[208:211], a[160:163], v[88:91]\n"
  "ds_read_b128 v[124:127], v27 offset:1024\n"
  "s_cbranch_vccnz Lslow62\n"
  "Lb62:\n"
  "v_mfma_f32_16x16x32_bf16 v[108:111], v[216:219], a[204:207], v[108:111]\n"
  "ds_read_b128 v[140:143], v27 offset:5120\n"
  "v_mfma_f32_16x16x32_bf16 v[108:111], v[220:223], a[220:223], v[108:111]\n"
  "v_mfma_f32_16x16x32_bf16 v[104:107], v[220:223], a[216:219], v[104:107]\n"
  "v_mfma_f32_16x16x32_bf16 v[104:107], v[216:219], a[200:203], v[104:107]\n"
  "s_waitcnt lgkmcnt(2)\n"
  "v_add_f32 v120, v120, v136\n"
  "v_add_f32 v121, v121, v137\n"
  "v_mfma_f32_16x16x32_bf16 v[92:95], v[216:219], a[196:199], v[92:95]\n"
  "v_add_f32 v122, v122, v138\n"
  "v_add_f32 v123, v123, v139\n"
  "v_mfma_f32_16x16x32_bf16 v[92:95], v[220:223], a[212:215], v[92:95]\n"
  "s_waitcnt lgkmcnt(0)\n"
  "v_add_f32 v124, v124, v140\n"
  "v_add_f32 v125, v125, v141\n"
  "v_mfma_f32_16x16x32_bf16 v[88:91], v[220:223], a[208:211], v[88:91]\n"
  "v_add_f32 v126, v126, v142\n"
  "v_add_f32 v127, v127, v143\n"
  "v_mfma_f32_16x16x32_bf16 v[88:91], v[216:219], a[192:195], v[88:91]\n"
  "s_add_i32 s84, s75, 0x4000\n"
  "ds_write_b128 v21, v[108:111] offset:1024\n"
  "ds_write_b128 v21, v[104:107] offset:0\n"
  "ds_write_b128 v26, v[92:95] offset:7168\n"
  "s_nop 3\n"
  "ds_write_b128 v26, v[88:91] offset:6144\n"
  "v_pk_mul_f32 v[124:125], v[124:125], s[32:33] op_sel_hi:[1,0]\n"
  "v_pk_mul_f32 v[126:127], v[126:127], s[32:33] op_sel_hi:[1,0]\n"
  "v_pk_mul_f32 v[120:121], v[120:121], s[32:33] op_sel_hi:[1,0]\n"
  "v_pk_mul_f32 v[122:123], v[122:123], s[32:33] op_sel_hi:[1,0]\n"
  "s_waitcnt lgkmcnt(0)\n"
  "s_barrier\n"
  "ds_read_b128 v[88:91], v27 offset:6144\n"
  "ds_read_b128 v[128:131], v27 offset:2048\n"
  "ds_read_b128 v[92:95], v27 offset:7168\n"
  "ds_read_b128 v[132:135], v27 offset:3072\n"
  "s_waitcnt lgkmcnt(2)\n"
  "v_pk_add_f32 v[128:129], v[128:129], v[88:89]\n"
  "v_pk_add_f32 v[130:131], v[130:131], v[90:91]\n"
  "v_pk_fma_f32 v[144:145], v[128:129], s[32:33], v[120:121] op_sel_hi:[1,0,1]\n"
  "v_pk_fma_f32 v[146:147], v[130:131], s[32:33], v[122:123] op_sel_hi:[1,0,1]\n"
  "v_cvt_pk_bf16_f32 v152, v44, v144\n"
  "v_cvt_pk_bf16_f32 v153, v45, v145\n"
  "v_cvt_pk_bf16_f32 v154, v46, v146\n"
  "v_cvt_pk_bf16_f32 v155, v47, v147\n"
  "buffer_store_dwordx4 v[152:155], v29, s[40:43], s75 offen nt\n"
  "s_waitcnt lgkmcnt(0)\n"
  "v_pk_add_f32 v[132:133], v[132:133], v[92:93]\n"
  "v_pk_add_f32 v[134:135], v[134:135], v[94:95]\n"
  "v_pk_fma_f32 v[148:149], v[132:133], s[32:33], v[124:125] op_sel_hi:[1,0,1]\n"
  "v_pk_fma_f32 v[150:151], v[134:135], s[32:33], v[126:127] op_sel_hi:[1,0,1]\n"
  "v_cvt_pk_bf16_f32 v156, v48, v148\n"
  "v_cvt_pk_bf16_f32 v157, v49, v149\n"
  "v_cvt_pk_bf16_f32 v158, v50, v150\n"
  "v_cvt_pk_bf16_f32 v159, v51, v151\n"
  "buffer_store_dwordx4 v[156:159], v29, s[40:43], s84 offen nt\n"
  "s_endpgm\n"
  "Lidle:\n"
  "s_and_b32 s88, s55, 3\n"
  "v_bfe_u32 v14, v0, 2, 4\n"
  "s_mul_i32 s89, s88, 65408\n"
  "v_lshl_add_u32 v13, v14, 12, s89\n"
  "v_lshl_add_u32 v13, v25, 3, v13\n"
  "s_lshl_b32 s88, s88, 4\n"
  "v_add_u32 v12, s88, v14\n"
  "v_mul_u32_u24 v12, 3584, v12\n"
  "v_lshl_add_u32 v12, v25, 3, v12\n"
  "s_bitcmp0_b32 s54, 0\n"
  "s_cselect_b32 s88, 384, 0\n"
  "s_sub_i32 s64, s64, s88\n"
  "s_waitcnt lgkmcnt(1)\n"
  "v_mfma_f32_16x16x32_f16 v[216:219], v[56:59], v[184:187], 0\n"
  "s_add_i32 s88, s64, 0x620000\n"
  "buffer_load_dword v30, v12, s[8:11], s88 offen\n"
  "v_mfma_f32_16x16x32_f16 v[220:223], v[60:63], v[184:187], 0\n"
  "ds_read_b128 v[192:195], v26 offset:1216\n"
  "v_mfma_f32_16x16x32_f16 v[224:227], v[64:67], v[184:187], 0\n"
  "ds_read_b128 v[196:199], v26 offset:1728\n"
  "v_mfma_f32_16x16x32_f16 v[228:231], v[68:71], v[184:187], 0\n"
  "v_readfirstlane_b32 s48, v4\n"
  "v_readfirstlane_b32 s49, v5\n"
  "v_mfma_f32_16x16x32_f16 v[232:235], v[72:75], v[184:187], 0\n"
  "v_and_b32 v4, 0x60006000, v4\n"
  "v_and_b32 v5, 0x60006000, v5\n"
  "v_mfma_f32_16x16x32_f16 v[236:239], v[76:79], v[184:187], 0\n"
  "v_and_b32 v6, 0x60006000, v6\n"
  "v_and_b32 v7, 0x60006000, v7\n"
  "v_mfma_f32_16x16x32_f16 v[240:243], v[80:83], v[184:187], 0\n"
  "v_mul_f32 v28, 0x13800000, v28\n"
  "s_mov_b32 s77, 262144\n"
  "v_mfma_f32_16x16x32_f16 v[244:247], v[84:87], v[184:187], 0\n"
  "ds_read_b128 v[200:203], v26 offset:2240\n"
  "s_waitcnt lgkmcnt(3)\n"
  "v_mfma_f32_16x16x32_f16 v[220:223], v[56:59], v[188:191], v[220:223]\n"
  "ds_read_b128 v[204:207], v26 offset:2752\n"
  "v_mfma_f32_16x16x32_f16 v[224:227], v[60:63], v[188:191], v[224:227]\n"
  "ds_read_b128 v[208:211], v26 offset:3264\n"
  "v_mfma_f32_16x16x32_f16 v[228:231], v[64:67], v[188:191], v[228:231]\n"
  "ds_read_b128 v[212:215], v26 offset:3776\n"
  "v_mfma_f32_16x16x32_f16 v[232:235], v[68:71], v[188:191], v[232:235]\n"
  "ds_read_b128 v[184:187], v26 offset:128\n"
  "v_mfma_f32_16x16x32_f16 v[236:239], v[72:75], v[188:191], v[236:239]\n"
  "ds_read_b64_tr_b16 v[120:121], v27 offset:192\n"
  "v_mfma_f32_16x16x32_f16 v[240:243], v[76:79], v[188:191], v[240:243]\n"
  "ds_read_b64_tr_b16 v[122:123], v27 offset:184\n"
  "v_mfma_f32_16x16x32_f16 v[244:247], v[80:83], v[188:191], v[244:247]\n"
  "ds_read_b128 v[188:191], v26 offset:640\n"
  "s_waitcnt lgkmcnt(9)\n"
  "v_mfma_f32_16x16x32_f16 v[224:227], v[56:59], v[192:195], v[224:227]\n"
  "ds_read_b64_tr_b16 v[124:125], v27 offset:704\n"
  "v_mfma_f32_16x16x32_f16 v[228:231], v[60:63], v[192:195], v[228:231]\n"
  "ds_read_b64_tr_b16 v[126:127], v27 offset:696\n"
  "v_mfma_f32_16x16x32_f16 v[232:235], v[64:67], v[192:195], v[232:235]\n"
  "ds_read_b64_tr_b16 v[128:129], v27 offset:1216\n"
  "v_mfma_f32_16x16x32_f16 v[236:239], v[68:71], v[192:195], v[236:239]\n"
  "ds_read_b64_tr_b16 v[130:131], v27 offset:1208\n"
  "v_mfma_f32_16x16x32_f16 v[240:243], v[72:75], v[192:195], v[240:243]\n"
  "ds_read_b64_tr_b16 v[132:133], v27 offset:1728\n"
  "v_mfma_f32_16x16x32_f16 v[244:247], v[76:79], v[192:195], v[244:247]\n"
  "ds_read_b128 v[192:195], v26 offset:1152\n"
  "s_waitcnt lgkmcnt(14)\n"
  "v_mfma_f32_16x16x32_f16 v[228:231], v[56:59], v[196:199], v[228:231]\n"
  "ds_read_b64_tr_b16 v[134:135], v27 offset:1720\n"
  "v_mfma_f32_16x16x32_f16 v[232:235], v[60:63], v[196:199], v[232:235]\n"
  "ds_read_b64_tr_b16 v[136:137], v27 offset:2240\n"
  "v_mfma_f32_16x16x32_f16 v[236:239], v[64:67], v[196:199], v[236:239]\n"
  "ds_read_b64_tr_b16 v[138:139], v27 offset:2232\n"
  "v_mfma_f32_16x16x32_f16 v[240:243], v[68:71], v[196:199], v[240:243]\n"
  "ds_read_b64_tr_b16 v[140:141], v27 offset:2752\n"
  "v_mfma_f32_16x16x32_f16 v[244:247], v[72:75], v[196:199], v[244:247]\n"
  "ds_read_b128 v[196:199], v26 offset:1664\n"
  "s_waitcnt lgkmcnt(14)\n"
  "v_mfma_f32_16x16x32_f16 v[232:235], v[56:59], v[200:203], v[232:235]\n"
  "ds_read_b64_tr_b16 v[142:143], v27 offset:2744\n"
  "v_mfma_f32_16x16x32_f16 v[236:239], v[60:63], v[200:203], v[236:239]\n"
  "ds_read_b64_tr_b16 v[144:145], v27 offset:3264\n"
  "v_mfma_f32_16x16x32_f16 v[240:243], v[64:67], v[200:203], v[240:243]\n"
  "ds_read_b64_tr_b16 v[146:147], v27 offset:3256\n"
  "v_mfma_f32_16x16x32_f16 v[244:247], v[68:71], v[200:203], v[244:247]\n"
  "ds_read_b128 v[200:203], v26 offset:2176\n"
  "v_mfma_f32_16x16x32_f16 v[236:239], v[56:59], v[204:207], v[236:239]\n"
  "ds_read_b64_tr_b16 v[148:149], v27 offset:3776\n"
  "v_mfma_f32_16x16x32_f16 v[240:243], v[60:63], v[204:207], v[240:243]\n"
  "ds_read_b64_tr_b16 v[150:151], v27 offset:3768\n"
  "v_mfma_f32_16x16x32_f16 v[244:247], v[64:67], v[204:207], v[244:247]\n"
  "ds_read_b128 v[204:207], v26 offset:2688\n"
  "v_mfma_f32_16x16x32_f16 v[240:243], v[56:59], v[208:211], v[240:243]\n"
  "ds_read_b64_tr_b16 v[152:153], v27 offset:256\n"
  "v_mfma_f32_16x16x32_f16 v[244:247], v[60:63], v[208:211], v[244:247]\n"
  "ds_read_b128 v[208:211], v26 offset:3200\n"
  "v_mfma_f32_16x16x32_f16 v[244:247], v[56:59], v[212:215], v[244:247]\n"
  "ds_read_b128 v[212:215], v26 offset:3712\n"
  "v_mfma_f32_16x16x32_f16 v[216:219], v[88:91], v[184:187], v[216:219]\n"
  "ds_read_b64_tr_b16 v[154:155], v27 offset:248\n"
  "v_mfma_f32_16x16x32_f16 v[220:223], v[92:95], v[184:187], v[220:223]\n"
  "ds_read_b64_tr_b16 v[156:157], v27 offset:768\n"
  "v_mfma_f32_16x16x32_f16 v[224:227], v[96:99], v[184:187], v[224:227]\n"
  "ds_read_b64_tr_b16 v[158:159], v27 offset:760\n"
  "v_mfma_f32_16x16x32_f16 v[228:231], v[100:103], v[184:187], v[228:231]\n"
  "ds_read_b64_tr_b16 v[160:161], v27 offset:1280\n"
  "v_mfma_f32_16x16x32_f16 v[232:235], v[104:107], v[184:187], v[232:235]\n"
  "ds_read_b64_tr_b16 v[162:163], v27 offset:1272\n"
  "v_mfma_f32_16x16x32_f16 v[236:239], v[108:111], v[184:187], v[236:239]\n"
  "ds_read_b64_tr_b16 v[164:165], v27 offset:1792\n"
  "v_mfma_f32_16x16x32_f16 v[240:243], v[112:115], v[184:187], v[240:243]\n"
  "ds_read_b64_tr_b16 v[166:167], v27 offset:1784\n"
  "v_mfma_f32_16x16x32_f16 v[244:247], v[116:119], v[184:187], v[244:247]\n"
  "ds_read_b128 v[184:187], v26 offset:64\n"
  "s_waitcnt lgkmcnt(14)\n"
  "v_mfma_f32_16x16x32_f16 v[220:223], v[88:91], v[188:191], v[220:223]\n"
  "ds_read_b64_tr_b16 v[168:169], v27 offset:2304\n"
  "v_mfma_f32_16x16x32_f16 v[224:227], v[92:95], v[188:191], v[224:227]\n"
  "ds_read_b64_tr_b16 v[170:171], v27 offset:2296\n"
  "v_mfma_f32_16x16x32_f16 v[228:231], v[96:99], v[188:191], v[228:231]\n"
  "ds_read_b64_tr_b16 v[172:173], v27 offset:2816\n"
  "v_mfma_f32_16x16x32_f16 v[232:235], v[100:103], v[188:191], v[232:235]\n"
  "ds_read_b64_tr_b16 v[174:175], v27 offset:2808\n"
  "v_mfma_f32_16x16x32_f16 v[236:239], v[104:107], v[188:191], v[236:239]\n"
  "ds_read_b64_tr_b16 v[176:177], v27 offset:3328\n"
  "v_mfma_f32_16x16x32_f16 v[240:243], v[108:111], v[188:191], v[240:243]\n"
  "ds_read_b64_tr_b16 v[178:179], v27 offset:3320\n"
  "v_mfma_f32_16x16x32_f16 v[244:247], v[112:115], v[188:191], v[244:247]\n"
  "ds_read_b128 v[188:191], v26 offset:576\n"
  "v_mfma_f32_16x16x32_f16 v[224:227], v[88:91], v[192:195], v[224:227]\n"
  "ds_read_b64_tr_b16 v[180:181], v27 offset:3840\n"
  "v_mfma_f32_16x16x32_f16 v[228:231], v[92:95], v[192:195], v[228:231]\n"
  "ds_read_b64_tr_b16 v[182:183], v27 offset:3832\n"
  "v_mfma_f32_16x16x32_f16 v[232:235], v[96:99], v[192:195], v[232:235]\n"
  "v_mfma_f32_16x16x32_f16 v[236:239], v[100:103], v[192:195], v[236:239]\n"
  "v_mfma_f32_16x16x32_f16 v[240:243], v[104:107], v[192:195], v[240:243]\n"
  "v_mfma_f32_16x16x32_f16 v[244:247], v[108:111], v[192:195], v[244:247]\n"
  "ds_read_b128 v[192:195], v26 offset:1088\n"
  "v_mfma_f32_16x16x32_f16 v[228:231], v[88:91], v[196:199], v[228:231]\n"
  "v_mfma_f32_16x16x32_f16 v[232:235], v[92:95], v[196:199], v[232:235]\n"
  "v_mfma_f32_16x16x32_f16 v[236:239], v[96:99], v[196:199], v[236:239]\n"
  "v_mfma_f32_16x16x32_f16 v[240:243], v[100:103], v[196:199], v[240:243]\n"
  "s_add_i32 s88, s65, 0x0\n"
  "buffer_load_dword v30, v13, s[12:15], s88 offen\n"
  "v_mfma_f32_16x16x32_f16 v[244:247], v[104:107], v[196:199], v[244:247]\n"
  "ds_read_b128 v[196:199], v26 offset:1600\n"
  "v_mfma_f32_16x16x32_f16 v[232:235], v[88:91], v[200:203], v[232:235]\n"
  "v_mfma_f32_16x16x32_f16 v[236:239], v[92:95], v[200:203], v[236:239]\n"
  "v_mfma_f32_16x16x32_f16 v[240:243], v[96:99], v[200:203], v[240:243]\n"
  "v_mfma_f32_16x16x32_f16 v[244:247], v[100:103], v[200:203], v[244:247]\n"
  "ds_read_b128 v[200:203], v26 offset:2112\n"
  "s_waitcnt lgkmcnt(8)\n"
  "v_mfma_f32_16x16x32_f16 v[236:239], v[88:91], v[204:207], v[236:239]\n"
  "v_mfma_f32_16x16x32_f16 v[240:243], v[92:95], v[204:207], v[240:243]\n"
  "v_mfma_f32_16x16x32_f16 v[244:247], v[96:99], v[204:207], v[244:247]\n"
  "ds_read_b128 v[204:207], v26 offset:2624\n"
  "v_mfma_f32_16x16x32_f16 v[240:243], v[88:91], v[208:211], v[240:243]\n"
  "v_mfma_f32_16x16x32_f16 v[244:247], v[92:95], v[208:211], v[244:247]\n"
  "ds_read_b128 v[208:211], v26 offset:3136\n"
  "v_mfma_f32_16x16x32_f16 v[244:247], v[88:91], v[212:215], v[244:247]\n"
  "ds_read_b128 v[212:215], v26 offset:3648\n"
  "v_mfma_f32_16x16x32_f16 v[216:219], v[120:123], v[184:187], v[216:219]\n"
  "v_mfma_f32_16x16x32_f16 v[220:223], v[124:127], v[184:187], v[220:223]\n"
  "v_mfma_f32_16x16x32_f16 v[224:227], v[128:131], v[184:187], v[224:227]\n"
  "v_mfma_f32_16x16x32_f16 v[228:231], v[132:135], v[184:187], v[228:231]\n"
  "v_mfma_f32_16x16x32_f16 v[232:235], v[136:139], v[184:187], v[232:235]\n"
  "v_mfma_f32_16x16x32_f16 v[236:239], v[140:143], v[184:187], v[236:239]\n"
  "v_mfma_f32_16x16x32_f16 v[240:243], v[144:147], v[184:187], v[240:243]\n"
  "v_mfma_f32_16x16x32_f16 v[244:247], v[148:151], v[184:187], v[244:247]\n"
  "ds_read_b128 v[184:187], v26 offset:0\n"
  "s_waitcnt lgkmcnt(5)\n"
  "v_mfma_f32_16x16x32_f16 v[220:223], v[120:123], v[188:191], v[220:223]\n"
  "v_mfma_f32_16x16x32_f16 v[224:227], v[124:127], v[188:191], v[224:227]\n"
  "v_mfma_f32_16x16x32_f16 v[228:231], v[128:131], v[188:191], v[228:231]\n"
  "v_mfma_f32_16x16x32_f16 v[232:235], v[132:135], v[188:191], v[232:235]\n"
  "v_mfma_f32_16x16x32_f16 v[236:239], v[136:139], v[188:191], v[236:239]\n"
  "v_mfma_f32_16x16x32_f16 v[240:243], v[140:143], v[188:191], v[240:243]\n"
  "v_mfma_f32_16x16x32_f16 v[244:247], v[144:147], v[188:191], v[244:247]\n"
  "ds_read_b128 v[188:191], v26 offset:512\n"
  "v_mfma_f32_16x16x32_f16 v[224:227], v[120:123], v[192:195], v[224:227]\n"
  "v_mfma_f32_16x16x32_f16 v[228:231], v[124:127], v[192:195], v[228:231]\n"
  "v_mfma_f32_16x16x32_f16 v[232:235], v[128:131], v[192:195], v[232:235]\n"
  "v_mfma_f32_16x16x32_f16 v[236:239], v[132:135], v[192:195], v[236:239]\n"
  "v_mfma_f32_16x16x32_f16 v[240:243], v[136:139], v[192:195], v[240:243]\n"
  "v_mfma_f32_16x16x32_f16 v[244:247], v[140:143], v[192:195], v[244:247]\n"
  "ds_read_b128 v[192:195], v26 offset:1024\n"
  "v_mfma_f32_16x16x32_f16 v[228:231], v[120:123], v[196:199], v[228:231]\n"
  "v_mfma_f32_16x16x32_f16 v[232:235], v[124:127], v[196:199], v[232:235]\n"
  "v_mfma_f32_16x16x32_f16 v[236:239], v[128:131], v[196:199], v[236:239]\n"
  "v_mfma_f32_16x16x32_f16 v[240:243], v[132:135], v[196:199], v[240:243]\n"
  "v_mfma_f32_16x16x32_f16 v[244:247], v[136:139], v[196:199], v[244:247]\n"
  "ds_read_b128 v[196:199], v26 offset:1536\n"
  "s_waitcnt lgkmcnt(3)\n"
  "v_mfma_f32_16x16x32_f16 v[232:235], v[120:123], v[200:203], v[232:235]\n"
  "v_mfma_f32_16x16x32_f16 v[236:239], v[124:127], v[200:203], v[236:239]\n"
  "v_mfma_f32_16x16x32_f16 v[240:243], v[128:131], v[200:203], v[240:243]\n"
  "v_mfma_f32_16x16x32_f16 v[244:247], v[132:135], v[200:203], v[244:247]\n"
  "ds_read_b128 v[200:203], v26 offset:2048\n"
  "v_mfma_f32_16x16x32_f16 v[236:239], v[120:123], v[204:207], v[236:239]\n"
  "v_mfma_f32_16x16x32_f16 v[240:243], v[124:127], v[204:207], v[240:243]\n"
  "v_mfma_f32_16x16x32_f16 v[244:247], v[128:131], v[204:207], v[244:247]\n"
  "ds_read_b128 v[204:207], v26 offset:2560\n"
  "v_mfma_f32_16x16x32_f16 v[240:243], v[120:123], v[208:211], v[240:243]\n"
  "v_mfma_f32_16x16x32_f16 v[244:247], v[124:127], v[208:211], v[244:247]\n"
  "ds_read_b128 v[208:211], v26 offset:3072\n"
  "v_mfma_f32_16x16x32_f16 v[244:247], v[120:123], v[212:215], v[244:247]\n"
  "ds_read_b128 v[212:215], v26 offset:3584\n"
  "v_mfma_f32_16x16x32_f16 v[216:219], v[152:155], v[184:187], v[216:219]\n"
  "v_mfma_f32_16x16x32_f16 v[220:223], v[156:159], v[184:187], v[220:223]\n"
  "s_waitcnt lgkmcnt(6)\n"
  "v_mfma_f32_16x16x32_f16 v[220:223], v[152:155], v[188:191], v[220:223]\n"
  "v_mfma_f32_16x16x32_f16 v[224:227], v[160:163], v[184:187], v[224:227]\n"
  "v_mfma_f32_16x16x32_f16 v[224:227], v[156:159], v[188:191], v[224:227]\n"
  "s_waitcnt lgkmcnt(0)\n"
  "s_barrier\n"
  "v_mfma_f32_16x16x32_f16 v[224:227], v[152:155], v[192:195], v[224:227]\n"
  "ds_write_b128 v29, v[216:219] offset:0\n"
  "v_mfma_f32_16x16x32_f16 v[228:231], v[164:167], v[184:187], v[228:231]\n"
  "v_mfma_f32_16x16x32_f16 v[228:231], v[160:163], v[188:191], v[228:231]\n"
  "v_mfma_f32_16x16x32_f16 v[228:231], v[156:159], v[192:195], v[228:231]\n"
  "ds_write_b128 v29, v[220:223] offset:1024\n"
  "v_mfma_f32_16x16x32_f16 v[228:231], v[152:155], v[196:199], v[228:231]\n"
  "v_mfma_f32_16x16x32_f16 v[232:235], v[168:171], v[184:187], v[232:235]\n"
  "v_mfma_f32_16x16x32_f16 v[232:235], v[164:167], v[188:191], v[232:235]\n"
  "ds_write_b128 v29, v[224:227] offset:2048\n"
  "v_mfma_f32_16x16x32_f16 v[232:235], v[160:163], v[192:195], v[232:235]\n"
  "s_add_i32 s88, s65, 0x100000\n"
  "buffer_load_dword v30, v13, s[12:15], s88 offen\n"
  "v_mfma_f32_16x16x32_f16 v[232:235], v[156:159], v[196:199], v[232:235]\n"
  "v_mfma_f32_16x16x32_f16 v[232:235], v[152:155], v[200:203], v[232:235]\n"
  "ds_write_b128 v29, v[228:231] offset:3072\n"
  "v_mfma_f32_16x16x32_f16 v[236:239], v[172:175], v[184:187], v[236:239]\n"
  "v_mfma_f32_16x16x32_f16 v[236:239], v[168:171], v[188:191], v[236:239]\n"
  "v_mfma_f32_16x16x32_f16 v[236:239], v[164:167], v[192:195], v[236:239]\n"
  "v_mfma_f32_16x16x32_f16 v[236:239], v[160:163], v[196:199], v[236:239]\n"
  "s_nop 2\n"
  "ds_write_b128 v29, v[232:235] offset:4096\n"
  "v_mfma_f32_16x16x32_f16 v[236:239], v[156:159], v[200:203], v[236:239]\n"
  "v_mfma_f32_16x16x32_f16 v[236:239], v[152:155], v[204:207], v[236:239]\n"
  "v_mfma_f32_16x16x32_f16 v[240:243], v[176:179], v[184:187], v[240:243]\n"
  "v_mfma_f32_16x16x32_f16 v[240:243], v[172:175], v[188:191], v[240:243]\n"
  "v_mfma_f32_16x16x32_f16 v[240:243], v[168:171], v[192:195], v[240:243]\n"
  "v_mfma_f32_16x16x32_f16 v[240:243], v[164:167], v[196:199], v[240:243]\n"
  "s_nop 3\n"
  "ds_write_b128 v29, v[236:239] offset:5120\n"
  "v_mfma_f32_16x16x32_f16 v[240:243], v[160:163], v[200:203], v[240:243]\n"
  "v_mfma_f32_16x16x32_f16 v[240:243], v[156:159], v[204:207], v[240:243]\n"
  "v_mfma_f32_16x16x32_f16 v[240:243], v[152:155], v[208:211], v[240:243]\n"
  "v_mfma_f32_16x16x32_f16 v[244:247], v[180:183], v[184:187], v[244:247]\n"
  "v_mfma_f32_16x16x32_f16 v[244:247], v[176:179], v[188:191], v[244:247]\n"
  "v_mfma_f32_16x16x32_f16 v[244:247], v[172:175], v[192:195], v[244:247]\n"
  "v_mfma_f32_16x16x32_f16 v[244:247], v[168:171], v[196:199], v[244:247]\n"
  "s_nop 3\n"
  "ds_write_b128 v29, v[240:243] offset:6144\n"
  "v_mfma_f32_16x16x32_f16 v[244:247], v[164:167], v[200:203], v[244:247]\n"
  "v_mfma_f32_16x16x32_f16 v[244:247], v[160:163], v[204:207], v[244:247]\n"
  "v_mfma_f32_16x16x32_f16 v[244:247], v[156:159], v[208:211], v[244:247]\n"
  "v_mfma_f32_16x16x32_f16 v[244:247], v[152:155], v[212:215], v[244:247]\n"
  "s_nop 7\n"
  "ds_write_b128 v29, v[244:247] offset:7168\n"
  "s_waitcnt lgkmcnt(0)\n"
  "s_barrier\n"
  "ds_read_b128 v[56:59], v52 offset:0\n"
  "ds_read_b128 v[60:63], v52 offset:256\n"
  "ds_read_b128 v[64:67], v52 offset:8192\n"
  "ds_read_b128 v[68:71], v52 offset:8448\n"
  "ds_read_b128 v[72:75], v52 offset:1024\n"
  "ds_read_b128 v[76:79], v52 offset:1280\n"
  "ds_read_b128 v[80:83], v52 offset:9216\n"
  "ds_read_b128 v[84:87], v52 offset:9472\n"
  "s_waitcnt lgkmcnt(4)\n"
  "v_pk_add_f32 v[62:63], v[62:63], v[70:71]\n"
  "v_pk_add_f32 v[60:61], v[60:61], v[68:69]\n"
  "v_pk_add_f32 v[58:59], v[58:59], v[66:67]\n"
  "v_pk_add_f32 v[56:57], v[56:57], v[64:65]\n"
  "v_pk_mul_f32 v[62:63], v[62:63], v[28:29] op_sel_hi:[1,0]\n"
  "v_pk_mul_f32 v[60:61], v[60:61], v[28:29] op_sel_hi:[1,0]\n"
  "v_pk_mul_f32 v[58:59], v[58:59], v[28:29] op_sel_hi:[1,0]\n"
  "v_pk_mul_f32 v[56:57], v[56:57], v[28:29] op_sel_hi:[1,0]\n"
  "v_cvt_pk_bf16_f32 v251, v62, v63\n"
  "v_cvt_pk_bf16_f32 v250, v60, v61\n"
  "v_cvt_pk_bf16_f32 v249, v58, v59\n"
  "v_cvt_pk_bf16_f32 v248, v56, v57\n"
  "v_or_b32 v248, v4, v248\n"
  "v_or_b32 v249, v5, v249\n"
  "v_or_b32 v250, v6, v250\n"
  "v_or_b32 v251, v7, v251\n"
  "buffer_store_dwordx4 v[248:251], v22, s[4:7], 0 offen sc1\n"
  "s_waitcnt lgkmcnt(0)\n"
  "v_pk_add_f32 v[78:79], v[78:79], v[86:87]\n"
  "v_pk_add_f32 v[76:77], v[76:77], v[84:85]\n"
  "v_pk_add_f32 v[74:75], v[74:75], v[82:83]\n"
  "v_pk_add_f32 v[72:73], v[72:73], v[80:81]\n"
  "v_pk_mul_f32 v[78:79], v[78:79], v[28:29] op_sel_hi:[1,0]\n"
  "v_pk_mul_f32 v[76:77], v[76:77], v[28:29] op_sel_hi:[1,0]\n"
  "v_pk_mul_f32 v[74:75], v[74:75], v[28:29] op_sel_hi:[1,0]\n"
  "v_pk_mul_f32 v[72:73], v[72:73], v[28:29] op_sel_hi:[1,0]\n"
  "v_cvt_pk_bf16_f32 v255, v78, v79\n"
  "v_cvt_pk_bf16_f32 v254, v76, v77\n"
  "v_cvt_pk_bf16_f32 v253, v74, v75\n"
  "v_cvt_pk_bf16_f32 v252, v72, v73\n"
  "v_or_b32 v252, v4, v252\n"
  "v_or_b32 v253, v5, v253\n"
  "v_or_b32 v254, v6, v254\n"
  "v_or_b32 v255, v7, v255\n"
  "buffer_store_dwordx4 v[252:255], v22, s[4:7], s77 offen sc1\n"
  "s_mul_i32 s86, s52, 1984\n"
  "v_add_u32 v24, s86, v24\n"
  "s_mov_b32 s33, 0x60006000\n"
  "v_bfe_u32 v35, v0, 4, 2\n"
  "v_lshlrev_b32 v35, 3, v35\n"
  "s_lshl_b32 s88, s60, 7\n"
  "s_lshl_b32 s89, s56, 6\n"
  "s_add_i32 s89, s88, s89\n"
  "buffer_load_dwordx2 v[248:249], v35, s[36:39], s89 offen\n"
  "buffer_load_dwordx2 v[250:251], v35, s[36:39], s89 offen offset:32\n"
  "s_lshl_b32 s88, s53, 6\n"
  "v_or_b32 v34, s88, v25\n"
  "v_lshrrev_b32 v34, 2, v34\n"
  "global_load_dword v252, v34, s[34:35]\n"
  "v_and_b32 v52, 0xf8, v0\n"
  "v_bfe_u32 v34, v0, 1, 2\n"
  "v_or_b32 v52, v52, v34\n"
  "v_lshlrev_b32 v52, 7, v52\n"
  "v_and_b32 v34, 1, v0\n"
  "v_lshl_or_b32 v52, v34, 4, v52\n"
  "s_lshr_b32 s86, s53, 3\n"
  "s_and_b32 s87, s53, 1\n"
  "s_lshl1_add_u32 s86, s86, s87\n"
  "s_mul_i32 s86, s86, 32768\n"
  "s_and_b32 s87, s53, 6\n"
  "s_lshl4_add_u32 s86, s87, s86\n"
  "s_add_i32 s86, s86, 0x290000\n"
  "v_add_u32 v52, s86, v52\n"
  "s_add_i32 s88, s65, 0x200000\n"
  "buffer_load_dword v30, v13, s[12:15], s88 offen\n"
  "s_waitcnt vmcnt(4)\n"
  "s_nop 0\n"
  "buffer_load_dwordx4 v[64:67], v52, s[4:7], 0 offen sc1\n"
  "buffer_load_dwordx4 v[68:71], v52, s[4:7], 0 offen offset:512 sc1\n"
  "v_and_b32 v34, 0x1f0, v1\n"
  "s_mov_b32 m0, s22\n"
  "s_add_i32 s66, s65, 0x0\n"
  "buffer_load_dwordx4 v16, s[12:15], s66 offen offset:0 lds\n"
  "s_mov_b32 s88, 262144\n"
  "v_mad_u32_u24 v3, v3, s88, v34\n"
  "s_lshl_b32 s88, s53, 9\n"
  "v_add_u32 v3, s88, v3\n"
  "v_and_b32 v156, 15, v0\n"
  "buffer_load_dwordx4 v17, s[12:15], s66 offen offset:1024 lds\n"
  "s_lshl_b32 s84, s57, 10\n"
  "v_or_b32_e64 v172, s84, v1\n"
  "v_xor_b32 v173, 0x400, v172\n"
  "s_lshl_b32 s84, s57, 8\n"
  "v_lshl_or_b32 v168, v156, 4, s84\n"
  "v_lshl_or_b32 v168, v35, 6, v168\n"
  "v_add_u32 v170, 131072, v168\n"
  "v_add_u32 v174, 114688, v172\n"
  "buffer_load_dwordx4 v18, s[12:15], s66 offen offset:2048 lds\n"
  "v_add_u32 v175, 114688, v173\n"
  "v_sub_u32 v156, v156, v35\n"
  "v_mov_b32 v157, 0x3f80\n"
  "v_mov_b32 v158, 1.0\n"
  "v_and_b32 v159, 16, v0\n"
  "v_lshl_or_b32 v159, v33, 7, v159\n"
  "v_xor_b32 v159, v159, v1\n"
  "v_bfe_u32 v29, v0, 2, 4\n"
  "buffer_load_dwordx4 v19, s[12:15], s66 offen offset:3072 lds\n"
  "v_and_b32 v27, 2, v0\n"
  "v_lshlrev_b32 v27, 2, v27\n"
  "v_and_or_b32 v27, v0, 1, v27\n"
  "v_xor_b32 v27, v27, v29\n"
  "v_lshlrev_b32 v27, 4, v27\n"
  "v_lshl_or_b32 v27, v25, 4, v27\n"
  "v_lshl_or_b32 v29, v29, 10, v25\n"
  "s_mul_i32 s84, s52, 8192\n"
  "v_add_u32 v26, s84, v159\n"
  "v_add_u32 v27, s84, v27\n"
  "s_mul_i32 s73, s61, 229376\n"
  "s_mul_i32 s84, s52, 4096\n"
  "s_add_i32 s73, s73, s84\n"
  "s_add_i32 s73, s73, 0xa10000\n"
  "s_lshl1_add_u32 s74, s62, s56\n"
  "s_lshl_b32 s50, s74, 9\n"
  "s_add_i32 s50, s50, 0x790000\n"
  "s_lshl_b32 s51, s53, 7\n"
  "s_add_i32 s51, s51, 0x790000\n"
  "s_cmp_eq_u32 s52, 0\n"
  "s_cselect_b32 s51, s51, 0x7fffffff\n"
  "s_lshl_b32 s74, s74, 11\n"
  "s_mul_i32 s84, s61, 262144\n"
  "s_add_i32 s74, s74, s84\n"
  "s_add_i32 s74, s74, 0x510000\n"
  "s_lshl1_add_u32 s75, s62, s56\n"
  "s_lshl_b32 s75, s75, 9\n"
  "s_lshl1_add_u32 s75, s61, s75\n"
  "s_add_i32 s75, s75, s57\n"
  "s_lshl_b32 s75, s75, 6\n"
  "s_add_i32 s0, s73, 0x4000\n"
  "s_add_i32 s1, s73, 0x8000\n"
  "s_add_i32 s3, s73, 0xc000\n"
  "s_add_i32 s94, s73, 0x10000\n"
  "s_add_i32 s95, s73, 0x14000\n"
  "s_add_i32 s30, s73, 0x18000\n"
  "v_and_b32 v156, 15, v156\n"
  "v_cmp_eq_u32 vcc, 0, v156\n"
  "v_cmp_eq_u32 s[84:85], 1, v156\n"
  "s_nop 0\n"
  "v_cndmask_b32 v160, 0, v157, vcc\n"
  "v_cndmask_b32 v160, v160, v158, s[84:85]\n"
  "v_cmp_eq_u32 vcc, 2, v156\n"
  "v_cmp_eq_u32 s[84:85], 3, v156\n"
  "s_nop 0\n"
  "v_cndmask_b32 v161, 0, v157, vcc\n"
  "v_cndmask_b32 v161, v161, v158, s[84:85]\n"
  "v_cmp_eq_u32 vcc, 4, v156\n"
  "v_cmp_eq_u32 s[84:85], 5, v156\n"
  "s_nop 0\n"
  "v_cndmask_b32 v162, 0, v157, vcc\n"
  "v_cndmask_b32 v162, v162, v158, s[84:85]\n"
  "v_cmp_eq_u32 vcc, 6, v156\n"
  "v_cmp_eq_u32 s[84:85], 7, v156\n"
  "s_nop 0\n"
  "v_cndmask_b32 v163, 0, v157, vcc\n"
  "v_cndmask_b32 v163, v163, v158, s[84:85]\n"
  "v_cmp_eq_u32_e64 vcc, 0, v33\n"
  "s_nop 1\n"
  "v_cndmask_b32 v164, v160, 0, vcc\n"
  "v_cndmask_b32 v165, v161, 0, vcc\n"
  "v_cndmask_b32 v166, v162, 0, vcc\n"
  "v_cndmask_b32 v167, v163, 0, vcc\n"
  "v_cndmask_b32 v160, 0, v160, vcc\n"
  "v_cndmask_b32 v161, 0, v161, vcc\n"
  "v_cndmask_b32 v162, 0, v162, vcc\n"
  "v_cndmask_b32 v163, 0, v163, vcc\n"
  "v_and_b32 v156, 2, v0\n"
  "v_cmp_eq_u32 vcc, 0, v156\n"
  "s_nop 1\n"
  "v_cndmask_b32 v34, v5, v4, vcc\n"
  "v_cndmask_b32 v35, v7, v6, vcc\n"
  "s_xor_b32 s84, s52, 1\n"
  "s_mul_i32 s84, s84, 8192\n"
  "s_add_i32 s84, s84, 0\n"
  "v_add_u32 v20, s84, v159\n"
  "s_xor_b32 s84, s52, 2\n"
  "s_mul_i32 s84, s84, 8192\n"
  "s_add_i32 s84, s84, 2048\n"
  "v_add_u32 v21, s84, v159\n"
  "s_xor_b32 s84, s52, 3\n"
  "s_mul_i32 s84, s84, 8192\n"
  "s_add_i32 s84, s84, 4096\n"
  "v_add_u32 v22, s84, v159\n"
  "s_movk_i32 s80, 0x6b80\n"
  "s_mov_b32 s81, 0x6b800000\n"
  "v_mov_b32 v56, s83\n"
  "s_mov_b32 s32, 0x6b800000\n"
  "s_mov_b32 s86, 0x13800000\n"
  "s_mov_b32 s87, 0x13800000\n"
  "s_mov_b32 s70, 0x510000\n"
  "s_mov_b32 s84, 0x530000\n"
  "s_waitcnt vmcnt(4)\n"
  "v_xor_b32 v68, v4, v68\n"
  "v_xor_b32 v69, v5, v69\n"
  "v_xor_b32 v70, v6, v70\n"
  "v_xor_b32 v71, v7, v71\n"
  "v_xor_b32 v64, v4, v64\n"
  "v_xor_b32 v65, v5, v65\n"
  "v_xor_b32 v66, v6, v66\n"
  "v_xor_b32_e64 v67, v7, v67\n"
  "v_or3_b32 v58, v68, v69, v70\n"
  "v_or3_b32 v58, v58, v71, v64\n"
  "v_or3_b32 v58, v58, v65, v66\n"
  "v_bitop3_b32 v58, v58, s33, v67 bitop3:0xc8\n"
  "v_cmp_ne_u32 vcc, 0, v58\n"
  "s_cbranch_vccnz Lslow63\n"
  "LbackYi:\n"
  "v_dot2c_f32_bf16 v36, s80, v64\n"
  "v_dot2c_f32_bf16 v37, s81, v64\n"
  "v_dot2c_f32_bf16 v44, s80, v68\n"
  "v_dot2c_f32_bf16 v45, s81, v68\n"
  "v_dot2c_f32_bf16 v38, s80, v65\n"
  "v_dot2c_f32_bf16 v39, s81, v65\n"
  "v_dot2c_f32_bf16 v46, s80, v69\n"
  "v_dot2c_f32_bf16 v47, s81, v69\n"
  "v_dot2c_f32_bf16 v40, s80, v66\n"
  "v_dot2c_f32_bf16 v41, s81, v66\n"
  "v_dot2c_f32_bf16 v48, s80, v70\n"
  "v_dot2c_f32_bf16 v49, s81, v70\n"
  "v_dot2c_f32_bf16 v42, s80, v67\n"
  "v_dot2c_f32_bf16 v43, s81, v67\n"
  "v_dot2c_f32_bf16 v50, s80, v71\n"
  "v_dot2c_f32_bf16 v51, s81, v71\n"
  "v_pk_mul_f32 v[192:193], v[36:37], v[36:37]\n"
  "v_pk_mul_f32 v[194:195], v[38:39], v[38:39]\n"
  "v_pk_mul_f32 v[196:197], v[40:41], v[40:41]\n"
  "v_pk_mul_f32 v[198:199], v[42:43], v[42:43]\n"
  "v_pk_add_f32 v[184:185], v[36:37], v[44:45]\n"
  "v_pk_add_f32 v[186:187], v[38:39], v[46:47]\n"
  "v_pk_add_f32 v[188:189], v[40:41], v[48:49]\n"
  "v_pk_add_f32 v[190:191], v[42:43], v[50:51]\n"
  "v_pk_fma_f32 v[192:193], v[44:45], v[44:45], v[192:193]\n"
  "v_pk_fma_f32 v[194:195], v[46:47], v[46:47], v[194:195]\n"
  "v_pk_fma_f32 v[196:197], v[48:49], v[48:49], v[196:197]\n"
  "v_pk_fma_f32 v[198:199], v[50:51], v[50:51], v[198:199]\n"
  "v_permlane32_swap_b32 v184, v192\n"
  "v_permlane32_swap_b32 v185, v193\n"
  "v_permlane32_swap_b32 v186, v194\n"
  "v_permlane32_swap_b32 v187, v195\n"
  "v_permlane32_swap_b32 v188, v196\n"
  "v_permlane32_swap_b32 v189, v197\n"
  "v_permlane32_swap_b32 v190, v198\n"
  "v_permlane32_swap_b32 v191, v199\n"
  "v_pk_add_f32 v[184:185], v[184:185], v[192:193]\n"
  "v_pk_add_f32 v[188:189], v[188:189], v[196:197]\n"
  "v_pk_add_f32 v[186:187], v[186:187], v[194:195]\n"
  "v_pk_add_f32 v[190:191], v[190:191], v[198:199]\n"
  "v_permlane16_swap_b32 v184, v188\n"
  "v_permlane16_swap_b32 v185, v189\n"
  "v_permlane16_swap_b32 v186, v190\n"
  "v_permlane16_swap_b32 v187, v191\n"
  "v_pk_add_f32 v[184:185], v[184:185], v[188:189]\n"
  "v_pk_add_f32 v[186:187], v[186:187], v[190:191]\n"
  "v_lshlrev_b32_e64 v140, 16, v252\n"
  "v_add_f32_dpp v192, v184, v184 row_ror:8 row_mask:0xf bank_mask:0x3\n"
  "v_add_f32_dpp v193, v185, v185 row_ror:8 row_mask:0xf bank_mask:0x3\n"
  "v_and_b32 v141, 0xffff0000, v252\n"
  "v_add_f32_dpp v192, v186, v186 row_ror:8 row_mask:0xf bank_mask:0xc\n"
  "v_add_f32_dpp v193, v187, v187 row_ror:8 row_mask:0xf bank_mask:0xc\n"
  "v_lshlrev_b32 v142, 1, v25\n"
  "v_add_f32_dpp v194, v192, v192 row_half_mirror row_mask:0xf bank_mask:0x5\n"
  "v_add_f32 v140, v32, v140\n"
  "v_add_f32 v141, v31, v141\n"
  "v_add_f32_dpp v194, v193, v193 row_half_mirror row_mask:0xf bank_mask:0xa\n"
  "v_mov_b32 v145, s48\n"
  "v_mov_b32 v147, s49\n"
  "v_add_f32_dpp v195, v194, v194 quad_perm:[1,0,3,2] row_mask:0xf bank_mask:0xf\n"
  "v_mov_b32 v149, s48\n"
  "v_mov_b32_e64 v151, s49\n"
  "v_add_f32_dpp v194, v195, v195 quad_perm:[2,3,0,1] row_mask:0xf bank_mask:0xf\n"
  "ds_write_b32 v24, v194 offset:0\n"
  "s_waitcnt lgkmcnt(0)\n"
  "s_barrier\n"
  "ds_read_b128 v[200:203], v25 offset:0\n"
  "ds_read_b128 v[204:207], v25 offset:2048\n"
  "ds_read_b128 v[208:211], v25 offset:4096\n"
  "ds_read_b128 v[212:215], v25 offset:6144\n"
  "s_waitcnt lgkmcnt(0)\n"
  "s_mov_b32 m0, s23\n"
  "s_add_i32 s66, s66, 0x20000\n"
  "buffer_load_dwordx4 v16, s[12:15], s66 offen offset:0 lds\n"
  "v_pk_add_f32 v[200:201], v[200:201], v[204:205]\n"
  "v_pk_add_f32 v[202:203], v[202:203], v[206:207]\n"
  "v_pk_add_f32 v[208:209], v[208:209], v[212:213]\n"
  "v_pk_add_f32 v[210:211], v[210:211], v[214:215]\n"
  "s_nop 0\n"
  "v_pk_add_f32 v[202:203], v[202:203], v[210:211]\n"
  "v_pk_add_f32 v[200:201], v[200:201], v[208:209]\n"
  "v_pk_mul_f32 v[202:203], v[202:203], s[82:83] op_sel_hi:[1,0]\n"
  "v_pk_mul_f32 v[136:137], v[200:201], s[82:83] op_sel_hi:[1,0]\n"
  "buffer_load_dwordx4 v17, s[12:15], s66 offen offset:1024 lds\n"
  "v_pk_fma_f32 v[202:203], v[136:137], v[136:137], v[202:203] neg_lo:[1,0,0] neg_hi:[1,0,0]\n"
  "s_nop 0\n"
  "v_max_f32 v202, 0, v202\n"
  "v_max_f32 v203, 0, v203\n"
  "v_pk_add_f32 v[204:205], v[202:203], v[56:57] op_sel_hi:[1,0]\n"
  "s_nop 0\n"
  "v_pk_fma_f32 v[138:139], v[204:205], v[56:57], v[202:203] op_sel_hi:[1,0,1]\n"
  "s_nop 0\n"
  "v_rsq_f32 v202, v138\n"
  "v_rsq_f32_e64 v203, v139\n"
  "s_nop 0\n"
  "v_pk_mul_f32 v[202:203], v[202:203], s[86:87] op_sel_hi:[1,0]\n"
  "buffer_load_dwordx4 v18, s[12:15], s66 offen offset:2048 lds\n"
  "v_pk_mul_f32 v[200:201], v[136:137], v[202:203] neg_lo:[1,0] neg_hi:[1,0]\n"
  "v_mov_b64_dpp v[216:217], v[202:203] row_newbcast:0 row_mask:0xf bank_mask:0xf\n"
  "v_mov_b64_dpp v[218:219], v[202:203] row_newbcast:1 row_mask:0xf bank_mask:0xf\n"
  "v_mov_b64_dpp v[220:221], v[202:203] row_newbcast:2 row_mask:0xf bank_mask:0xf\n"
  "v_mov_b64_dpp v[222:223], v[202:203] row_newbcast:3 row_mask:0xf bank_mask:0xf\n"
  "v_mov_b64_dpp v[224:225], v[200:201] row_newbcast:0 row_mask:0xf bank_mask:0xf\n"
  "v_mov_b64_dpp v[226:227], v[200:201] row_newbcast:1 row_mask:0xf bank_mask:0xf\n"
  "v_mov_b64_dpp v[228:229], v[200:201] row_newbcast:2 row_mask:0xf bank_mask:0xf\n"
  "buffer_load_dwordx4 v19, s[12:15], s66 offen offset:3072 lds\n"
  "v_mov_b64_dpp v[230:231], v[200:201] row_newbcast:3 row_mask:0xf bank_mask:0xf\n"
  "v_pk_fma_f32 v[200:201], v[36:37], v[216:217], v[224:225]\n"
  "v_pk_fma_f32 v[202:203], v[38:39], v[218:219], v[226:227]\n"
  "v_pk_fma_f32 v[204:205], v[40:41], v[220:221], v[228:229]\n"
  "v_pk_fma_f32 v[206:207], v[42:43], v[222:223], v[230:231]\n"
  "v_cvt_pk_bf16_f32 v184, v200, v201\n"
  "v_cvt_pk_bf16_f32 v185, v202, v203\n"
  "v_cvt_pk_bf16_f32 v186, v204, v205\n"
  "v_cvt_pk_bf16_f32 v187, v206, v207\n"
  "v_or_b32 v184, v34, v184\n"
  "v_or_b32 v186, v35, v186\n"
  "buffer_store_dwordx4 v[184:187], v3, s[4:7], s70 offen sc1\n"
  "v_pk_fma_f32 v[208:209], v[44:45], v[216:217], v[224:225]\n"
  "v_pk_fma_f32 v[210:211], v[46:47], v[218:219], v[226:227]\n"
  "v_pk_fma_f32 v[212:213], v[48:49], v[220:221], v[228:229]\n"
  "v_pk_fma_f32 v[214:215], v[50:51], v[222:223], v[230:231]\n"
  "v_cvt_pk_bf16_f32 v188, v208, v209\n"
  "v_cvt_pk_bf16_f32 v189, v210, v211\n"
  "v_cvt_pk_bf16_f32 v190, v212, v213\n"
  "v_cvt_pk_bf16_f32 v191, v214, v215\n"
  "v_or_b32 v188, v34, v188\n"
  "v_or_b32 v190, v35, v190\n"
  "buffer_store_dwordx4 v[188:191], v3, s[4:7], s84 offen sc1\n"
  "v_sqrt_f32 v144, v138\n"
  "v_sqrt_f32 v148, v139\n"
  "v_pk_add_f32 v[136:137], v[136:137], v[140:141]\n"
  "v_and_b32 v32, 0xf0, v1\n"
  "v_mul_f32 v146, s87, v136\n"
  "v_mul_f32 v150, s87, v137\n"
  "s_mov_b64 exec, 15\n"
  "buffer_store_dwordx4 v[144:147], v142, s[4:7], s51 offen sc1\n"
  "buffer_store_dwordx4 v[148:151], v142, s[4:7], s51 offen offset:16 sc1\n"
  "s_mov_b64 exec, -1\n"
  "s_add_i32 s88, s65, 0x300000\n"
  "buffer_load_dword v30, v13, s[12:15], s88 offen\n"
  "s_add_i32 s88, s65, 0x400000\n"
  "buffer_load_dword v30, v13, s[12:15], s88 offen\n"
  "s_add_i32 s88, s65, 0x500000\n"
  "buffer_load_dword v30, v13, s[12:15], s88 offen\n"
  "s_add_i32 s88, s65, 0x600000\n"
  "buffer_load_dword v30, v13, s[12:15], s88 offen\n"
  "s_mov_b32 m0, s24\n"
  "s_add_i32 s66, s65, 0x100000\n"
  "buffer_load_dwordx4 v16, s[12:15], s66 offen offset:0 lds\n"
  "buffer_load_dwordx4 v17, s[12:15], s66 offen offset:1024 lds\n"
  "buffer_load_dwordx4 v18, s[12:15], s66 offen offset:2048 lds\n"
  "buffer_load_dwordx4 v19, s[12:15], s66 offen offset:3072 lds\n"
  "s_mov_b32 m0, s25\n"
  "s_add_i32 s66, s66, 0x20000\n"
  "buffer_load_dwordx4 v16, s[12:15], s66 offen offset:0 lds\n"
  "buffer_load_dwordx4 v17, s[12:15], s66 offen offset:1024 lds\n"
  "buffer_load_dwordx4 v18, s[12:15], s66 offen offset:2048 lds\n"
  "buffer_load_dwordx4 v19, s[12:15], s66 offen offset:3072 lds\n"
  "s_mov_b32 m0, s26\n"
  "s_add_i32 s66, s65, 0x200000\n"
  "buffer_load_dwordx4 v16, s[12:15], s66 offen offset:0 lds\n"
  "buffer_load_dwordx4 v17, s[12:15], s66 offen offset:1024 lds\n"
  "buffer_load_dwordx4 v18, s[12:15], s66 offen offset:2048 lds\n"
  "buffer_load_dwordx4 v19, s[12:15], s66 offen offset:3072 lds\n"
  "s_mov_b32 m0, s27\n"
  "s_add_i32 s66, s66, 0x20000\n"
  "buffer_load_dwordx4 v16, s[12:15], s66 offen offset:0 lds\n"
  "buffer_load_dwordx4 v17, s[12:15], s66 offen offset:1024 lds\n"
  "buffer_load_dwordx4 v18, s[12:15], s66 offen offset:2048 lds\n"
  "buffer_load_dwordx4 v19, s[12:15], s66 offen offset:3072 lds\n"
  "s_mov_b32 m0, s21\n"
  "s_add_i32 s66, s65, 0x300000\n"
  "buffer_load_dwordx4 v16, s[12:15], s66 offen offset:0 lds\n"
  "buffer_load_dwordx4 v17, s[12:15], s66 offen offset:1024 lds\n"
  "buffer_load_dwordx4 v18, s[12:15], s66 offen offset:2048 lds\n"
  "buffer_load_dwordx4 v19, s[12:15], s66 offen offset:3072 lds\n"
  "s_mov_b32 m0, s20\n"
  "s_add_i32 s66, s66, 0x20000\n"
  "buffer_load_dwordx4 v16, s[12:15], s66 offen offset:0 lds\n"
  "buffer_load_dwordx4 v17, s[12:15], s66 offen offset:1024 lds\n"
  "buffer_load_dwordx4 v18, s[12:15], s66 offen offset:2048 lds\n"
  "buffer_load_dwordx4 v19, s[12:15], s66 offen offset:3072 lds\n"
  "s_waitcnt vmcnt(36)\n"
  "ds_read_b64_tr_b16 a[0:1], v8 offset:32768\n"
  "ds_read_b64_tr_b16 a[2:3], v8 offset:33280\n"
  "ds_read_b64_tr_b16 a[4:5], v9 offset:32768\n"
  "ds_read_b64_tr_b16 a[6:7], v9 offset:33280\n"
  "ds_read_b64_tr_b16 a[8:9], v10 offset:32768\n"
  "ds_read_b64_tr_b16 a[10:11], v10 offset:33280\n"
  "ds_read_b64_tr_b16 a[12:13], v11 offset:32768\n"
  "ds_read_b64_tr_b16 a[14:15], v11 offset:33280\n"
  "s_mov_b32 m0, s22\n"
  "s_add_i32 s66, s65, 0x400000\n"
  "s_waitcnt lgkmcnt(0)\n"
  "buffer_load_dwordx4 v16, s[12:15], s66 offen offset:0 lds\n"
  "buffer_load_dwordx4 v17, s[12:15], s66 offen offset:1024 lds\n"
  "buffer_load_dwordx4 v18, s[12:15], s66 offen offset:2048 lds\n"
  "buffer_load_dwordx4 v19, s[12:15], s66 offen offset:3072 lds\n"
  "s_waitcnt vmcnt(36)\n"
  "ds_read_b64_tr_b16 a[16:17], v8 offset:49152\n"
  "ds_read_b64_tr_b16 a[18:19], v8 offset:49664\n"
  "ds_read_b64_tr_b16 a[20:21], v9 offset:49152\n"
  "ds_read_b64_tr_b16 a[22:23], v9 offset:49664\n"
  "ds_read_b64_tr_b16 a[24:25], v10 offset:49152\n"
  "ds_read_b64_tr_b16 a[26:27], v10 offset:49664\n"
  "ds_read_b64_tr_b16 a[28:29], v11 offset:49152\n"
  "ds_read_b64_tr_b16 a[30:31], v11 offset:49664\n"
  "s_mov_b32 m0, s23\n"
  "s_add_i32 s66, s66, 0x20000\n"
  "s_waitcnt lgkmcnt(0)\n"
  "buffer_load_dwordx4 v16, s[12:15], s66 offen offset:0 lds\n"
  "buffer_load_dwordx4 v17, s[12:15], s66 offen offset:1024 lds\n"
  "buffer_load_dwordx4 v18, s[12:15], s66 offen offset:2048 lds\n"
  "buffer_load_dwordx4 v19, s[12:15], s66 offen offset:3072 lds\n"
  "s_waitcnt vmcnt(28)\n"
  "ds_read_b64_tr_b16 a[32:33], v23 offset:0\n"
  "ds_read_b64_tr_b16 a[34:35], v23 offset:512\n"
  "ds_read_b64_tr_b16 a[36:37], v53 offset:0\n"
  "ds_read_b64_tr_b16 a[38:39], v53 offset:512\n"
  "ds_read_b64_tr_b16 a[40:41], v54 offset:0\n"
  "ds_read_b64_tr_b16 a[42:43], v54 offset:512\n"
  "ds_read_b64_tr_b16 a[44:45], v55 offset:0\n"
  "ds_read_b64_tr_b16 a[46:47], v55 offset:512\n"
  "s_mov_b32 m0, s24\n"
  "s_add_i32 s66, s65, 0x500000\n"
  "s_waitcnt lgkmcnt(0)\n"
  "buffer_load_dwordx4 v16, s[12:15], s66 offen offset:0 lds\n"
  "buffer_load_dwordx4 v17, s[12:15], s66 offen offset:1024 lds\n"
  "buffer_load_dwordx4 v18, s[12:15], s66 offen offset:2048 lds\n"
  "buffer_load_dwordx4 v19, s[12:15], s66 offen offset:3072 lds\n"
  "s_waitcnt vmcnt(28)\n"
  "ds_read_b64_tr_b16 a[48:49], v23 offset:16384\n"
  "ds_read_b64_tr_b16 a[50:51], v23 offset:16896\n"
  "ds_read_b64_tr_b16 a[52:53], v53 offset:16384\n"
  "ds_read_b64_tr_b16 a[54:55], v53 offset:16896\n"
  "ds_read_b64_tr_b16 a[56:57], v54 offset:16384\n"
  "ds_read_b64_tr_b16 a[58:59], v54 offset:16896\n"
  "ds_read_b64_tr_b16 a[60:61], v55 offset:16384\n"
  "ds_read_b64_tr_b16 a[62:63], v55 offset:16896\n"
  "s_waitcnt vmcnt(24)\n"
  "ds_read_b64_tr_b16 a[64:65], v23 offset:32768\n"
  "ds_read_b64_tr_b16 a[66:67], v23 offset:33280\n"
  "ds_read_b64_tr_b16 a[68:69], v53 offset:32768\n"
  "ds_read_b64_tr_b16 a[70:71], v53 offset:33280\n"
  "ds_read_b64_tr_b16 a[72:73], v54 offset:32768\n"
  "ds_read_b64_tr_b16 a[74:75], v54 offset:33280\n"
  "ds_read_b64_tr_b16 a[76:77], v55 offset:32768\n"
  "ds_read_b64_tr_b16 a[78:79], v55 offset:33280\n"
  "s_waitcnt vmcnt(20)\n"
  "ds_read_b64_tr_b16 a[80:81], v23 offset:49152\n"
  "ds_read_b64_tr_b16 a[82:83], v23 offset:49664\n"
  "ds_read_b64_tr_b16 a[84:85], v53 offset:49152\n"
  "ds_read_b64_tr_b16 a[86:87], v53 offset:49664\n"
  "ds_read_b64_tr_b16 a[88:89], v54 offset:49152\n"
  "ds_read_b64_tr_b16 a[90:91], v54 offset:49664\n"
  "ds_read_b64_tr_b16 a[92:93], v55 offset:49152\n"
  "ds_read_b64_tr_b16 a[94:95], v55 offset:49664\n"
  "s_waitcnt vmcnt(16)\n"
  "ds_read_b64_tr_b16 a[96:97], v8 offset:16384\n"
  "ds_read_b64_tr_b16 a[98:99], v8 offset:16896\n"
  "ds_read_b64_tr_b16 a[100:101], v9 offset:16384\n"
  "ds_read_b64_tr_b16 a[102:103], v9 offset:16896\n"
  "ds_read_b64_tr_b16 a[104:105], v10 offset:16384\n"
  "ds_read_b64_tr_b16 a[106:107], v10 offset:16896\n"
  "ds_read_b64_tr_b16 a[108:109], v11 offset:16384\n"
  "ds_read_b64_tr_b16 a[110:111], v11 offset:16896\n"
  "buffer_load_dwordx4 v[36:39], v168, s[4:7], s74 offen sc1\n"
  "buffer_load_dwordx4 v[40:43], v170, s[4:7], s74 offen sc1\n"
  "buffer_load_dwordx4 v[176:179], v32, s[4:7], s50 offen offset:0 sc1\n"
  "buffer_load_dwordx4 v[180:183], v32, s[4:7], s50 offen offset:256 sc1\n"
  "s_mov_b64 vcc, 1\n"
  "s_branch Lslow64\n"
  "LbackI:\n"
  "s_waitcnt vmcnt(0)\n"
  "v_xor_b32 v183, s49, v183\n"
  "v_xor_b32 v181, s48, v181\n"
  "v_xor_b32 v179, s49, v179\n"
  "v_xor_b32 v177, s48, v177\n"
  "v_xor_b32 v40, v34, v40\n"
  "v_xor_b32 v42, v35, v42\n"
  "v_xor_b32 v36, v34, v36\n"
  "v_xor_b32 v38, v35, v38\n"
  "v_or3_b32 v52, v40, v42, v36\n"
  "v_bitop3_b32 v52, v52, s33, v38 bitop3:0xc8\n"
  "v_or3_b32 v52, v52, v177, v179\n"
  "v_or3_b32 v52, v52, v181, v183\n"
  "v_cmp_ne_u32 vcc, 0, v52\n"
  "s_cbranch_vccnz Lslow65\n"
  "LbackHi:\n"
  "v_mfma_f32_16x16x32_bf16 v[56:59], v[36:39], v[160:163], 0\n"
  "v_mfma_f32_16x16x32_bf16 v[60:63], v[36:39], v[164:167], 0\n"
  "buffer_load_dwordx4 v[192:195], v172, s[4:7], s73 offen offset:0 sc1\n"
  "buffer_load_dwordx4 v[196:199], v173, s[4:7], s73 offen offset:0 sc1\n"
  "buffer_load_dwordx4 v[200:203], v172, s[4:7], s73 offen offset:2048 sc1\n"
  "buffer_load_dwordx4 v[204:207], v173, s[4:7], s73 offen offset:2048 sc1\n"
  "buffer_load_dwordx4 v[208:211], v172, s[4:7], s0 offen offset:0 sc1\n"
  "buffer_load_dwordx4 v[212:215], v173, s[4:7], s0 offen offset:0 sc1\n"
  "buffer_load_dwordx4 v[216:219], v172, s[4:7], s0 offen offset:2048 sc1\n"
  "buffer_load_dwordx4 v[220:223], v173, s[4:7], s0 offen offset:2048 sc1\n"
  "v_fma_f32 v56, v56, v176, v178\n"
  "v_fma_f32 v57, v57, v176, v178\n"
  "v_fma_f32 v58, v58, v176, v178\n"
  "v_fma_f32 v59, v59, v176, v178\n"
  "v_fma_f32 v60, v60, v180, v182\n"
  "v_fma_f32 v61, v61, v180, v182\n"
  "v_fma_f32 v62, v62, v180, v182\n"
  "v_fma_f32 v63, v63, v180, v182\n"
  "s_waitcnt vmcnt(7)\n"
  "v_xor_b32 v192, v34, v192\n"
  "v_xor_b32 v194, v35, v194\n"
  "s_waitcnt vmcnt(6)\n"
  "v_xor_b32 v196, v34, v196\n"
  "v_xor_b32 v198, v35, v198\n"
  "v_or3_b32 v52, v192, v194, v196\n"
  "v_bitop3_b32 v52, v52, s33, v198 bitop3:0xc8\n"
  "v_cmp_ne_u32 vcc, 0, v52\n"
  "s_cbranch_vccnz Lslow66\n"
  "Lb66:\n"
  "v_mfma_f32_16x16x32_bf16 v[76:79], v[192:195], a[12:15], 0\n"
  "s_waitcnt vmcnt(5)\n"
  "v_xor_b32 v200, v34, v200\n"
  "v_xor_b32 v202, v35, v202\n"
  "v_mfma_f32_16x16x32_bf16 v[84:87], v[196:199], a[12:15], 0\n"
  "buffer_load_dwordx4 v[224:227], v172, s[4:7], s1 offen offset:0 sc1\n"
  "v_mfma_f32_16x16x32_bf16 v[80:83], v[196:199], a[8:11], 0\n"
  "s_waitcnt vmcnt(5)\n"
  "v_xor_b32 v204, v34, v204\n"
  "v_xor_b32 v206, v35, v206\n"
  "v_mfma_f32_16x16x32_bf16 v[72:75], v[192:195], a[8:11], 0\n"
  "buffer_load_dwordx4 v[228:231], v173, s[4:7], s1 offen offset:0 sc1\n"
  "ds_read_b64_tr_b16 a[112:113], v8 offset:0\n"
  "ds_read_b64_tr_b16 a[114:115], v8 offset:512\n"
  "ds_read_b64_tr_b16 a[116:117], v9 offset:0\n"
  "ds_read_b64_tr_b16 a[118:119], v9 offset:512\n"
  "v_mfma_f32_16x16x32_bf16 v[60:63], v[192:195], a[4:7], v[60:63]\n"
  "v_or3_b32 v52, v200, v202, v204\n"
  "v_bitop3_b32 v52, v52, s33, v206 bitop3:0xc8\n"
  "v_mfma_f32_16x16x32_bf16 v[68:71], v[196:199], a[4:7], 0\n"
  "v_cmp_ne_u32 vcc, 0, v52\n"
  "s_mov_b32 m0, s25\n"
  "s_add_i32 s66, s66, 0x20000\n"
  "s_waitcnt lgkmcnt(4)\n"
  "buffer_load_dwordx4 v16, s[12:15], s66 offen offset:0 lds\n"
  "v_mfma_f32_16x16x32_bf16 v[64:67], v[196:199], a[0:3], 0\n"
  "ds_read_b64_tr_b16 a[120:121], v10 offset:0\n"
  "ds_read_b64_tr_b16 a[122:123], v10 offset:512\n"
  "ds_read_b64_tr_b16 a[124:125], v11 offset:0\n"
  "ds_read_b64_tr_b16 a[126:127], v11 offset:512\n"
  "v_mfma_f32_16x16x32_bf16 v[56:59], v[192:195], a[0:3], v[56:59]\n"
  "buffer_load_dwordx4 v[232:235], v172, s[4:7], s1 offen offset:2048 sc1\n"
  "buffer_load_dwordx4 v[236:239], v173, s[4:7], s1 offen offset:2048 sc1\n"
  "s_cbranch_vccnz Lslow67\n"
  "Lb67:\n"
  "v_mfma_f32_16x16x32_bf16 v[76:79], v[200:203], a[28:31], v[76:79]\n"
  "s_waitcnt vmcnt(8)\n"
  "v_xor_b32 v208, v34, v208\n"
  "v_xor_b32 v210, v35, v210\n"
  "v_mfma_f32_16x16x32_bf16 v[84:87], v[204:207], a[28:31], v[84:87]\n"
  "buffer_load_dwordx4 v17, s[12:15], s66 offen offset:1024 lds\n"
  "v_mfma_f32_16x16x32_bf16 v[80:83], v[204:207], a[24:27], v[80:83]\n"
  "s_waitcnt vmcnt(8)\n"
  "v_xor_b32 v212, v34, v212\n"
  "v_xor_b32 v214, v35, v214\n"
  "v_mfma_f32_16x16x32_bf16 v[72:75], v[200:203], a[24:27], v[72:75]\n"
  "buffer_load_dwordx4 v[240:243], v172, s[4:7], s3 offen offset:0 sc1\n"
  "ds_read_b64_tr_b16 a[128:129], v8 offset:32768\n"
  "ds_read_b64_tr_b16 a[130:131], v8 offset:33280\n"
  "ds_read_b64_tr_b16 a[132:133], v9 offset:32768\n"
  "ds_read_b64_tr_b16 a[134:135], v9 offset:33280\n"
  "v_mfma_f32_16x16x32_bf16 v[60:63], v[200:203], a[20:23], v[60:63]\n"
  "v_or3_b32 v52, v208, v210, v212\n"
  "v_bitop3_b32 v52, v52, s33, v214 bitop3:0xc8\n"
  "v_mfma_f32_16x16x32_bf16 v[68:71], v[204:207], a[20:23], v[68:71]\n"
  "v_cmp_ne_u32_e64 vcc, 0, v52\n"
  "buffer_load_dwordx4 v[244:247], v173, s[4:7], s3 offen offset:0 sc1\n"
  "v_mfma_f32_16x16x32_bf16 v[64:67], v[204:207], a[16:19], v[64:67]\n"
  "ds_read_b64_tr_b16 a[136:137], v10 offset:32768\n"
  "ds_read_b64_tr_b16 a[138:139], v10 offset:33280\n"
  "ds_read_b64_tr_b16 a[140:141], v11 offset:32768\n"
  "ds_read_b64_tr_b16 a[142:143], v11 offset:33280\n"
  "v_mfma_f32_16x16x32_bf16 v[56:59], v[200:203], a[16:19], v[56:59]\n"
  "buffer_load_dwordx4 v18, s[12:15], s66 offen offset:2048 lds\n"
  "buffer_load_dwordx4 v[248:251], v172, s[4:7], s3 offen offset:2048 sc1\n"
  "s_cbranch_vccnz Lslow68\n"
  "Lb68:\n"
  "v_mfma_f32_16x16x32_bf16 v[76:79], v[208:211], a[44:47], v[76:79]\n"
  "s_waitcnt vmcnt(11)\n"
  "v_xor_b32 v216, v34, v216\n"
  "v_xor_b32 v218, v35, v218\n"
  "v_mfma_f32_16x16x32_bf16 v[84:87], v[212:215], a[44:47], v[84:87]\n"
  "buffer_load_dwordx4 v[252:255], v173, s[4:7], s3 offen offset:2048 sc1\n"
  "v_mfma_f32_16x16x32_bf16 v[80:83], v[212:215], a[40:43], v[80:83]\n"
  "s_waitcnt vmcnt(11)\n"
  "v_xor_b32 v220, v34, v220\n"
  "v_xor_b32_e64 v222, v35, v222\n"
  "v_mfma_f32_16x16x32_bf16 v[72:75], v[208:211], a[40:43], v[72:75]\n"
  "buffer_load_dwordx4 v19, s[12:15], s66 offen offset:3072 lds\n"
  "ds_read_b64_tr_b16 a[144:145], v8 offset:49152\n"
  "ds_read_b64_tr_b16 a[146:147], v8 offset:49664\n"
  "ds_read_b64_tr_b16 a[148:149], v9 offset:49152\n"
  "ds_read_b64_tr_b16 a[150:151], v9 offset:49664\n"
  "v_mfma_f32_16x16x32_bf16 v[60:63], v[208:211], a[36:39], v[60:63]\n"
  "v_or3_b32 v52, v216, v218, v220\n"
  "v_bitop3_b32 v52, v52, s33, v222 bitop3:0xc8\n"
  "v_mfma_f32_16x16x32_bf16 v[68:71], v[212:215], a[36:39], v[68:71]\n"
  "v_cmp_ne_u32_e64 vcc, 0, v52\n"
  "v_mfma_f32_16x16x32_bf16 v[64:67], v[212:215], a[32:35], v[64:67]\n"
  "ds_read_b64_tr_b16 a[152:153], v10 offset:49152\n"
  "ds_read_b64_tr_b16 a[154:155], v10 offset:49664\n"
  "ds_read_b64_tr_b16 a[156:157], v11 offset:49152\n"
  "ds_read_b64_tr_b16 a[158:159], v11 offset:49664\n"
  "v_mfma_f32_16x16x32_bf16 v[56:59], v[208:211], a[32:35], v[56:59]\n"
  "s_cbranch_vccnz Lslow69\n"
  "Lb69:\n"
  "v_mfma_f32_16x16x32_bf16 v[88:91], v[40:43], v[160:163], 0\n"
  "v_mfma_f32_16x16x32_bf16 v[92:95], v[40:43], v[164:167], 0\n"
  "v_mfma_f32_16x16x32_bf16 v[76:79], v[216:219], a[60:63], v[76:79]\n"
  "s_waitcnt vmcnt(11)\n"
  "v_xor_b32 v224, v34, v224\n"
  "v_xor_b32 v226, v35, v226\n"
  "v_mfma_f32_16x16x32_bf16 v[84:87], v[220:223], a[60:63], v[84:87]\n"
  "buffer_load_dwordx4 v[192:195], v172, s[4:7], s94 offen offset:0 sc1\n"
  "v_mfma_f32_16x16x32_bf16 v[80:83], v[220:223], a[56:59], v[80:83]\n"
  "s_waitcnt vmcnt(11)\n"
  "v_xor_b32 v228, v34, v228\n"
  "v_xor_b32_e64 v230, v35, v230\n"
  "v_mfma_f32_16x16x32_bf16 v[72:75], v[216:219], a[56:59], v[72:75]\n"
  "buffer_load_dwordx4 v[196:199], v173, s[4:7], s94 offen offset:0 sc1\n"
  "ds_read_b64_tr_b16 a[160:161], v23 offset:0\n"
  "ds_read_b64_tr_b16 a[162:163], v23 offset:512\n"
  "ds_read_b64_tr_b16 a[164:165], v53 offset:0\n"
  "ds_read_b64_tr_b16 a[166:167], v53 offset:512\n"
  "v_mfma_f32_16x16x32_bf16 v[60:63], v[216:219], a[52:55], v[60:63]\n"
  "v_or3_b32 v52, v224, v226, v228\n"
  "v_bitop3_b32 v52, v52, s33, v230 bitop3:0xc8\n"
  "v_mfma_f32_16x16x32_bf16 v[68:71], v[220:223], a[52:55], v[68:71]\n"
  "v_cmp_ne_u32 vcc, 0, v52\n"
  "s_mov_b32 m0, s26\n"
  "s_add_i32 s66, s65, 0x600000\n"
  "buffer_load_dwordx4 v16, s[12:15], s66 offen offset:0 lds\n"
  "v_mfma_f32_16x16x32_bf16 v[64:67], v[220:223], a[48:51], v[64:67]\n"
  "ds_read_b64_tr_b16 a[168:169], v54 offset:0\n"
  "ds_read_b64_tr_b16 a[170:171], v54 offset:512\n"
  "ds_read_b64_tr_b16 a[172:173], v55 offset:0\n"
  "ds_read_b64_tr_b16 a[174:175], v55 offset:512\n"
  "v_mfma_f32_16x16x32_bf16 v[56:59], v[216:219], a[48:51], v[56:59]\n"
  "s_cbranch_vccnz Lslow70\n"
  "Lb70:\n"
  "v_fma_f32 v88, v88, v176, v178\n"
  "v_fma_f32 v89, v89, v176, v178\n"
  "v_fma_f32 v90, v90, v176, v178\n"
  "v_fma_f32 v91, v91, v176, v178\n"
  "v_fma_f32 v92, v92, v180, v182\n"
  "v_fma_f32 v93, v93, v180, v182\n"
  "v_fma_f32 v94, v94, v180, v182\n"
  "v_fma_f32 v95, v95, v180, v182\n"
  "v_mfma_f32_16x16x32_bf16 v[76:79], v[224:227], a[76:79], v[76:79]\n"
  "s_waitcnt vmcnt(11)\n"
  "v_xor_b32 v232, v34, v232\n"
  "v_xor_b32 v234, v35, v234\n"
  "v_mfma_f32_16x16x32_bf16 v[84:87], v[228:231], a[76:79], v[84:87]\n"
  "buffer_load_dwordx4 v[200:203], v172, s[4:7], s94 offen offset:2048 sc1\n"
  "s_barrier\n"
  "v_mfma_f32_16x16x32_bf16 v[80:83], v[228:231], a[72:75], v[80:83]\n"
  "s_waitcnt vmcnt(11)\n"
  "v_xor_b32 v236, v34, v236\n"
  "v_xor_b32 v238, v35, v238\n"
  "v_mfma_f32_16x16x32_bf16 v[72:75], v[224:227], a[72:75], v[72:75]\n"
  "buffer_load_dwordx4 v[204:207], v173, s[4:7], s94 offen offset:2048 sc1\n"
  "v_mfma_f32_16x16x32_bf16 v[60:63], v[224:227], a[68:71], v[60:63]\n"
  "v_or3_b32 v52, v232, v234, v236\n"
  "v_bitop3_b32 v52, v52, s33, v238 bitop3:0xc8\n"
  "v_mfma_f32_16x16x32_bf16 v[68:71], v[228:231], a[68:71], v[68:71]\n"
  "v_cmp_ne_u32 vcc, 0, v52\n"
  "buffer_load_dwordx4 v17, s[12:15], s66 offen offset:1024 lds\n"
  "v_mfma_f32_16x16x32_bf16 v[64:67], v[228:231], a[64:67], v[64:67]\n"
  "v_mfma_f32_16x16x32_bf16 v[56:59], v[224:227], a[64:67], v[56:59]\n"
  "s_cbranch_vccnz Lslow71\n"
  "Lb71:\n"
  "v_mfma_f32_16x16x32_bf16 v[76:79], v[232:235], a[92:95], v[76:79]\n"
  "s_waitcnt vmcnt(11)\n"
  "v_xor_b32 v240, v34, v240\n"
  "v_xor_b32 v242, v35, v242\n"
  "v_mfma_f32_16x16x32_bf16 v[84:87], v[236:239], a[92:95], v[84:87]\n"
  "buffer_load_dwordx4 v[208:211], v172, s[4:7], s95 offen offset:0 sc1\n"
  "v_mfma_f32_16x16x32_bf16 v[80:83], v[236:239], a[88:91], v[80:83]\n"
  "s_waitcnt vmcnt(11)\n"
  "v_xor_b32 v244, v34, v244\n"
  "v_xor_b32 v246, v35, v246\n"
  "v_mfma_f32_16x16x32_bf16 v[72:75], v[232:235], a[88:91], v[72:75]\n"
  "buffer_load_dwordx4 v[212:215], v173, s[4:7], s95 offen offset:0 sc1\n"
  "v_mfma_f32_16x16x32_bf16 v[60:63], v[232:235], a[84:87], v[60:63]\n"
  "v_or3_b32 v52, v240, v242, v244\n"
  "v_bitop3_b32 v52, v52, s33, v246 bitop3:0xc8\n"
  "v_mfma_f32_16x16x32_bf16 v[68:71], v[236:239], a[84:87], v[68:71]\n"
  "v_cmp_ne_u32 vcc, 0, v52\n"
  "buffer_load_dwordx4 v18, s[12:15], s66 offen offset:2048 lds\n"
  "v_mfma_f32_16x16x32_bf16 v[64:67], v[236:239], a[80:83], v[64:67]\n"
  "v_mfma_f32_16x16x32_bf16 v[56:59], v[232:235], a[80:83], v[56:59]\n"
  "s_cbranch_vccnz Lslow72\n"
  "Lb72:\n"
  "v_mfma_f32_16x16x32_bf16 v[76:79], v[240:243], a[108:111], v[76:79]\n"
  "s_waitcnt vmcnt(11)\n"
  "v_xor_b32 v248, v34, v248\n"
  "v_xor_b32 v250, v35, v250\n"
  "v_mfma_f32_16x16x32_bf16 v[84:87], v[244:247], a[108:111], v[84:87]\n"
  "buffer_load_dwordx4 v[216:219], v172, s[4:7], s95 offen offset:2048 sc1\n"
  "v_mfma_f32_16x16x32_bf16 v[80:83], v[244:247], a[104:107], v[80:83]\n"
  "s_waitcnt vmcnt(11)\n"
  "v_xor_b32 v252, v34, v252\n"
  "v_xor_b32 v254, v35, v254\n"
  "v_mfma_f32_16x16x32_bf16 v[72:75], v[240:243], a[104:107], v[72:75]\n"
  "buffer_load_dwordx4 v[220:223], v173, s[4:7], s95 offen offset:2048 sc1\n"
  "v_mfma_f32_16x16x32_bf16 v[60:63], v[240:243], a[100:103], v[60:63]\n"
  "v_or3_b32 v52, v248, v250, v252\n"
  "v_bitop3_b32 v52, v52, s33, v254 bitop3:0xc8\n"
  "v_mfma_f32_16x16x32_bf16 v[68:71], v[244:247], a[100:103], v[68:71]\n"
  "v_cmp_ne_u32 vcc, 0, v52\n"
  "buffer_load_dwordx4 v19, s[12:15], s66 offen offset:3072 lds\n"
  "v_mfma_f32_16x16x32_bf16 v[64:67], v[244:247], a[96:99], v[64:67]\n"
  "v_mfma_f32_16x16x32_bf16 v[56:59], v[240:243], a[96:99], v[56:59]\n"
  "s_cbranch_vccnz Lslow73\n"
  "Lb73:\n"
  "s_waitcnt lgkmcnt(0)\n"
  "v_mfma_f32_16x16x32_bf16 v[76:79], v[248:251], a[124:127], v[76:79]\n"
  "s_waitcnt vmcnt(11)\n"
  "v_xor_b32 v192, v34, v192\n"
  "v_xor_b32 v194, v35, v194\n"
  "v_mfma_f32_16x16x32_bf16 v[84:87], v[252:255], a[124:127], v[84:87]\n"
  "buffer_load_dwordx4 v[224:227], v172, s[4:7], s30 offen offset:0 sc1\n"
  "v_mfma_f32_16x16x32_bf16 v[80:83], v[252:255], a[120:123], v[80:83]\n"
  "s_waitcnt vmcnt(11)\n"
  "v_xor_b32 v196, v34, v196\n"
  "v_xor_b32 v198, v35, v198\n"
  "v_mfma_f32_16x16x32_bf16 v[72:75], v[248:251], a[120:123], v[72:75]\n"
  "buffer_load_dwordx4 v[228:231], v173, s[4:7], s30 offen offset:0 sc1\n"
  "v_mfma_f32_16x16x32_bf16 v[60:63], v[248:251], a[116:119], v[60:63]\n"
  "v_or3_b32 v52, v192, v194, v196\n"
  "v_bitop3_b32 v52, v52, s33, v198 bitop3:0xc8\n"
  "v_mfma_f32_16x16x32_bf16 v[68:71], v[252:255], a[116:119], v[68:71]\n"
  "v_cmp_ne_u32 vcc, 0, v52\n"
  "s_mov_b32 m0, s27\n"
  "s_add_i32 s66, s66, 0x20000\n"
  "buffer_load_dwordx4 v16, s[12:15], s66 offen offset:0 lds\n"
  "v_mfma_f32_16x16x32_bf16 v[64:67], v[252:255], a[112:115], v[64:67]\n"
  "v_mfma_f32_16x16x32_bf16 v[56:59], v[248:251], a[112:115], v[56:59]\n"
  "s_cbranch_vccnz Lslow74\n"
  "Lb74:\n"
  "v_mfma_f32_16x16x32_bf16 v[76:79], v[192:195], a[140:143], v[76:79]\n"
  "s_waitcnt vmcnt(11)\n"
  "v_xor_b32 v200, v34, v200\n"
  "v_xor_b32 v202, v35, v202\n"
  "v_mfma_f32_16x16x32_bf16 v[84:87], v[196:199], a[140:143], v[84:87]\n"
  "buffer_load_dwordx4 v17, s[12:15], s66 offen offset:1024 lds\n"
  "v_mfma_f32_16x16x32_bf16 v[80:83], v[196:199], a[136:139], v[80:83]\n"
  "s_waitcnt vmcnt(11)\n"
  "v_xor_b32 v204, v34, v204\n"
  "v_xor_b32 v206, v35, v206\n"
  "v_mfma_f32_16x16x32_bf16 v[72:75], v[192:195], a[136:139], v[72:75]\n"
  "buffer_load_dwordx4 v18, s[12:15], s66 offen offset:2048 lds\n"
  "v_mfma_f32_16x16x32_bf16 v[60:63], v[192:195], a[132:135], v[60:63]\n"
  "v_or3_b32 v52, v200, v202, v204\n"
  "v_bitop3_b32 v52, v52, s33, v206 bitop3:0xc8\n"
  "v_mfma_f32_16x16x32_bf16 v[68:71], v[196:199], a[132:135], v[68:71]\n"
  "v_cmp_ne_u32 vcc, 0, v52\n"
  "buffer_load_dwordx4 v19, s[12:15], s66 offen offset:3072 lds\n"
  "v_mfma_f32_16x16x32_bf16 v[64:67], v[196:199], a[128:131], v[64:67]\n"
  "v_mfma_f32_16x16x32_bf16 v[56:59], v[192:195], a[128:131], v[56:59]\n"
  "s_cbranch_vccnz Lslow75\n"
  "Lb75:\n"
  "v_mfma_f32_16x16x32_bf16 v[76:79], v[200:203], a[156:159], v[76:79]\n"
  "s_waitcnt vmcnt(11)\n"
  "v_xor_b32 v208, v34, v208\n"
  "v_xor_b32 v210, v35, v210\n"
  "v_mfma_f32_16x16x32_bf16 v[84:87], v[204:207], a[156:159], v[84:87]\n"
  "buffer_load_dwordx4 v[232:235], v172, s[4:7], s30 offen offset:2048 sc1\n"
  "v_mfma_f32_16x16x32_bf16 v[80:83], v[204:207], a[152:155], v[80:83]\n"
  "s_waitcnt vmcnt(11)\n"
  "v_xor_b32 v212, v34, v212\n"
  "v_xor_b32 v214, v35, v214\n"
  "v_mfma_f32_16x16x32_bf16 v[72:75], v[200:203], a[152:155], v[72:75]\n"
  "buffer_load_dwordx4 v[236:239], v173, s[4:7], s30 offen offset:2048 sc1\n"
  "ds_read_b64_tr_b16 a[176:177], v23 offset:16384\n"
  "ds_read_b64_tr_b16 a[178:179], v23 offset:16896\n"
  "ds_read_b64_tr_b16 a[180:181], v53 offset:16384\n"
  "ds_read_b64_tr_b16 a[182:183], v53 offset:16896\n"
  "v_mfma_f32_16x16x32_bf16 v[60:63], v[200:203], a[148:151], v[60:63]\n"
  "v_or3_b32 v52, v208, v210, v212\n"
  "v_bitop3_b32 v52, v52, s33, v214 bitop3:0xc8\n"
  "v_mfma_f32_16x16x32_bf16 v[68:71], v[204:207], a[148:151], v[68:71]\n"
  "v_cmp_ne_u32_e64 vcc, 0, v52\n"
  "buffer_load_dwordx4 v[240:243], v174, s[4:7], s73 offen offset:0 sc1\n"
  "v_mfma_f32_16x16x32_bf16 v[64:67], v[204:207], a[144:147], v[64:67]\n"
  "ds_read_b64_tr_b16 a[184:185], v54 offset:16384\n"
  "ds_read_b64_tr_b16 a[186:187], v54 offset:16896\n"
  "ds_read_b64_tr_b16 a[188:189], v55 offset:16384\n"
  "ds_read_b64_tr_b16 a[190:191], v55 offset:16896\n"
  "v_mfma_f32_16x16x32_bf16 v[56:59], v[200:203], a[144:147], v[56:59]\n"
  "s_cbranch_vccnz Lslow76\n"
  "Lb76:\n"
  "v_mfma_f32_16x16x32_bf16 v[76:79], v[208:211], a[172:175], v[76:79]\n"
  "s_waitcnt vmcnt(11)\n"
  "v_xor_b32 v216, v34, v216\n"
  "v_xor_b32 v218, v35, v218\n"
  "v_mfma_f32_16x16x32_bf16 v[84:87], v[212:215], a[172:175], v[84:87]\n"
  "buffer_load_dwordx4 v[244:247], v175, s[4:7], s73 offen offset:0 sc1\n"
  "v_mfma_f32_16x16x32_bf16 v[80:83], v[212:215], a[168:171], v[80:83]\n"
  "s_waitcnt vmcnt(11)\n"
  "v_xor_b32 v220, v34, v220\n"
  "v_xor_b32 v222, v35, v222\n"
  "v_mfma_f32_16x16x32_bf16 v[72:75], v[208:211], a[168:171], v[72:75]\n"
  "buffer_load_dwordx4 v[248:251], v174, s[4:7], s73 offen offset:2048 sc1\n"
  "s_waitcnt vmcnt(11)\n"
  "ds_read_b64_tr_b16 a[192:193], v23 offset:32768\n"
  "ds_read_b64_tr_b16 a[194:195], v23 offset:33280\n"
  "ds_read_b64_tr_b16 a[196:197], v53 offset:32768\n"
  "ds_read_b64_tr_b16 a[198:199], v53 offset:33280\n"
  "v_mfma_f32_16x16x32_bf16 v[60:63], v[208:211], a[164:167], v[60:63]\n"
  "v_or3_b32 v52, v216, v218, v220\n"
  "v_bitop3_b32 v52, v52, s33, v222 bitop3:0xc8\n"
  "v_mfma_f32_16x16x32_bf16 v[68:71], v[212:215], a[164:167], v[68:71]\n"
  "v_cmp_ne_u32_e64 vcc, 0, v52\n"
  "buffer_load_dwordx4 v[252:255], v175, s[4:7], s73 offen offset:2048 sc1\n"
  "v_mfma_f32_16x16x32_bf16 v[64:67], v[212:215], a[160:163], v[64:67]\n"
  "ds_read_b64_tr_b16 a[200:201], v54 offset:32768\n"
  "ds_read_b64_tr_b16 a[202:203], v54 offset:33280\n"
  "ds_read_b64_tr_b16 a[204:205], v55 offset:32768\n"
  "ds_read_b64_tr_b16 a[206:207], v55 offset:33280\n"
  "v_mfma_f32_16x16x32_bf16 v[56:59], v[208:211], a[160:163], v[56:59]\n"
  "s_cbranch_vccnz Lslow77\n"
  "Lb77:\n"
  "s_waitcnt lgkmcnt(8)\n"
  "v_mfma_f32_16x16x32_bf16 v[76:79], v[216:219], a[188:191], v[76:79]\n"
  "s_waitcnt vmcnt(11)\n"
  "v_xor_b32 v224, v34, v224\n"
  "v_xor_b32 v226, v35, v226\n"
  "v_mfma_f32_16x16x32_bf16 v[84:87], v[220:223], a[188:191], v[84:87]\n"
  "buffer_load_dwordx4 v[192:195], v174, s[4:7], s0 offen offset:0 sc1\n"
  "v_mfma_f32_16x16x32_bf16 v[80:83], v[220:223], a[184:187], v[80:83]\n"
  "s_waitcnt vmcnt(11)\n"
  "v_xor_b32 v228, v34, v228\n"
  "v_xor_b32_e64 v230, v35, v230\n"
  "v_mfma_f32_16x16x32_bf16 v[72:75], v[216:219], a[184:187], v[72:75]\n"
  "buffer_load_dwordx4 v[196:199], v175, s[4:7], s0 offen offset:0 sc1\n"
  "s_waitcnt vmcnt(8)\n"
  "ds_read_b64_tr_b16 a[208:209], v23 offset:49152\n"
  "ds_read_b64_tr_b16 a[210:211], v23 offset:49664\n"
  "ds_read_b64_tr_b16 a[212:213], v53 offset:49152\n"
  "ds_read_b64_tr_b16 a[214:215], v53 offset:49664\n"
  "v_mfma_f32_16x16x32_bf16 v[60:63], v[216:219], a[180:183], v[60:63]\n"
  "v_or3_b32 v52, v224, v226, v228\n"
  "v_bitop3_b32 v52, v52, s33, v230 bitop3:0xc8\n"
  "v_mfma_f32_16x16x32_bf16 v[68:71], v[220:223], a[180:183], v[68:71]\n"
  "v_cmp_ne_u32_e64 vcc, 0, v52\n"
  "buffer_load_dwordx4 v[200:203], v174, s[4:7], s0 offen offset:2048 sc1\n"
  "v_mfma_f32_16x16x32_bf16 v[64:67], v[220:223], a[176:179], v[64:67]\n"
  "ds_read_b64_tr_b16 a[216:217], v54 offset:49152\n"
  "ds_read_b64_tr_b16 a[218:219], v54 offset:49664\n"
  "ds_read_b64_tr_b16 a[220:221], v55 offset:49152\n"
  "ds_read_b64_tr_b16 a[222:223], v55 offset:49664\n"
  "v_mfma_f32_16x16x32_bf16 v[56:59], v[216:219], a[176:179], v[56:59]\n"
  "buffer_load_dwordx4 v[204:207], v175, s[4:7], s0 offen offset:2048 sc1\n"
  "buffer_load_dwordx4 v[208:211], v174, s[4:7], s1 offen offset:0 sc1\n"
  "s_cbranch_vccnz Lslow78\n"
  "Lb78:\n"
  "s_waitcnt lgkmcnt(8)\n"
  "v_mfma_f32_16x16x32_bf16 v[76:79], v[224:227], a[204:207], v[76:79]\n"
  "s_waitcnt vmcnt(10)\n"
  "v_xor_b32 v232, v34, v232\n"
  "v_xor_b32 v234, v35, v234\n"
  "v_mfma_f32_16x16x32_bf16 v[84:87], v[228:231], a[204:207], v[84:87]\n"
  "buffer_load_dwordx4 v[212:215], v175, s[4:7], s1 offen offset:0 sc1\n"
  "v_mfma_f32_16x16x32_bf16 v[80:83], v[228:231], a[200:203], v[80:83]\n"
  "s_waitcnt vmcnt(10)\n"
  "v_xor_b32 v236, v34, v236\n"
  "v_xor_b32 v238, v35, v238\n"
  "v_mfma_f32_16x16x32_bf16 v[72:75], v[224:227], a[200:203], v[72:75]\n"
  "buffer_load_dwordx4 v[216:219], v174, s[4:7], s1 offen offset:2048 sc1\n"
  "v_mfma_f32_16x16x32_bf16 v[60:63], v[224:227], a[196:199], v[60:63]\n"
  "v_or3_b32 v52, v232, v234, v236\n"
  "v_bitop3_b32 v52, v52, s33, v238 bitop3:0xc8\n"
  "v_mfma_f32_16x16x32_bf16 v[68:71], v[228:231], a[196:199], v[68:71]\n"
  "v_cmp_ne_u32 vcc, 0, v52\n"
  "buffer_load_dwordx4 v[220:223], v175, s[4:7], s1 offen offset:2048 sc1\n"
  "v_mfma_f32_16x16x32_bf16 v[64:67], v[228:231], a[192:195], v[64:67]\n"
  "v_mfma_f32_16x16x32_bf16 v[56:59], v[224:227], a[192:195], v[56:59]\n"
  "s_cbranch_vccnz Lslow79\n"
  "Lb79:\n"
  "s_waitcnt lgkmcnt(0)\n"
  "v_mfma_f32_16x16x32_bf16 v[76:79], v[232:235], a[220:223], v[76:79]\n"
  "s_waitcnt vmcnt(11)\n"
  "v_xor_b32 v240, v34, v240\n"
  "v_xor_b32 v242, v35, v242\n"
  "v_mfma_f32_16x16x32_bf16 v[84:87], v[236:239], a[220:223], v[84:87]\n"
  "buffer_load_dwordx4 v[224:227], v175, s[4:7], s3 offen offset:0 sc1\n"
  "v_mfma_f32_16x16x32_bf16 v[80:83], v[236:239], a[216:219], v[80:83]\n"
  "s_waitcnt vmcnt(11)\n"
  "v_xor_b32 v244, v34, v244\n"
  "v_xor_b32 v246, v35, v246\n"
  "v_mfma_f32_16x16x32_bf16 v[72:75], v[232:235], a[216:219], v[72:75]\n"
  "buffer_load_dwordx4 v[228:231], v175, s[4:7], s3 offen offset:2048 sc1\n"
  "v_mfma_f32_16x16x32_bf16 v[60:63], v[232:235], a[212:215], v[60:63]\n"
  "v_or3_b32 v52, v240, v242, v244\n"
  "v_bitop3_b32 v52, v52, s33, v246 bitop3:0xc8\n"
  "v_mfma_f32_16x16x32_bf16 v[68:71], v[236:239], a[212:215], v[68:71]\n"
  "v_cmp_ne_u32 vcc, 0, v52\n"
  "v_mfma_f32_16x16x32_bf16 v[64:67], v[236:239], a[208:211], v[64:67]\n"
  "v_mfma_f32_16x16x32_bf16 v[56:59], v[232:235], a[208:211], v[56:59]\n"
  "s_cbranch_vccnz Lslow80\n"
  "Lb80:\n"
  "s_nop 11\n"
  "s_branch Ljoin\n"
  "Lend:\n"
  "Lslow0:\n"
  "s_add_u32 s76, s76, 1\n"
  "s_cmp_gt_u32 s76, 0x200000\n"
  "s_cbranch_scc1 LbackUa\n"
  "buffer_load_dwordx4 v[248:251], v20, s[4:7], 0 offen sc1\n"
  "buffer_load_dwordx4 v[252:255], v20, s[4:7], s77 offen sc1\n"
  "s_waitcnt vmcnt(0)\n"
  "v_bitop3_b32 v184, v248, v252, v4 bitop3:0x7e\n"
  "v_bitop3_b32 v185, v249, v253, v5 bitop3:0x7e\n"
  "v_bitop3_b32 v186, v250, v254, v6 bitop3:0x7e\n"
  "v_bitop3_b32 v187, v251, v255, v7 bitop3:0x7e\n"
  "v_or3_b32 v184, v184, v185, v186\n"
  "v_bitop3_b32 v184, v184, s84, v187 bitop3:0xc8\n"
  "v_cmp_ne_u32 vcc, 0, v184\n"
  "s_cbranch_vccnz Lslow0\n"
  "s_branch LbackUa\n"
  "Lslow1:\n"
  "s_add_u32 s76, s76, 1\n"
  "s_cmp_gt_u32 s76, 0x200000\n"
  "s_cbranch_scc1 LbackYa\n"
  "buffer_load_dwordx4 v[64:67], v52, s[4:7], 0 offen sc1\n"
  "buffer_load_dwordx4 v[68:71], v52, s[4:7], 0 offen offset:512 sc1\n"
  "s_waitcnt vmcnt(0)\n"
  "v_xor_b32 v68, v4, v68\n"
  "v_xor_b32 v69, v5, v69\n"
  "v_xor_b32 v70, v6, v70\n"
  "v_xor_b32 v71, v7, v71\n"
  "v_xor_b32 v64, v4, v64\n"
  "v_xor_b32 v65, v5, v65\n"
  "v_xor_b32 v66, v6, v66\n"
  "v_xor_b32 v67, v7, v67\n"
  "v_or3_b32 v58, v68, v69, v70\n"
  "v_or3_b32 v58, v58, v71, v64\n"
  "v_or3_b32 v58, v58, v65, v66\n"
  "v_bitop3_b32 v58, v58, s33, v67 bitop3:0xc8\n"
  "v_cmp_ne_u32 vcc, 0, v58\n"
  "s_cbranch_vccnz Lslow1\n"
  "s_branch LbackYa\n"
  "Lslow2:\n"
  "s_add_u32 s76, s76, 1\n"
  "s_cmp_gt_u32 s76, 0x200000\n"
  "s_cbranch_scc1 Lb2\n"
  "buffer_load_dwordx4 v[192:195], v168, s[4:7], s72 offen offset:0 sc1\n"
  "buffer_load_dwordx4 v[196:199], v169, s[4:7], s72 offen offset:0 sc1\n"
  "s_waitcnt vmcnt(0)\n"
  "v_xor_b32 v192, v34, v192\n"
  "v_xor_b32 v194, v35, v194\n"
  "v_xor_b32 v196, v34, v196\n"
  "v_xor_b32 v198, v35, v198\n"
  "v_or3_b32 v52, v192, v194, v196\n"
  "v_bitop3_b32 v52, v52, s33, v198 bitop3:0xc8\n"
  "v_cmp_ne_u32 vcc, 0, v52\n"
  "s_cbranch_vccnz Lslow2\n"
  "buffer_load_dwordx4 v[200:203], v168, s[4:7], s72 offen offset:2048 sc1\n"
  "buffer_load_dwordx4 v[204:207], v169, s[4:7], s72 offen offset:2048 sc1\n"
  "buffer_load_dwordx4 v[208:211], v168, s[4:7], s58 offen offset:0 sc1\n"
  "buffer_load_dwordx4 v[212:215], v169, s[4:7], s58 offen offset:0 sc1\n"
  "buffer_load_dwordx4 v[216:219], v168, s[4:7], s58 offen offset:2048 sc1\n"
  "buffer_load_dwordx4 v[220:223], v169, s[4:7], s58 offen offset:2048 sc1\n"
  "buffer_load_dwordx4 v[224:227], v168, s[4:7], s67 offen offset:0 sc1\n"
  "buffer_load_dwordx4 v[228:231], v169, s[4:7], s67 offen offset:0 sc1\n"
  "buffer_load_dwordx4 v[232:235], v168, s[4:7], s67 offen offset:2048 sc1\n"
  "buffer_load_dwordx4 v[236:239], v169, s[4:7], s67 offen offset:2048 sc1\n"
  "s_waitcnt vmcnt(0)\n"
  "s_branch Lb2\n"
  "Lslow3:\n"
  "s_add_u32 s76, s76, 1\n"
  "s_cmp_gt_u32 s76, 0x200000\n"
  "s_cbranch_scc1 Lb3\n"
  "buffer_load_dwordx4 v[200:203], v168, s[4:7], s72 offen offset:2048 sc1\n"
  "buffer_load_dwordx4 v[204:207], v169, s[4:7], s72 offen offset:2048 sc1\n"
  "s_waitcnt vmcnt(0)\n"
  "v_xor_b32 v200, v34, v200\n"
  "v_xor_b32 v202, v35, v202\n"
  "v_xor_b32 v204, v34, v204\n"
  "v_xor_b32 v206, v35, v206\n"
  "v_or3_b32 v52, v200, v202, v204\n"
  "v_bitop3_b32 v52, v52, s33, v206 bitop3:0xc8\n"
  "v_cmp_ne_u32_e64 vcc, 0, v52\n"
  "s_cbranch_vccnz Lslow3\n"
  "buffer_load_dwordx4 v[208:211], v168, s[4:7], s58 offen offset:0 sc1\n"
  "buffer_load_dwordx4 v[212:215], v169, s[4:7], s58 offen offset:0 sc1\n"
  "buffer_load_dwordx4 v[216:219], v168, s[4:7], s58 offen offset:2048 sc1\n"
  "buffer_load_dwordx4 v[220:223], v169, s[4:7], s58 offen offset:2048 sc1\n"
  "buffer_load_dwordx4 v[224:227], v168, s[4:7], s67 offen offset:0 sc1\n"
  "buffer_load_dwordx4 v[228:231], v169, s[4:7], s67 offen offset:0 sc1\n"
  "buffer_load_dwordx4 v[232:235], v168, s[4:7], s67 offen offset:2048 sc1\n"
  "buffer_load_dwordx4 v[236:239], v169, s[4:7], s67 offen offset:2048 sc1\n"
  "buffer_load_dwordx4 v[240:243], v168, s[4:7], s68 offen offset:0 sc1\n"
  "buffer_load_dwordx4 v[244:247], v169, s[4:7], s68 offen offset:0 sc1\n"
  "buffer_load_dwordx4 v[248:251], v168, s[4:7], s68 offen offset:2048 sc1\n"
  "buffer_load_dwordx4 v[252:255], v169, s[4:7], s68 offen offset:2048 sc1\n"
  "buffer_load_dwordx4 v[192:195], v168, s[4:7], s78 offen offset:0 sc1\n"
  "s_waitcnt vmcnt(0)\n"
  "s_branch Lb3\n"
  "Lslow4:\n"
  "s_add_u32 s76, s76, 1\n"
  "s_cmp_gt_u32 s76, 0x200000\n"
  "s_cbranch_scc1 Lb4\n"
  "buffer_load_dwordx4 v[208:211], v168, s[4:7], s58 offen offset:0 sc1\n"
  "buffer_load_dwordx4 v[212:215], v169, s[4:7], s58 offen offset:0 sc1\n"
  "s_waitcnt vmcnt(0)\n"
  "v_xor_b32 v208, v34, v208\n"
  "v_xor_b32 v210, v35, v210\n"
  "v_xor_b32 v212, v34, v212\n"
  "v_xor_b32 v214, v35, v214\n"
  "v_or3_b32 v52, v208, v210, v212\n"
  "v_bitop3_b32 v52, v52, s33, v214 bitop3:0xc8\n"
  "v_cmp_ne_u32_e64 vcc, 0, v52\n"
  "s_cbranch_vccnz Lslow4\n"
  "buffer_load_dwordx4 v[216:219], v168, s[4:7], s58 offen offset:2048 sc1\n"
  "buffer_load_dwordx4 v[220:223], v169, s[4:7], s58 offen offset:2048 sc1\n"
  "buffer_load_dwordx4 v[224:227], v168, s[4:7], s67 offen offset:0 sc1\n"
  "buffer_load_dwordx4 v[228:231], v169, s[4:7], s67 offen offset:0 sc1\n"
  "buffer_load_dwordx4 v[232:235], v168, s[4:7], s67 offen offset:2048 sc1\n"
  "buffer_load_dwordx4 v[236:239], v169, s[4:7], s67 offen offset:2048 sc1\n"
  "buffer_load_dwordx4 v[240:243], v168, s[4:7], s68 offen offset:0 sc1\n"
  "buffer_load_dwordx4 v[244:247], v169, s[4:7], s68 offen offset:0 sc1\n"
  "buffer_load_dwordx4 v[248:251], v168, s[4:7], s68 offen offset:2048 sc1\n"
  "buffer_load_dwordx4 v[252:255], v169, s[4:7], s68 offen offset:2048 sc1\n"
  "buffer_load_dwordx4 v[192:195], v168, s[4:7], s78 offen offset:0 sc1\n"
  "buffer_load_dwordx4 v[196:199], v169, s[4:7], s78 offen offset:0 sc1\n"
  "buffer_load_dwordx4 v[200:203], v168, s[4:7], s78 offen offset:2048 sc1\n"
  "s_waitcnt vmcnt(0)\n"
  "s_branch Lb4\n"
  "Lslow5:\n"
  "s_add_u32 s76, s76, 1\n"
  "s_cmp_gt_u32 s76, 0x200000\n"
  "s_cbranch_scc1 Lb5\n"
  "buffer_load_dwordx4 v[216:219], v168, s[4:7], s58 offen offset:2048 sc1\n"
  "buffer_load_dwordx4 v[220:223], v169, s[4:7], s58 offen offset:2048 sc1\n"
  "s_waitcnt vmcnt(0)\n"
  "v_xor_b32 v216, v34, v216\n"
  "v_xor_b32 v218, v35, v218\n"
  "v_xor_b32 v220, v34, v220\n"
  "v_xor_b32 v222, v35, v222\n"
  "v_or3_b32 v52, v216, v218, v220\n"
  "v_bitop3_b32 v52, v52, s33, v222 bitop3:0xc8\n"
  "v_cmp_ne_u32_e64 vcc, 0, v52\n"
  "s_cbranch_vccnz Lslow5\n"
  "buffer_load_dwordx4 v[224:227], v168, s[4:7], s67 offen offset:0 sc1\n"
  "buffer_load_dwordx4 v[228:231], v169, s[4:7], s67 offen offset:0 sc1\n"
  "buffer_load_dwordx4 v[232:235], v168, s[4:7], s67 offen offset:2048 sc1\n"
  "buffer_load_dwordx4 v[236:239], v169, s[4:7], s67 offen offset:2048 sc1\n"
  "buffer_load_dwordx4 v[240:243], v168, s[4:7], s68 offen offset:0 sc1\n"
  "buffer_load_dwordx4 v[244:247], v169, s[4:7], s68 offen offset:0 sc1\n"
  "buffer_load_dwordx4 v[248:251], v168, s[4:7], s68 offen offset:2048 sc1\n"
  "buffer_load_dwordx4 v[252:255], v169, s[4:7], s68 offen offset:2048 sc1\n"
  "buffer_load_dwordx4 v[192:195], v168, s[4:7], s78 offen offset:0 sc1\n"
  "buffer_load_dwordx4 v[196:199], v169, s[4:7], s78 offen offset:0 sc1\n"
  "buffer_load_dwordx4 v[200:203], v168, s[4:7], s78 offen offset:2048 sc1\n"
  "buffer_load_dwordx4 v[204:207], v169, s[4:7], s78 offen offset:2048 sc1\n"
  "s_waitcnt vmcnt(0)\n"
  "s_branch Lb5\n"
  "Lslow6:\n"
  "s_add_u32 s76, s76, 1\n"
  "s_cmp_gt_u32 s76, 0x200000\n"
  "s_cbranch_scc1 Lb6\n"
  "buffer_load_dwordx4 v[224:227], v168, s[4:7], s67 offen offset:0 sc1\n"
  "buffer_load_dwordx4 v[228:231], v169, s[4:7], s67 offen offset:0 sc1\n"
  "s_waitcnt vmcnt(0)\n"
  "v_xor_b32 v224, v34, v224\n"
  "v_xor_b32 v226, v35, v226\n"
  "v_xor_b32 v228, v34, v228\n"
  "v_xor_b32 v230, v35, v230\n"
  "v_or3_b32 v52, v224, v226, v228\n"
  "v_bitop3_b32 v52, v52, s33, v230 bitop3:0xc8\n"
  "v_cmp_ne_u32_e64 vcc, 0, v52\n"
  "s_cbranch_vccnz Lslow6\n"
  "buffer_load_dwordx4 v[232:235], v168, s[4:7], s67 offen offset:2048 sc1\n"
  "buffer_load_dwordx4 v[236:239], v169, s[4:7], s67 offen offset:2048 sc1\n"
  "buffer_load_dwordx4 v[240:243], v168, s[4:7], s68 offen offset:0 sc1\n"
  "buffer_load_dwordx4 v[244:247], v169, s[4:7], s68 offen offset:0 sc1\n"
  "buffer_load_dwordx4 v[248:251], v168, s[4:7], s68 offen offset:2048 sc1\n"
  "buffer_load_dwordx4 v[252:255], v169, s[4:7], s68 offen offset:2048 sc1\n"
  "buffer_load_dwordx4 v[192:195], v168, s[4:7], s78 offen offset:0 sc1\n"
  "buffer_load_dwordx4 v[196:199], v169, s[4:7], s78 offen offset:0 sc1\n"
  "buffer_load_dwordx4 v[200:203], v168, s[4:7], s78 offen offset:2048 sc1\n"
  "buffer_load_dwordx4 v[204:207], v169, s[4:7], s78 offen offset:2048 sc1\n"
  "buffer_load_dwordx4 v[208:211], v168, s[4:7], s79 offen offset:0 sc1\n"
  "buffer_load_dwordx4 v[212:215], v169, s[4:7], s79 offen offset:0 sc1\n"
  "s_waitcnt vmcnt(0)\n"
  "s_branch Lb6\n"
  "Lslow7:\n"
  "s_add_u32 s76, s76, 1\n"
  "s_cmp_gt_u32 s76, 0x200000\n"
  "s_cbranch_scc1 Lb7\n"
  "buffer_load_dwordx4 v[232:235], v168, s[4:7], s67 offen offset:2048 sc1\n"
  "buffer_load_dwordx4 v[236:239], v169, s[4:7], s67 offen offset:2048 sc1\n"
  "s_waitcnt vmcnt(0)\n"
  "v_xor_b32 v232, v34, v232\n"
  "v_xor_b32 v234, v35, v234\n"
  "v_xor_b32 v236, v34, v236\n"
  "v_xor_b32 v238, v35, v238\n"
  "v_or3_b32 v52, v232, v234, v236\n"
  "v_bitop3_b32 v52, v52, s33, v238 bitop3:0xc8\n"
  "v_cmp_ne_u32_e64 vcc, 0, v52\n"
  "s_cbranch_vccnz Lslow7\n"
  "buffer_load_dwordx4 v[240:243], v168, s[4:7], s68 offen offset:0 sc1\n"
  "buffer_load_dwordx4 v[244:247], v169, s[4:7], s68 offen offset:0 sc1\n"
  "buffer_load_dwordx4 v[248:251], v168, s[4:7], s68 offen offset:2048 sc1\n"
  "buffer_load_dwordx4 v[252:255], v169, s[4:7], s68 offen offset:2048 sc1\n"
  "buffer_load_dwordx4 v[192:195], v168, s[4:7], s78 offen offset:0 sc1\n"
  "buffer_load_dwordx4 v[196:199], v169, s[4:7], s78 offen offset:0 sc1\n"
  "buffer_load_dwordx4 v[200:203], v168, s[4:7], s78 offen offset:2048 sc1\n"
  "buffer_load_dwordx4 v[204:207], v169, s[4:7], s78 offen offset:2048 sc1\n"
  "buffer_load_dwordx4 v[208:211], v168, s[4:7], s79 offen offset:0 sc1\n"
  "buffer_load_dwordx4 v[212:215], v169, s[4:7], s79 offen offset:0 sc1\n"
  "buffer_load_dwordx4 v[216:219], v168, s[4:7], s79 offen offset:2048 sc1\n"
  "buffer_load_dwordx4 v[220:223], v169, s[4:7], s79 offen offset:2048 sc1\n"
  "s_waitcnt vmcnt(0)\n"
  "s_branch Lb7\n"
  "Lslow8:\n"
  "s_add_u32 s76, s76, 1\n"
  "s_cmp_gt_u32 s76, 0x200000\n"
  "s_cbranch_scc1 Lb8\n"
  "buffer_load_dwordx4 v[240:243], v168, s[4:7], s68 offen offset:0 sc1\n"
  "buffer_load_dwordx4 v[244:247], v169, s[4:7], s68 offen offset:0 sc1\n"
  "s_waitcnt vmcnt(0)\n"
  "v_xor_b32 v240, v34, v240\n"
  "v_xor_b32 v242, v35, v242\n"
  "v_xor_b32 v244, v34, v244\n"
  "v_xor_b32 v246, v35, v246\n"
  "v_or3_b32 v52, v240, v242, v244\n"
  "v_bitop3_b32 v52, v52, s33, v246 bitop3:0xc8\n"
  "v_cmp_ne_u32_e64 vcc, 0, v52\n"
  "s_cbranch_vccnz Lslow8\n"
  "buffer_load_dwordx4 v[248:251], v168, s[4:7], s68 offen offset:2048 sc1\n"
  "buffer_load_dwordx4 v[252:255], v169, s[4:7], s68 offen offset:2048 sc1\n"
  "buffer_load_dwordx4 v[192:195], v168, s[4:7], s78 offen offset:0 sc1\n"
  "buffer_load_dwordx4 v[196:199], v169, s[4:7], s78 offen offset:0 sc1\n"
  "buffer_load_dwordx4 v[200:203], v168, s[4:7], s78 offen offset:2048 sc1\n"
  "buffer_load_dwordx4 v[204:207], v169, s[4:7], s78 offen offset:2048 sc1\n"
  "buffer_load_dwordx4 v[208:211], v168, s[4:7], s79 offen offset:0 sc1\n"
  "buffer_load_dwordx4 v[212:215], v169, s[4:7], s79 offen offset:0 sc1\n"
  "buffer_load_dwordx4 v[216:219], v168, s[4:7], s79 offen offset:2048 sc1\n"
  "buffer_load_dwordx4 v[220:223], v169, s[4:7], s79 offen offset:2048 sc1\n"
  "buffer_load_dwordx4 v[224:227], v168, s[4:7], s69 offen offset:0 sc1\n"
  "s_waitcnt vmcnt(0)\n"
  "s_branch Lb8\n"
  "Lslow9:\n"
  "s_add_u32 s76, s76, 1\n"
  "s_cmp_gt_u32 s76, 0x200000\n"
  "s_cbranch_scc1 Lb9\n"
  "buffer_load_dwordx4 v[248:251], v168, s[4:7], s68 offen offset:2048 sc1\n"
  "buffer_load_dwordx4 v[252:255], v169, s[4:7], s68 offen offset:2048 sc1\n"
  "s_waitcnt vmcnt(0)\n"
  "v_xor_b32 v248, v34, v248\n"
  "v_xor_b32 v250, v35, v250\n"
  "v_xor_b32 v252, v34, v252\n"
  "v_xor_b32 v254, v35, v254\n"
  "v_or3_b32 v52, v248, v250, v252\n"
  "v_bitop3_b32 v52, v52, s33, v254 bitop3:0xc8\n"
  "v_cmp_ne_u32_e64 vcc, 0, v52\n"
  "s_cbranch_vccnz Lslow9\n"
  "buffer_load_dwordx4 v[192:195], v168, s[4:7], s78 offen offset:0 sc1\n"
  "buffer_load_dwordx4 v[196:199], v169, s[4:7], s78 offen offset:0 sc1\n"
  "buffer_load_dwordx4 v[200:203], v168, s[4:7], s78 offen offset:2048 sc1\n"
  "buffer_load_dwordx4 v[204:207], v169, s[4:7], s78 offen offset:2048 sc1\n"
  "buffer_load_dwordx4 v[208:211], v168, s[4:7], s79 offen offset:0 sc1\n"
  "buffer_load_dwordx4 v[212:215], v169, s[4:7], s79 offen offset:0 sc1\n"
  "buffer_load_dwordx4 v[216:219], v168, s[4:7], s79 offen offset:2048 sc1\n"
  "buffer_load_dwordx4 v[220:223], v169, s[4:7], s79 offen offset:2048 sc1\n"
  "buffer_load_dwordx4 v[224:227], v168, s[4:7], s69 offen offset:0 sc1\n"
  "buffer_load_dwordx4 v[228:231], v169, s[4:7], s69 offen offset:0 sc1\n"
  "buffer_load_dwordx4 v[232:235], v168, s[4:7], s69 offen offset:2048 sc1\n"
  "s_waitcnt vmcnt(0)\n"
  "s_branch Lb9\n"
  "Lslow10:\n"
  "s_add_u32 s76, s76, 1\n"
  "s_cmp_gt_u32 s76, 0x200000\n"
  "s_cbranch_scc1 Lb10\n"
  "buffer_load_dwordx4 v[192:195], v168, s[4:7], s78 offen offset:0 sc1\n"
  "buffer_load_dwordx4 v[196:199], v169, s[4:7], s78 offen offset:0 sc1\n"
  "s_waitcnt vmcnt(0)\n"
  "v_xor_b32 v192, v34, v192\n"
  "v_xor_b32 v194, v35, v194\n"
  "v_xor_b32 v196, v34, v196\n"
  "v_xor_b32 v198, v35, v198\n"
  "v_or3_b32 v52, v192, v194, v196\n"
  "v_bitop3_b32 v52, v52, s33, v198 bitop3:0xc8\n"
  "v_cmp_ne_u32_e64 vcc, 0, v52\n"
  "s_cbranch_vccnz Lslow10\n"
  "buffer_load_dwordx4 v[200:203], v168, s[4:7], s78 offen offset:2048 sc1\n"
  "buffer_load_dwordx4 v[204:207], v169, s[4:7], s78 offen offset:2048 sc1\n"
  "buffer_load_dwordx4 v[208:211], v168, s[4:7], s79 offen offset:0 sc1\n"
  "buffer_load_dwordx4 v[212:215], v169, s[4:7], s79 offen offset:0 sc1\n"
  "buffer_load_dwordx4 v[216:219], v168, s[4:7], s79 offen offset:2048 sc1\n"
  "buffer_load_dwordx4 v[220:223], v169, s[4:7], s79 offen offset:2048 sc1\n"
  "buffer_load_dwordx4 v[224:227], v168, s[4:7], s69 offen offset:0 sc1\n"
  "buffer_load_dwordx4 v[228:231], v169, s[4:7], s69 offen offset:0 sc1\n"
  "buffer_load_dwordx4 v[232:235], v168, s[4:7], s69 offen offset:2048 sc1\n"
  "buffer_load_dwordx4 v[236:239], v169, s[4:7], s69 offen offset:2048 sc1\n"
  "buffer_load_dwordx4 v[240:243], v168, s[4:7], s77 offen offset:0 sc1\n"
  "buffer_load_dwordx4 v[244:247], v169, s[4:7], s77 offen offset:0 sc1\n"
  "s_waitcnt vmcnt(0)\n"
  "s_branch Lb10\n"
  "Lslow11:\n"
  "s_add_u32 s76, s76, 1\n"
  "s_cmp_gt_u32 s76, 0x200000\n"
  "s_cbranch_scc1 Lb11\n"
  "buffer_load_dwordx4 v[200:203], v168, s[4:7], s78 offen offset:2048 sc1\n"
  "buffer_load_dwordx4 v[204:207], v169, s[4:7], s78 offen offset:2048 sc1\n"
  "s_waitcnt vmcnt(0)\n"
  "v_xor_b32 v200, v34, v200\n"
  "v_xor_b32 v202, v35, v202\n"
  "v_xor_b32 v204, v34, v204\n"
  "v_xor_b32 v206, v35, v206\n"
  "v_or3_b32 v52, v200, v202, v204\n"
  "v_bitop3_b32 v52, v52, s33, v206 bitop3:0xc8\n"
  "v_cmp_ne_u32 vcc, 0, v52\n"
  "s_cbranch_vccnz Lslow11\n"
  "s_branch Lb11\n"
  "Lslow12:\n"
  "s_add_u32 s76, s76, 1\n"
  "s_cmp_gt_u32 s76, 0x200000\n"
  "s_cbranch_scc1 Lb12\n"
  "buffer_load_dwordx4 v[208:211], v168, s[4:7], s79 offen offset:0 sc1\n"
  "buffer_load_dwordx4 v[212:215], v169, s[4:7], s79 offen offset:0 sc1\n"
  "s_waitcnt vmcnt(0)\n"
  "v_xor_b32 v208, v34, v208\n"
  "v_xor_b32 v210, v35, v210\n"
  "v_xor_b32 v212, v34, v212\n"
  "v_xor_b32 v214, v35, v214\n"
  "v_or3_b32 v52, v208, v210, v212\n"
  "v_bitop3_b32 v52, v52, s33, v214 bitop3:0xc8\n"
  "v_cmp_ne_u32 vcc, 0, v52\n"
  "s_cbranch_vccnz Lslow12\n"
  "s_branch Lb12\n"
  "Lslow13:\n"
  "s_add_u32 s76, s76, 1\n"
  "s_cmp_gt_u32 s76, 0x200000\n"
  "s_cbranch_scc1 Lb13\n"
  "buffer_load_dwordx4 v[216:219], v168, s[4:7], s79 offen offset:2048 sc1\n"
  "buffer_load_dwordx4 v[220:223], v169, s[4:7], s79 offen offset:2048 sc1\n"
  "s_waitcnt vmcnt(0)\n"
  "v_xor_b32 v216, v34, v216\n"
  "v_xor_b32 v218, v35, v218\n"
  "v_xor_b32 v220, v34, v220\n"
  "v_xor_b32 v222, v35, v222\n"
  "v_or3_b32 v52, v216, v218, v220\n"
  "v_bitop3_b32 v52, v52, s33, v222 bitop3:0xc8\n"
  "v_cmp_ne_u32 vcc, 0, v52\n"
  "s_cbranch_vccnz Lslow13\n"
  "s_branch Lb13\n"
  "Lslow14:\n"
  "s_add_u32 s76, s76, 1\n"
  "s_cmp_gt_u32 s76, 0x200000\n"
  "s_cbranch_scc1 Lb14\n"
  "buffer_load_dwordx4 v[224:227], v168, s[4:7], s69 offen offset:0 sc1\n"
  "buffer_load_dwordx4 v[228:231], v169, s[4:7], s69 offen offset:0 sc1\n"
  "s_waitcnt vmcnt(0)\n"
  "v_xor_b32 v224, v34, v224\n"
  "v_xor_b32 v226, v35, v226\n"
  "v_xor_b32 v228, v34, v228\n"
  "v_xor_b32 v230, v35, v230\n"
  "v_or3_b32 v52, v224, v226, v228\n"
  "v_bitop3_b32 v52, v52, s33, v230 bitop3:0xc8\n"
  "v_cmp_ne_u32 vcc, 0, v52\n"
  "s_cbranch_vccnz Lslow14\n"
  "s_branch Lb14\n"
  "Lslow15:\n"
  "s_add_u32 s76, s76, 1\n"
  "s_cmp_gt_u32 s76, 0x200000\n"
  "s_cbranch_scc1 Lb15\n"
  "buffer_load_dwordx4 v[232:235], v168, s[4:7], s69 offen offset:2048 sc1\n"
  "buffer_load_dwordx4 v[236:239], v169, s[4:7], s69 offen offset:2048 sc1\n"
  "s_waitcnt vmcnt(0)\n"
  "v_xor_b32 v232, v34, v232\n"
  "v_xor_b32 v234, v35, v234\n"
  "v_xor_b32 v236, v34, v236\n"
  "v_xor_b32 v238, v35, v238\n"
  "v_or3_b32 v52, v232, v234, v236\n"
  "v_bitop3_b32 v52, v52, s33, v238 bitop3:0xc8\n"
  "v_cmp_ne_u32 vcc, 0, v52\n"
  "s_cbranch_vccnz Lslow15\n"
  "s_branch Lb15\n"
  "Lslow16:\n"
  "s_add_u32 s76, s76, 1\n"
  "s_cmp_gt_u32 s76, 0x200000\n"
  "s_cbranch_scc1 Lb16\n"
  "buffer_load_dwordx4 v[240:243], v168, s[4:7], s77 offen offset:0 sc1\n"
  "buffer_load_dwordx4 v[244:247], v169, s[4:7], s77 offen offset:0 sc1\n"
  "s_waitcnt vmcnt(0)\n"
  "v_xor_b32 v240, v34, v240\n"
  "v_xor_b32 v242, v35, v242\n"
  "v_xor_b32 v244, v34, v244\n"
  "v_xor_b32 v246, v35, v246\n"
  "v_or3_b32 v52, v240, v242, v244\n"
  "v_bitop3_b32 v52, v52, s33, v246 bitop3:0xc8\n"
  "v_cmp_ne_u32 vcc, 0, v52\n"
  "s_cbranch_vccnz Lslow16\n"
  "s_branch Lb16\n"
  "Lslow17:\n"
  "s_add_u32 s76, s76, 1\n"
  "s_cmp_gt_u32 s76, 0x200000\n"
  "s_cbranch_scc1 Lb17\n"
  "buffer_load_dwordx4 v[248:251], v168, s[4:7], s77 offen offset:2048 sc1\n"
  "buffer_load_dwordx4 v[252:255], v169, s[4:7], s77 offen offset:2048 sc1\n"
  "s_waitcnt vmcnt(0)\n"
  "v_xor_b32 v248, v34, v248\n"
  "v_xor_b32 v250, v35, v250\n"
  "v_xor_b32 v252, v34, v252\n"
  "v_xor_b32 v254, v35, v254\n"
  "v_or3_b32 v52, v248, v250, v252\n"
  "v_bitop3_b32 v52, v52, s33, v254 bitop3:0xc8\n"
  "v_cmp_ne_u32 vcc, 0, v52\n"
  "s_cbranch_vccnz Lslow17\n"
  "s_branch Lb17\n"
  "Lslow18:\n"
  "s_add_u32 s76, s76, 1\n"
  "s_cmp_gt_u32 s76, 0x200000\n"
  "s_cbranch_scc1 Lb18\n"
  "buffer_load_dwordx4 v[192:195], v170, s[4:7], s72 offen offset:0 sc1\n"
  "buffer_load_dwordx4 v[196:199], v171, s[4:7], s72 offen offset:0 sc1\n"
  "s_waitcnt vmcnt(0)\n"
  "v_xor_b32 v192, v34, v192\n"
  "v_xor_b32 v194, v35, v194\n"
  "v_xor_b32 v196, v34, v196\n"
  "v_xor_b32 v198, v35, v198\n"
  "v_or3_b32 v52, v192, v194, v196\n"
  "v_bitop3_b32 v52, v52, s33, v198 bitop3:0xc8\n"
  "v_cmp_ne_u32_e64 vcc, 0, v52\n"
  "s_cbranch_vccnz Lslow18\n"
  "buffer_load_dwordx4 v[200:203], v170, s[4:7], s72 offen offset:2048 sc1\n"
  "buffer_load_dwordx4 v[204:207], v171, s[4:7], s72 offen offset:2048 sc1\n"
  "buffer_load_dwordx4 v[208:211], v170, s[4:7], s58 offen offset:0 sc1\n"
  "buffer_load_dwordx4 v[212:215], v171, s[4:7], s58 offen offset:0 sc1\n"
  "buffer_load_dwordx4 v[216:219], v170, s[4:7], s58 offen offset:2048 sc1\n"
  "buffer_load_dwordx4 v[220:223], v171, s[4:7], s58 offen offset:2048 sc1\n"
  "buffer_load_dwordx4 v[224:227], v170, s[4:7], s67 offen offset:0 sc1\n"
  "buffer_load_dwordx4 v[228:231], v171, s[4:7], s67 offen offset:0 sc1\n"
  "buffer_load_dwordx4 v[232:235], v170, s[4:7], s67 offen offset:2048 sc1\n"
  "buffer_load_dwordx4 v[236:239], v171, s[4:7], s67 offen offset:2048 sc1\n"
  "buffer_load_dwordx4 v[240:243], v170, s[4:7], s68 offen offset:0 sc1\n"
  "buffer_load_dwordx4 v[244:247], v171, s[4:7], s68 offen offset:0 sc1\n"
  "buffer_load_dwordx4 v[248:251], v170, s[4:7], s68 offen offset:2048 sc1\n"
  "s_waitcnt vmcnt(0)\n"
  "s_branch Lb18\n"
  "Lslow19:\n"
  "s_add_u32 s76, s76, 1\n"
  "s_cmp_gt_u32 s76, 0x200000\n"
  "s_cbranch_scc1 Lb19\n"
  "buffer_load_dwordx4 v[200:203], v170, s[4:7], s72 offen offset:2048 sc1\n"
  "buffer_load_dwordx4 v[204:207], v171, s[4:7], s72 offen offset:2048 sc1\n"
  "s_waitcnt vmcnt(0)\n"
  "v_xor_b32 v200, v34, v200\n"
  "v_xor_b32 v202, v35, v202\n"
  "v_xor_b32 v204, v34, v204\n"
  "v_xor_b32 v206, v35, v206\n"
  "v_or3_b32 v52, v200, v202, v204\n"
  "v_bitop3_b32 v52, v52, s33, v206 bitop3:0xc8\n"
  "v_cmp_ne_u32_e64 vcc, 0, v52\n"
  "s_cbranch_vccnz Lslow19\n"
  "buffer_load_dwordx4 v[208:211], v170, s[4:7], s58 offen offset:0 sc1\n"
  "buffer_load_dwordx4 v[212:215], v171, s[4:7], s58 offen offset:0 sc1\n"
  "buffer_load_dwordx4 v[216:219], v170, s[4:7], s58 offen offset:2048 sc1\n"
  "buffer_load_dwordx4 v[220:223], v171, s[4:7], s58 offen offset:2048 sc1\n"
  "buffer_load_dwordx4 v[224:227], v170, s[4:7], s67 offen offset:0 sc1\n"
  "buffer_load_dwordx4 v[228:231], v171, s[4:7], s67 offen offset:0 sc1\n"
  "buffer_load_dwordx4 v[232:235], v170, s[4:7], s67 offen offset:2048 sc1\n"
  "buffer_load_dwordx4 v[236:239], v171, s[4:7], s67 offen offset:2048 sc1\n"
  "buffer_load_dwordx4 v[240:243], v170, s[4:7], s68 offen offset:0 sc1\n"
  "buffer_load_dwordx4 v[244:247], v171, s[4:7], s68 offen offset:0 sc1\n"
  "buffer_load_dwordx4 v[248:251], v170, s[4:7], s68 offen offset:2048 sc1\n"
  "s_waitcnt vmcnt(0)\n"
  "s_branch Lb19\n"
  "Lslow20:\n"
  "s_add_u32 s76, s76, 1\n"
  "s_cmp_gt_u32 s76, 0x200000\n"
  "s_cbranch_scc1 Lb20\n"
  "buffer_load_dwordx4 v[208:211], v170, s[4:7], s58 offen offset:0 sc1\n"
  "buffer_load_dwordx4 v[212:215], v171, s[4:7], s58 offen offset:0 sc1\n"
  "s_waitcnt vmcnt(0)\n"
  "v_xor_b32 v208, v34, v208\n"
  "v_xor_b32 v210, v35, v210\n"
  "v_xor_b32 v212, v34, v212\n"
  "v_xor_b32 v214, v35, v214\n"
  "v_or3_b32 v52, v208, v210, v212\n"
  "v_bitop3_b32 v52, v52, s33, v214 bitop3:0xc8\n"
  "v_cmp_ne_u32_e64 vcc, 0, v52\n"
  "s_cbranch_vccnz Lslow20\n"
  "buffer_load_dwordx4 v[216:219], v170, s[4:7], s58 offen offset:2048 sc1\n"
  "buffer_load_dwordx4 v[220:223], v171, s[4:7], s58 offen offset:2048 sc1\n"
  "buffer_load_dwordx4 v[224:227], v170, s[4:7], s67 offen offset:0 sc1\n"
  "buffer_load_dwordx4 v[228:231], v171, s[4:7], s67 offen offset:0 sc1\n"
  "buffer_load_dwordx4 v[232:235], v170, s[4:7], s67 offen offset:2048 sc1\n"
  "buffer_load_dwordx4 v[236:239], v171, s[4:7], s67 offen offset:2048 sc1\n"
  "buffer_load_dwordx4 v[240:243], v170, s[4:7], s68 offen offset:0 sc1\n"
  "buffer_load_dwordx4 v[244:247], v171, s[4:7], s68 offen offset:0 sc1\n"
  "buffer_load_dwordx4 v[248:251], v170, s[4:7], s68 offen offset:2048 sc1\n"
  "buffer_load_dwordx4 v[252:255], v171, s[4:7], s68 offen offset:2048 sc1\n"
  "buffer_load_dwordx4 v[192:195], v170, s[4:7], s78 offen offset:0 sc1\n"
  "buffer_load_dwordx4 v[196:199], v171, s[4:7], s78 offen offset:0 sc1\n"
  "s_waitcnt vmcnt(0)\n"
  "s_branch Lb20\n"
  "Lslow21:\n"
  "s_add_u32 s76, s76, 1\n"
  "s_cmp_gt_u32 s76, 0x200000\n"
  "s_cbranch_scc1 Lb21\n"
  "buffer_load_dwordx4 v[216:219], v170, s[4:7], s58 offen offset:2048 sc1\n"
  "buffer_load_dwordx4 v[220:223], v171, s[4:7], s58 offen offset:2048 sc1\n"
  "s_waitcnt vmcnt(0)\n"
  "v_xor_b32 v216, v34, v216\n"
  "v_xor_b32 v218, v35, v218\n"
  "v_xor_b32 v220, v34, v220\n"
  "v_xor_b32 v222, v35, v222\n"
  "v_or3_b32 v52, v216, v218, v220\n"
  "v_bitop3_b32 v52, v52, s33, v222 bitop3:0xc8\n"
  "v_cmp_ne_u32_e64 vcc, 0, v52\n"
  "s_cbranch_vccnz Lslow21\n"
  "buffer_load_dwordx4 v[224:227], v170, s[4:7], s67 offen offset:0 sc1\n"
  "buffer_load_dwordx4 v[228:231], v171, s[4:7], s67 offen offset:0 sc1\n"
  "buffer_load_dwordx4 v[232:235], v170, s[4:7], s67 offen offset:2048 sc1\n"
  "buffer_load_dwordx4 v[236:239], v171, s[4:7], s67 offen offset:2048 sc1\n"
  "buffer_load_dwordx4 v[240:243], v170, s[4:7], s68 offen offset:0 sc1\n"
  "buffer_load_dwordx4 v[244:247], v171, s[4:7], s68 offen offset:0 sc1\n"
  "buffer_load_dwordx4 v[248:251], v170, s[4:7], s68 offen offset:2048 sc1\n"
  "buffer_load_dwordx4 v[252:255], v171, s[4:7], s68 offen offset:2048 sc1\n"
  "buffer_load_dwordx4 v[192:195], v170, s[4:7], s78 offen offset:0 sc1\n"
  "buffer_load_dwordx4 v[196:199], v171, s[4:7], s78 offen offset:0 sc1\n"
  "buffer_load_dwordx4 v[200:203], v170, s[4:7], s78 offen offset:2048 sc1\n"
  "buffer_load_dwordx4 v[204:207], v171, s[4:7], s78 offen offset:2048 sc1\n"
  "s_waitcnt vmcnt(0)\n"
  "s_branch Lb21\n"
  "Lslow22:\n"
  "s_add_u32 s76, s76, 1\n"
  "s_cmp_gt_u32 s76, 0x200000\n"
  "s_cbranch_scc1 Lb22\n"
  "buffer_load_dwordx4 v[224:227], v170, s[4:7], s67 offen offset:0 sc1\n"
  "buffer_load_dwordx4 v[228:231], v171, s[4:7], s67 offen offset:0 sc1\n"
  "s_waitcnt vmcnt(0)\n"
  "v_xor_b32 v224, v34, v224\n"
  "v_xor_b32 v226, v35, v226\n"
  "v_xor_b32 v228, v34, v228\n"
  "v_xor_b32 v230, v35, v230\n"
  "v_or3_b32 v52, v224, v226, v228\n"
  "v_bitop3_b32 v52, v52, s33, v230 bitop3:0xc8\n"
  "v_cmp_ne_u32_e64 vcc, 0, v52\n"
  "s_cbranch_vccnz Lslow22\n"
  "buffer_load_dwordx4 v[232:235], v170, s[4:7], s67 offen offset:2048 sc1\n"
  "buffer_load_dwordx4 v[236:239], v171, s[4:7], s67 offen offset:2048 sc1\n"
  "buffer_load_dwordx4 v[240:243], v170, s[4:7], s68 offen offset:0 sc1\n"
  "buffer_load_dwordx4 v[244:247], v171, s[4:7], s68 offen offset:0 sc1\n"
  "buffer_load_dwordx4 v[248:251], v170, s[4:7], s68 offen offset:2048 sc1\n"
  "buffer_load_dwordx4 v[252:255], v171, s[4:7], s68 offen offset:2048 sc1\n"
  "buffer_load_dwordx4 v[192:195], v170, s[4:7], s78 offen offset:0 sc1\n"
  "buffer_load_dwordx4 v[196:199], v171, s[4:7], s78 offen offset:0 sc1\n"
  "buffer_load_dwordx4 v[200:203], v170, s[4:7], s78 offen offset:2048 sc1\n"
  "buffer_load_dwordx4 v[204:207], v171, s[4:7], s78 offen offset:2048 sc1\n"
  "buffer_load_dwordx4 v[208:211], v170, s[4:7], s79 offen offset:0 sc1\n"
  "buffer_load_dwordx4 v[212:215], v171, s[4:7], s79 offen offset:0 sc1\n"
  "s_waitcnt vmcnt(0)\n"
  "s_branch Lb22\n"
  "Lslow23:\n"
  "s_add_u32 s76, s76, 1\n"
  "s_cmp_gt_u32 s76, 0x200000\n"
  "s_cbranch_scc1 Lb23\n"
  "buffer_load_dwordx4 v[232:235], v170, s[4:7], s67 offen offset:2048 sc1\n"
  "buffer_load_dwordx4 v[236:239], v171, s[4:7], s67 offen offset:2048 sc1\n"
  "s_waitcnt vmcnt(0)\n"
  "v_xor_b32 v232, v34, v232\n"
  "v_xor_b32 v234, v35, v234\n"
  "v_xor_b32 v236, v34, v236\n"
  "v_xor_b32 v238, v35, v238\n"
  "v_or3_b32 v52, v232, v234, v236\n"
  "v_bitop3_b32 v52, v52, s33, v238 bitop3:0xc8\n"
  "v_cmp_ne_u32_e64 vcc, 0, v52\n"
  "s_cbranch_vccnz Lslow23\n"
  "buffer_load_dwordx4 v[240:243], v170, s[4:7], s68 offen offset:0 sc1\n"
  "buffer_load_dwordx4 v[244:247], v171, s[4:7], s68 offen offset:0 sc1\n"
  "buffer_load_dwordx4 v[248:251], v170, s[4:7], s68 offen offset:2048 sc1\n"
  "buffer_load_dwordx4 v[252:255], v171, s[4:7], s68 offen offset:2048 sc1\n"
  "buffer_load_dwordx4 v[192:195], v170, s[4:7], s78 offen offset:0 sc1\n"
  "buffer_load_dwordx4 v[196:199], v171, s[4:7], s78 offen offset:0 sc1\n"
  "buffer_load_dwordx4 v[200:203], v170, s[4:7], s78 offen offset:2048 sc1\n"
  "buffer_load_dwordx4 v[204:207], v171, s[4:7], s78 offen offset:2048 sc1\n"
  "buffer_load_dwordx4 v[208:211], v170, s[4:7], s79 offen offset:0 sc1\n"
  "buffer_load_dwordx4 v[212:215], v171, s[4:7], s79 offen offset:0 sc1\n"
  "buffer_load_dwordx4 v[216:219], v170, s[4:7], s79 offen offset:2048 sc1\n"
  "s_waitcnt vmcnt(0)\n"
  "s_branch Lb23\n"
  "Lslow24:\n"
  "s_add_u32 s76, s76, 1\n"
  "s_cmp_gt_u32 s76, 0x200000\n"
  "s_cbranch_scc1 Lb24\n"
  "buffer_load_dwordx4 v[240:243], v170, s[4:7], s68 offen offset:0 sc1\n"
  "buffer_load_dwordx4 v[244:247], v171, s[4:7], s68 offen offset:0 sc1\n"
  "s_waitcnt vmcnt(0)\n"
  "v_xor_b32 v240, v34, v240\n"
  "v_xor_b32 v242, v35, v242\n"
  "v_xor_b32 v244, v34, v244\n"
  "v_xor_b32 v246, v35, v246\n"
  "v_or3_b32 v52, v240, v242, v244\n"
  "v_bitop3_b32 v52, v52, s33, v246 bitop3:0xc8\n"
  "v_cmp_ne_u32_e64 vcc, 0, v52\n"
  "s_cbranch_vccnz Lslow24\n"
  "buffer_load_dwordx4 v[248:251], v170, s[4:7], s68 offen offset:2048 sc1\n"
  "buffer_load_dwordx4 v[252:255], v171, s[4:7], s68 offen offset:2048 sc1\n"
  "buffer_load_dwordx4 v[192:195], v170, s[4:7], s78 offen offset:0 sc1\n"
  "buffer_load_dwordx4 v[196:199], v171, s[4:7], s78 offen offset:0 sc1\n"
  "buffer_load_dwordx4 v[200:203], v170, s[4:7], s78 offen offset:2048 sc1\n"
  "buffer_load_dwordx4 v[204:207], v171, s[4:7], s78 offen offset:2048 sc1\n"
  "buffer_load_dwordx4 v[208:211], v170, s[4:7], s79 offen offset:0 sc1\n"
  "buffer_load_dwordx4 v[212:215], v171, s[4:7], s79 offen offset:0 sc1\n"
  "buffer_load_dwordx4 v[216:219], v170, s[4:7], s79 offen offset:2048 sc1\n"
  "buffer_load_dwordx4 v[220:223], v171, s[4:7], s79 offen offset:2048 sc1\n"
  "s_waitcnt vmcnt(0)\n"
  "s_branch Lb24\n"
  "Lslow25:\n"
  "s_add_u32 s76, s76, 1\n"
  "s_cmp_gt_u32 s76, 0x200000\n"
  "s_cbranch_scc1 Lb25\n"
  "buffer_load_dwordx4 v[248:251], v170, s[4:7], s68 offen offset:2048 sc1\n"
  "buffer_load_dwordx4 v[252:255], v171, s[4:7], s68 offen offset:2048 sc1\n"
  "s_waitcnt vmcnt(0)\n"
  "v_xor_b32 v248, v34, v248\n"
  "v_xor_b32 v250, v35, v250\n"
  "v_xor_b32 v252, v34, v252\n"
  "v_xor_b32 v254, v35, v254\n"
  "v_or3_b32 v52, v248, v250, v252\n"
  "v_bitop3_b32 v52, v52, s33, v254 bitop3:0xc8\n"
  "v_cmp_ne_u32_e64 vcc, 0, v52\n"
  "s_cbranch_vccnz Lslow25\n"
  "buffer_load_dwordx4 v[192:195], v170, s[4:7], s78 offen offset:0 sc1\n"
  "buffer_load_dwordx4 v[196:199], v171, s[4:7], s78 offen offset:0 sc1\n"
  "buffer_load_dwordx4 v[200:203], v170, s[4:7], s78 offen offset:2048 sc1\n"
  "buffer_load_dwordx4 v[204:207], v171, s[4:7], s78 offen offset:2048 sc1\n"
  "buffer_load_dwordx4 v[208:211], v170, s[4:7], s79 offen offset:0 sc1\n"
  "buffer_load_dwordx4 v[212:215], v171, s[4:7], s79 offen offset:0 sc1\n"
  "buffer_load_dwordx4 v[216:219], v170, s[4:7], s79 offen offset:2048 sc1\n"
  "buffer_load_dwordx4 v[220:223], v171, s[4:7], s79 offen offset:2048 sc1\n"
  "buffer_load_dwordx4 v[224:227], v170, s[4:7], s69 offen offset:0 sc1\n"
  "s_waitcnt vmcnt(0)\n"
  "s_branch Lb25\n"
  "Lslow26:\n"
  "s_add_u32 s76, s76, 1\n"
  "s_cmp_gt_u32 s76, 0x200000\n"
  "s_cbranch_scc1 Lb26\n"
  "buffer_load_dwordx4 v[192:195], v170, s[4:7], s78 offen offset:0 sc1\n"
  "buffer_load_dwordx4 v[196:199], v171, s[4:7], s78 offen offset:0 sc1\n"
  "s_waitcnt vmcnt(0)\n"
  "v_xor_b32 v192, v34, v192\n"
  "v_xor_b32 v194, v35, v194\n"
  "v_xor_b32 v196, v34, v196\n"
  "v_xor_b32 v198, v35, v198\n"
  "v_or3_b32 v52, v192, v194, v196\n"
  "v_bitop3_b32 v52, v52, s33, v198 bitop3:0xc8\n"
  "v_cmp_ne_u32_e64 vcc, 0, v52\n"
  "s_cbranch_vccnz Lslow26\n"
  "buffer_load_dwordx4 v[200:203], v170, s[4:7], s78 offen offset:2048 sc1\n"
  "buffer_load_dwordx4 v[204:207], v171, s[4:7], s78 offen offset:2048 sc1\n"
  "buffer_load_dwordx4 v[208:211], v170, s[4:7], s79 offen offset:0 sc1\n"
  "buffer_load_dwordx4 v[212:215], v171, s[4:7], s79 offen offset:0 sc1\n"
  "buffer_load_dwordx4 v[216:219], v170, s[4:7], s79 offen offset:2048 sc1\n"
  "buffer_load_dwordx4 v[220:223], v171, s[4:7], s79 offen offset:2048 sc1\n"
  "buffer_load_dwordx4 v[224:227], v170, s[4:7], s69 offen offset:0 sc1\n"
  "buffer_load_dwordx4 v[228:231], v171, s[4:7], s69 offen offset:0 sc1\n"
  "buffer_load_dwordx4 v[232:235], v170, s[4:7], s69 offen offset:2048 sc1\n"
  "s_waitcnt vmcnt(0)\n"
  "s_branch Lb26\n"
  "Lslow27:\n"
  "s_add_u32 s76, s76, 1\n"
  "s_cmp_gt_u32 s76, 0x200000\n"
  "s_cbranch_scc1 Lb27\n"
  "buffer_load_dwordx4 v[200:203], v170, s[4:7], s78 offen offset:2048 sc1\n"
  "buffer_load_dwordx4 v[204:207], v171, s[4:7], s78 offen offset:2048 sc1\n"
  "s_waitcnt vmcnt(0)\n"
  "v_xor_b32 v200, v34, v200\n"
  "v_xor_b32 v202, v35, v202\n"
  "v_xor_b32 v204, v34, v204\n"
  "v_xor_b32 v206, v35, v206\n"
  "v_or3_b32 v52, v200, v202, v204\n"
  "v_bitop3_b32 v52, v52, s33, v206 bitop3:0xc8\n"
  "v_cmp_ne_u32 vcc, 0, v52\n"
  "s_cbranch_vccnz Lslow27\n"
  "s_branch Lb27\n"
  "Lslow28:\n"
  "s_add_u32 s76, s76, 1\n"
  "s_cmp_gt_u32 s76, 0x200000\n"
  "s_cbranch_scc1 Lb28\n"
  "buffer_load_dwordx4 v[208:211], v170, s[4:7], s79 offen offset:0 sc1\n"
  "buffer_load_dwordx4 v[212:215], v171, s[4:7], s79 offen offset:0 sc1\n"
  "s_waitcnt vmcnt(0)\n"
  "v_xor_b32 v208, v34, v208\n"
  "v_xor_b32 v210, v35, v210\n"
  "v_xor_b32 v212, v34, v212\n"
  "v_xor_b32 v214, v35, v214\n"
  "v_or3_b32 v52, v208, v210, v212\n"
  "v_bitop3_b32 v52, v52, s33, v214 bitop3:0xc8\n"
  "v_cmp_ne_u32 vcc, 0, v52\n"
  "s_cbranch_vccnz Lslow28\n"
  "s_branch Lb28\n"
  "Lslow29:\n"
  "s_add_u32 s76, s76, 1\n"
  "s_cmp_gt_u32 s76, 0x200000\n"
  "s_cbranch_scc1 Lb29\n"
  "buffer_load_dwordx4 v[216:219], v170, s[4:7], s79 offen offset:2048 sc1\n"
  "buffer_load_dwordx4 v[220:223], v171, s[4:7], s79 offen offset:2048 sc1\n"
  "s_waitcnt vmcnt(0)\n"
  "v_xor_b32 v216, v34, v216\n"
  "v_xor_b32 v218, v35, v218\n"
  "v_xor_b32 v220, v34, v220\n"
  "v_xor_b32 v222, v35, v222\n"
  "v_or3_b32 v52, v216, v218, v220\n"
  "v_bitop3_b32 v52, v52, s33, v222 bitop3:0xc8\n"
  "v_cmp_ne_u32 vcc, 0, v52\n"
  "s_cbranch_vccnz Lslow29\n"
  "s_branch Lb29\n"
  "Lslow30:\n"
  "s_add_u32 s76, s76, 1\n"
  "s_cmp_gt_u32 s76, 0x200000\n"
  "s_cbranch_scc1 Lb30\n"
  "buffer_load_dwordx4 v[224:227], v170, s[4:7], s69 offen offset:0 sc1\n"
  "buffer_load_dwordx4 v[228:231], v171, s[4:7], s69 offen offset:0 sc1\n"
  "s_waitcnt vmcnt(0)\n"
  "v_xor_b32 v224, v34, v224\n"
  "v_xor_b32 v226, v35, v226\n"
  "v_xor_b32 v228, v34, v228\n"
  "v_xor_b32 v230, v35, v230\n"
  "v_or3_b32 v52, v224, v226, v228\n"
  "v_bitop3_b32 v52, v52, s33, v230 bitop3:0xc8\n"
  "v_cmp_ne_u32 vcc, 0, v52\n"
  "s_cbranch_vccnz Lslow30\n"
  "s_branch Lb30\n"
  "Lslow31:\n"
  "s_add_u32 s76, s76, 1\n"
  "s_cmp_gt_u32 s76, 0x200000\n"
  "s_cbranch_scc1 Lb31\n"
  "buffer_load_dwordx4 v[232:235], v170, s[4:7], s69 offen offset:2048 sc1\n"
  "buffer_load_dwordx4 v[236:239], v171, s[4:7], s69 offen offset:2048 sc1\n"
  "s_waitcnt vmcnt(0)\n"
  "v_xor_b32 v232, v34, v232\n"
  "v_xor_b32 v234, v35, v234\n"
  "v_xor_b32 v236, v34, v236\n"
  "v_xor_b32 v238, v35, v238\n"
  "v_or3_b32 v52, v232, v234, v236\n"
  "v_bitop3_b32 v52, v52, s33, v238 bitop3:0xc8\n"
  "v_cmp_ne_u32 vcc, 0, v52\n"
  "s_cbranch_vccnz Lslow31\n"
  "s_branch Lb31\n"
  "Lslow32:\n"
  "s_add_u32 s76, s76, 1\n"
  "s_cmp_gt_u32 s76, 0x200000\n"
  "s_cbranch_scc1 Lb32\n"
  "buffer_load_dwordx4 v[240:243], v170, s[4:7], s77 offen offset:0 sc1\n"
  "buffer_load_dwordx4 v[244:247], v171, s[4:7], s77 offen offset:0 sc1\n"
  "s_waitcnt vmcnt(0)\n"
  "v_xor_b32 v240, v34, v240\n"
  "v_xor_b32 v242, v35, v242\n"
  "v_xor_b32 v244, v34, v244\n"
  "v_xor_b32 v246, v35, v246\n"
  "v_or3_b32 v52, v240, v242, v244\n"
  "v_bitop3_b32 v52, v52, s33, v246 bitop3:0xc8\n"
  "v_cmp_ne_u32 vcc, 0, v52\n"
  "s_cbranch_vccnz Lslow32\n"
  "s_branch Lb32\n"
  "Lslow33:\n"
  "s_add_u32 s76, s76, 1\n"
  "s_cmp_gt_u32 s76, 0x200000\n"
  "s_cbranch_scc1 Lb33\n"
  "buffer_load_dwordx4 v[248:251], v170, s[4:7], s77 offen offset:2048 sc1\n"
  "buffer_load_dwordx4 v[252:255], v171, s[4:7], s77 offen offset:2048 sc1\n"
  "s_waitcnt vmcnt(0)\n"
  "v_xor_b32 v248, v34, v248\n"
  "v_xor_b32 v250, v35, v250\n"
  "v_xor_b32 v252, v34, v252\n"
  "v_xor_b32 v254, v35, v254\n"
  "v_or3_b32 v52, v248, v250, v252\n"
  "v_bitop3_b32 v52, v52, s33, v254 bitop3:0xc8\n"
  "v_cmp_ne_u32 vcc, 0, v52\n"
  "s_cbranch_vccnz Lslow33\n"
  "s_branch Lb33\n"
  "Lslow34:\n"
  "s_add_u32 s76, s76, 1\n"
  "s_cmp_gt_u32 s76, 0x200000\n"
  "s_cbranch_scc1 LbackHa\n"
  "buffer_load_dwordx4 v[36:39], v168, s[4:7], s74 offen sc1\n"
  "buffer_load_dwordx4 v[40:43], v170, s[4:7], s74 offen sc1\n"
  "buffer_load_dwordx4 v[176:179], v32, s[4:7], s50 offen offset:0 sc1\n"
  "buffer_load_dwordx4 v[180:183], v32, s[4:7], s50 offen offset:256 sc1\n"
  "s_waitcnt vmcnt(0)\n"
  "v_xor_b32 v183, s49, v183\n"
  "v_xor_b32 v181, s48, v181\n"
  "v_xor_b32 v179, s49, v179\n"
  "v_xor_b32 v177, s48, v177\n"
  "v_xor_b32 v40, v34, v40\n"
  "v_xor_b32 v42, v35, v42\n"
  "v_xor_b32 v36, v34, v36\n"
  "v_xor_b32_e64 v38, v35, v38\n"
  "v_or3_b32 v52, v40, v42, v36\n"
  "v_bitop3_b32 v52, v52, s33, v38 bitop3:0xc8\n"
  "v_or3_b32 v52, v52, v177, v179\n"
  "v_or3_b32 v52, v52, v181, v183\n"
  "v_cmp_ne_u32 vcc, 0, v52\n"
  "s_cbranch_vccnz Lslow34\n"
  "s_branch LbackHa\n"
  "Lslow35:\n"
  "s_add_u32 s76, s76, 1\n"
  "s_cmp_gt_u32 s76, 0x200000\n"
  "s_cbranch_scc1 Lb35\n"
  "buffer_load_dwordx4 v[192:195], v172, s[4:7], s73 offen offset:0 sc1\n"
  "buffer_load_dwordx4 v[196:199], v173, s[4:7], s73 offen offset:0 sc1\n"
  "s_waitcnt vmcnt(0)\n"
  "v_xor_b32 v192, v34, v192\n"
  "v_xor_b32 v194, v35, v194\n"
  "v_xor_b32 v196, v34, v196\n"
  "v_xor_b32 v198, v35, v198\n"
  "v_or3_b32 v52, v192, v194, v196\n"
  "v_bitop3_b32 v52, v52, s33, v198 bitop3:0xc8\n"
  "v_cmp_ne_u32 vcc, 0, v52\n"
  "s_cbranch_vccnz Lslow35\n"
  "buffer_load_dwordx4 v[200:203], v172, s[4:7], s73 offen offset:2048 sc1\n"
  "buffer_load_dwordx4 v[204:207], v173, s[4:7], s73 offen offset:2048 sc1\n"
  "buffer_load_dwordx4 v[208:211], v172, s[4:7], s0 offen offset:0 sc1\n"
  "buffer_load_dwordx4 v[212:215], v173, s[4:7], s0 offen offset:0 sc1\n"
  "buffer_load_dwordx4 v[216:219], v172, s[4:7], s0 offen offset:2048 sc1\n"
  "buffer_load_dwordx4 v[220:223], v173, s[4:7], s0 offen offset:2048 sc1\n"
  "buffer_load_dwordx4 v[224:227], v172, s[4:7], s1 offen offset:0 sc1\n"
  "buffer_load_dwordx4 v[228:231], v173, s[4:7], s1 offen offset:0 sc1\n"
  "buffer_load_dwordx4 v[232:235], v172, s[4:7], s1 offen offset:2048 sc1\n"
  "buffer_load_dwordx4 v[236:239], v173, s[4:7], s1 offen offset:2048 sc1\n"
  "buffer_load_dwordx4 v[240:243], v172, s[4:7], s3 offen offset:0 sc1\n"
  "buffer_load_dwordx4 v[244:247], v173, s[4:7], s3 offen offset:0 sc1\n"
  "buffer_load_dwordx4 v[248:251], v172, s[4:7], s3 offen offset:2048 sc1\n"
  "buffer_load_dwordx4 v[252:255], v173, s[4:7], s3 offen offset:2048 sc1\n"
  "s_waitcnt vmcnt(0)\n"
  "s_branch Lb35\n"
  "Lslow36:\n"
  "s_add_u32 s76, s76, 1\n"
  "s_cmp_gt_u32 s76, 0x200000\n"
  "s_cbranch_scc1 Lb36\n"
  "buffer_load_dwordx4 v[200:203], v172, s[4:7], s73 offen offset:2048 sc1\n"
  "buffer_load_dwordx4 v[204:207], v173, s[4:7], s73 offen offset:2048 sc1\n"
  "s_waitcnt vmcnt(0)\n"
  "v_xor_b32 v200, v34, v200\n"
  "v_xor_b32 v202, v35, v202\n"
  "v_xor_b32 v204, v34, v204\n"
  "v_xor_b32 v206, v35, v206\n"
  "v_or3_b32 v52, v200, v202, v204\n"
  "v_bitop3_b32 v52, v52, s33, v206 bitop3:0xc8\n"
  "v_cmp_ne_u32_e64 vcc, 0, v52\n"
  "s_cbranch_vccnz Lslow36\n"
  "buffer_load_dwordx4 v[208:211], v172, s[4:7], s0 offen offset:0 sc1\n"
  "buffer_load_dwordx4 v[212:215], v173, s[4:7], s0 offen offset:0 sc1\n"
  "buffer_load_dwordx4 v[216:219], v172, s[4:7], s0 offen offset:2048 sc1\n"
  "buffer_load_dwordx4 v[220:223], v173, s[4:7], s0 offen offset:2048 sc1\n"
  "buffer_load_dwordx4 v[224:227], v172, s[4:7], s1 offen offset:0 sc1\n"
  "buffer_load_dwordx4 v[228:231], v173, s[4:7], s1 offen offset:0 sc1\n"
  "buffer_load_dwordx4 v[232:235], v172, s[4:7], s1 offen offset:2048 sc1\n"
  "buffer_load_dwordx4 v[236:239], v173, s[4:7], s1 offen offset:2048 sc1\n"
  "buffer_load_dwordx4 v[240:243], v172, s[4:7], s3 offen offset:0 sc1\n"
  "buffer_load_dwordx4 v[244:247], v173, s[4:7], s3 offen offset:0 sc1\n"
  "buffer_load_dwordx4 v[248:251], v172, s[4:7], s3 offen offset:2048 sc1\n"
  "buffer_load_dwordx4 v[252:255], v173, s[4:7], s3 offen offset:2048 sc1\n"
  "s_waitcnt vmcnt(0)\n"
  "s_branch Lb36\n"
  "Lslow37:\n"
  "s_add_u32 s76, s76, 1\n"
  "s_cmp_gt_u32 s76, 0x200000\n"
  "s_cbranch_scc1 Lb37\n"
  "buffer_load_dwordx4 v[208:211], v172, s[4:7], s0 offen offset:0 sc1\n"
  "buffer_load_dwordx4 v[212:215], v173, s[4:7], s0 offen offset:0 sc1\n"
  "s_waitcnt vmcnt(0)\n"
  "v_xor_b32 v208, v34, v208\n"
  "v_xor_b32 v210, v35, v210\n"
  "v_xor_b32 v212, v34, v212\n"
  "v_xor_b32 v214, v35, v214\n"
  "v_or3_b32 v52, v208, v210, v212\n"
  "v_bitop3_b32 v52, v52, s33, v214 bitop3:0xc8\n"
  "v_cmp_ne_u32_e64 vcc, 0, v52\n"
  "s_cbranch_vccnz Lslow37\n"
  "buffer_load_dwordx4 v[216:219], v172, s[4:7], s0 offen offset:2048 sc1\n"
  "buffer_load_dwordx4 v[220:223], v173, s[4:7], s0 offen offset:2048 sc1\n"
  "buffer_load_dwordx4 v[224:227], v172, s[4:7], s1 offen offset:0 sc1\n"
  "buffer_load_dwordx4 v[228:231], v173, s[4:7], s1 offen offset:0 sc1\n"
  "buffer_load_dwordx4 v[232:235], v172, s[4:7], s1 offen offset:2048 sc1\n"
  "buffer_load_dwordx4 v[236:239], v173, s[4:7], s1 offen offset:2048 sc1\n"
  "buffer_load_dwordx4 v[240:243], v172, s[4:7], s3 offen offset:0 sc1\n"
  "buffer_load_dwordx4 v[244:247], v173, s[4:7], s3 offen offset:0 sc1\n"
  "buffer_load_dwordx4 v[248:251], v172, s[4:7], s3 offen offset:2048 sc1\n"
  "buffer_load_dwordx4 v[252:255], v173, s[4:7], s3 offen offset:2048 sc1\n"
  "s_waitcnt vmcnt(0)\n"
  "s_branch Lb37\n"
  "Lslow38:\n"
  "s_add_u32 s76, s76, 1\n"
  "s_cmp_gt_u32 s76, 0x200000\n"
  "s_cbranch_scc1 Lb38\n"
  "buffer_load_dwordx4 v[216:219], v172, s[4:7], s0 offen offset:2048 sc1\n"
  "buffer_load_dwordx4 v[220:223], v173, s[4:7], s0 offen offset:2048 sc1\n"
  "s_waitcnt vmcnt(0)\n"
  "v_xor_b32 v216, v34, v216\n"
  "v_xor_b32 v218, v35, v218\n"
  "v_xor_b32 v220, v34, v220\n"
  "v_xor_b32 v222, v35, v222\n"
  "v_or3_b32 v52, v216, v218, v220\n"
  "v_bitop3_b32 v52, v52, s33, v222 bitop3:0xc8\n"
  "v_cmp_ne_u32_e64 vcc, 0, v52\n"
  "s_cbranch_vccnz Lslow38\n"
  "buffer_load_dwordx4 v[224:227], v172, s[4:7], s1 offen offset:0 sc1\n"
  "buffer_load_dwordx4 v[228:231], v173, s[4:7], s1 offen offset:0 sc1\n"
  "buffer_load_dwordx4 v[232:235], v172, s[4:7], s1 offen offset:2048 sc1\n"
  "buffer_load_dwordx4 v[236:239], v173, s[4:7], s1 offen offset:2048 sc1\n"
  "buffer_load_dwordx4 v[240:243], v172, s[4:7], s3 offen offset:0 sc1\n"
  "buffer_load_dwordx4 v[244:247], v173, s[4:7], s3 offen offset:0 sc1\n"
  "buffer_load_dwordx4 v[248:251], v172, s[4:7], s3 offen offset:2048 sc1\n"
  "buffer_load_dwordx4 v[252:255], v173, s[4:7], s3 offen offset:2048 sc1\n"
  "buffer_load_dwordx4 v[192:195], v172, s[4:7], s94 offen offset:0 sc1\n"
  "buffer_load_dwordx4 v[196:199], v173, s[4:7], s94 offen offset:0 sc1\n"
  "s_waitcnt vmcnt(0)\n"
  "s_branch Lb38\n"
  "Lslow39:\n"
  "s_add_u32 s76, s76, 1\n"
  "s_cmp_gt_u32 s76, 0x200000\n"
  "s_cbranch_scc1 Lb39\n"
  "buffer_load_dwordx4 v[224:227], v172, s[4:7], s1 offen offset:0 sc1\n"
  "buffer_load_dwordx4 v[228:231], v173, s[4:7], s1 offen offset:0 sc1\n"
  "s_waitcnt vmcnt(0)\n"
  "v_xor_b32 v224, v34, v224\n"
  "v_xor_b32 v226, v35, v226\n"
  "v_xor_b32 v228, v34, v228\n"
  "v_xor_b32 v230, v35, v230\n"
  "v_or3_b32 v52, v224, v226, v228\n"
  "v_bitop3_b32 v52, v52, s33, v230 bitop3:0xc8\n"
  "v_cmp_ne_u32_e64 vcc, 0, v52\n"
  "s_cbranch_vccnz Lslow39\n"
  "buffer_load_dwordx4 v[232:235], v172, s[4:7], s1 offen offset:2048 sc1\n"
  "buffer_load_dwordx4 v[236:239], v173, s[4:7], s1 offen offset:2048 sc1\n"
  "buffer_load_dwordx4 v[240:243], v172, s[4:7], s3 offen offset:0 sc1\n"
  "buffer_load_dwordx4 v[244:247], v173, s[4:7], s3 offen offset:0 sc1\n"
  "buffer_load_dwordx4 v[248:251], v172, s[4:7], s3 offen offset:2048 sc1\n"
  "buffer_load_dwordx4 v[252:255], v173, s[4:7], s3 offen offset:2048 sc1\n"
  "buffer_load_dwordx4 v[192:195], v172, s[4:7], s94 offen offset:0 sc1\n"
  "buffer_load_dwordx4 v[196:199], v173, s[4:7], s94 offen offset:0 sc1\n"
  "buffer_load_dwordx4 v[200:203], v172, s[4:7], s94 offen offset:2048 sc1\n"
  "buffer_load_dwordx4 v[204:207], v173, s[4:7], s94 offen offset:2048 sc1\n"
  "s_waitcnt vmcnt(0)\n"
  "s_branch Lb39\n"
  "Lslow40:\n"
  "s_add_u32 s76, s76, 1\n"
  "s_cmp_gt_u32 s76, 0x200000\n"
  "s_cbranch_scc1 Lb40\n"
  "buffer_load_dwordx4 v[232:235], v172, s[4:7], s1 offen offset:2048 sc1\n"
  "buffer_load_dwordx4 v[236:239], v173, s[4:7], s1 offen offset:2048 sc1\n"
  "s_waitcnt vmcnt(0)\n"
  "v_xor_b32 v232, v34, v232\n"
  "v_xor_b32 v234, v35, v234\n"
  "v_xor_b32 v236, v34, v236\n"
  "v_xor_b32 v238, v35, v238\n"
  "v_or3_b32 v52, v232, v234, v236\n"
  "v_bitop3_b32 v52, v52, s33, v238 bitop3:0xc8\n"
  "v_cmp_ne_u32_e64 vcc, 0, v52\n"
  "s_cbranch_vccnz Lslow40\n"
  "buffer_load_dwordx4 v[240:243], v172, s[4:7], s3 offen offset:0 sc1\n"
  "buffer_load_dwordx4 v[244:247], v173, s[4:7], s3 offen offset:0 sc1\n"
  "buffer_load_dwordx4 v[248:251], v172, s[4:7], s3 offen offset:2048 sc1\n"
  "buffer_load_dwordx4 v[252:255], v173, s[4:7], s3 offen offset:2048 sc1\n"
  "buffer_load_dwordx4 v[192:195], v172, s[4:7], s94 offen offset:0 sc1\n"
  "buffer_load_dwordx4 v[196:199], v173, s[4:7], s94 offen offset:0 sc1\n"
  "buffer_load_dwordx4 v[200:203], v172, s[4:7], s94 offen offset:2048 sc1\n"
  "buffer_load_dwordx4 v[204:207], v173, s[4:7], s94 offen offset:2048 sc1\n"
  "buffer_load_dwordx4 v[208:211], v172, s[4:7], s95 offen offset:0 sc1\n"
  "s_waitcnt vmcnt(0)\n"
  "s_branch Lb40\n"
  "Lslow41:\n"
  "s_add_u32 s76, s76, 1\n"
  "s_cmp_gt_u32 s76, 0x200000\n"
  "s_cbranch_scc1 Lb41\n"
  "buffer_load_dwordx4 v[240:243], v172, s[4:7], s3 offen offset:0 sc1\n"
  "buffer_load_dwordx4 v[244:247], v173, s[4:7], s3 offen offset:0 sc1\n"
  "s_waitcnt vmcnt(0)\n"
  "v_xor_b32 v240, v34, v240\n"
  "v_xor_b32 v242, v35, v242\n"
  "v_xor_b32 v244, v34, v244\n"
  "v_xor_b32 v246, v35, v246\n"
  "v_or3_b32 v52, v240, v242, v244\n"
  "v_bitop3_b32 v52, v52, s33, v246 bitop3:0xc8\n"
  "v_cmp_ne_u32_e64 vcc, 0, v52\n"
  "s_cbranch_vccnz Lslow41\n"
  "buffer_load_dwordx4 v[248:251], v172, s[4:7], s3 offen offset:2048 sc1\n"
  "buffer_load_dwordx4 v[252:255], v173, s[4:7], s3 offen offset:2048 sc1\n"
  "buffer_load_dwordx4 v[192:195], v172, s[4:7], s94 offen offset:0 sc1\n"
  "buffer_load_dwordx4 v[196:199], v173, s[4:7], s94 offen offset:0 sc1\n"
  "buffer_load_dwordx4 v[200:203], v172, s[4:7], s94 offen offset:2048 sc1\n"
  "buffer_load_dwordx4 v[204:207], v173, s[4:7], s94 offen offset:2048 sc1\n"
  "buffer_load_dwordx4 v[208:211], v172, s[4:7], s95 offen offset:0 sc1\n"
  "buffer_load_dwordx4 v[212:215], v173, s[4:7], s95 offen offset:0 sc1\n"
  "s_waitcnt vmcnt(0)\n"
  "s_branch Lb41\n"
  "Lslow42:\n"
  "s_add_u32 s76, s76, 1\n"
  "s_cmp_gt_u32 s76, 0x200000\n"
  "s_cbranch_scc1 Lb42\n"
  "buffer_load_dwordx4 v[248:251], v172, s[4:7], s3 offen offset:2048 sc1\n"
  "buffer_load_dwordx4 v[252:255], v173, s[4:7], s3 offen offset:2048 sc1\n"
  "s_waitcnt vmcnt(0)\n"
  "v_xor_b32 v248, v34, v248\n"
  "v_xor_b32 v250, v35, v250\n"
  "v_xor_b32 v252, v34, v252\n"
  "v_xor_b32 v254, v35, v254\n"
  "v_or3_b32 v52, v248, v250, v252\n"
  "v_bitop3_b32 v52, v52, s33, v254 bitop3:0xc8\n"
  "v_cmp_ne_u32_e64 vcc, 0, v52\n"
  "s_cbranch_vccnz Lslow42\n"
  "buffer_load_dwordx4 v[192:195], v172, s[4:7], s94 offen offset:0 sc1\n"
  "buffer_load_dwordx4 v[196:199], v173, s[4:7], s94 offen offset:0 sc1\n"
  "buffer_load_dwordx4 v[200:203], v172, s[4:7], s94 offen offset:2048 sc1\n"
  "buffer_load_dwordx4 v[204:207], v173, s[4:7], s94 offen offset:2048 sc1\n"
  "buffer_load_dwordx4 v[208:211], v172, s[4:7], s95 offen offset:0 sc1\n"
  "buffer_load_dwordx4 v[212:215], v173, s[4:7], s95 offen offset:0 sc1\n"
  "buffer_load_dwordx4 v[216:219], v172, s[4:7], s95 offen offset:2048 sc1\n"
  "buffer_load_dwordx4 v[220:223], v173, s[4:7], s95 offen offset:2048 sc1\n"
  "s_waitcnt vmcnt(0)\n"
  "s_branch Lb42\n"
  "Lslow43:\n"
  "s_add_u32 s76, s76, 1\n"
  "s_cmp_gt_u32 s76, 0x200000\n"
  "s_cbranch_scc1 Lb43\n"
  "buffer_load_dwordx4 v[192:195], v172, s[4:7], s94 offen offset:0 sc1\n"
  "buffer_load_dwordx4 v[196:199], v173, s[4:7], s94 offen offset:0 sc1\n"
  "s_waitcnt vmcnt(0)\n"
  "v_xor_b32 v192, v34, v192\n"
  "v_xor_b32 v194, v35, v194\n"
  "v_xor_b32 v196, v34, v196\n"
  "v_xor_b32 v198, v35, v198\n"
  "v_or3_b32 v52, v192, v194, v196\n"
  "v_bitop3_b32 v52, v52, s33, v198 bitop3:0xc8\n"
  "v_cmp_ne_u32_e64 vcc, 0, v52\n"
  "s_cbranch_vccnz Lslow43\n"
  "buffer_load_dwordx4 v[200:203], v172, s[4:7], s94 offen offset:2048 sc1\n"
  "buffer_load_dwordx4 v[204:207], v173, s[4:7], s94 offen offset:2048 sc1\n"
  "buffer_load_dwordx4 v[208:211], v172, s[4:7], s95 offen offset:0 sc1\n"
  "buffer_load_dwordx4 v[212:215], v173, s[4:7], s95 offen offset:0 sc1\n"
  "buffer_load_dwordx4 v[216:219], v172, s[4:7], s95 offen offset:2048 sc1\n"
  "buffer_load_dwordx4 v[220:223], v173, s[4:7], s95 offen offset:2048 sc1\n"
  "buffer_load_dwordx4 v[224:227], v172, s[4:7], s30 offen offset:0 sc1\n"
  "buffer_load_dwordx4 v[228:231], v173, s[4:7], s30 offen offset:0 sc1\n"
  "s_waitcnt vmcnt(0)\n"
  "s_branch Lb43\n"
  "Lslow44:\n"
  "s_add_u32 s76, s76, 1\n"
  "s_cmp_gt_u32 s76, 0x200000\n"
  "s_cbranch_scc1 Lb44\n"
  "buffer_load_dwordx4 v[200:203], v172, s[4:7], s94 offen offset:2048 sc1\n"
  "buffer_load_dwordx4 v[204:207], v173, s[4:7], s94 offen offset:2048 sc1\n"
  "s_waitcnt vmcnt(0)\n"
  "v_xor_b32 v200, v34, v200\n"
  "v_xor_b32 v202, v35, v202\n"
  "v_xor_b32 v204, v34, v204\n"
  "v_xor_b32 v206, v35, v206\n"
  "v_or3_b32 v52, v200, v202, v204\n"
  "v_bitop3_b32 v52, v52, s33, v206 bitop3:0xc8\n"
  "v_cmp_ne_u32 vcc, 0, v52\n"
  "s_cbranch_vccnz Lslow44\n"
  "s_branch Lb44\n"
  "Lslow45:\n"
  "s_add_u32 s76, s76, 1\n"
  "s_cmp_gt_u32 s76, 0x200000\n"
  "s_cbranch_scc1 Lb45\n"
  "buffer_load_dwordx4 v[208:211], v172, s[4:7], s95 offen offset:0 sc1\n"
  "buffer_load_dwordx4 v[212:215], v173, s[4:7], s95 offen offset:0 sc1\n"
  "s_waitcnt vmcnt(0)\n"
  "v_xor_b32 v208, v34, v208\n"
  "v_xor_b32 v210, v35, v210\n"
  "v_xor_b32 v212, v34, v212\n"
  "v_xor_b32 v214, v35, v214\n"
  "v_or3_b32 v52, v208, v210, v212\n"
  "v_bitop3_b32 v52, v52, s33, v214 bitop3:0xc8\n"
  "v_cmp_ne_u32 vcc, 0, v52\n"
  "s_cbranch_vccnz Lslow45\n"
  "s_branch Lb45\n"
  "Lslow46:\n"
  "s_add_u32 s76, s76, 1\n"
  "s_cmp_gt_u32 s76, 0x200000\n"
  "s_cbranch_scc1 Lb46\n"
  "buffer_load_dwordx4 v[216:219], v172, s[4:7], s95 offen offset:2048 sc1\n"
  "buffer_load_dwordx4 v[220:223], v173, s[4:7], s95 offen offset:2048 sc1\n"
  "s_waitcnt vmcnt(0)\n"
  "v_xor_b32 v216, v34, v216\n"
  "v_xor_b32 v218, v35, v218\n"
  "v_xor_b32 v220, v34, v220\n"
  "v_xor_b32 v222, v35, v222\n"
  "v_or3_b32 v52, v216, v218, v220\n"
  "v_bitop3_b32 v52, v52, s33, v222 bitop3:0xc8\n"
  "v_cmp_ne_u32 vcc, 0, v52\n"
  "s_cbranch_vccnz Lslow46\n"
  "s_branch Lb46\n"
  "Lslow47:\n"
  "s_add_u32 s76, s76, 1\n"
  "s_cmp_gt_u32 s76, 0x200000\n"
  "s_cbranch_scc1 Lb47\n"
  "buffer_load_dwordx4 v[224:227], v172, s[4:7], s30 offen offset:0 sc1\n"
  "buffer_load_dwordx4 v[228:231], v173, s[4:7], s30 offen offset:0 sc1\n"
  "s_waitcnt vmcnt(0)\n"
  "v_xor_b32 v224, v34, v224\n"
  "v_xor_b32 v226, v35, v226\n"
  "v_xor_b32 v228, v34, v228\n"
  "v_xor_b32 v230, v35, v230\n"
  "v_or3_b32 v52, v224, v226, v228\n"
  "v_bitop3_b32 v52, v52, s33, v230 bitop3:0xc8\n"
  "v_cmp_ne_u32 vcc, 0, v52\n"
  "s_cbranch_vccnz Lslow47\n"
  "s_branch Lb47\n"
  "Lslow48:\n"
  "s_add_u32 s76, s76, 1\n"
  "s_cmp_gt_u32 s76, 0x200000\n"
  "s_cbranch_scc1 Lb48\n"
  "buffer_load_dwordx4 v[232:235], v172, s[4:7], s30 offen offset:2048 sc1\n"
  "buffer_load_dwordx4 v[236:239], v173, s[4:7], s30 offen offset:2048 sc1\n"
  "s_waitcnt vmcnt(0)\n"
  "v_xor_b32 v232, v34, v232\n"
  "v_xor_b32 v234, v35, v234\n"
  "v_xor_b32 v236, v34, v236\n"
  "v_xor_b32 v238, v35, v238\n"
  "v_or3_b32 v52, v232, v234, v236\n"
  "v_bitop3_b32 v52, v52, s33, v238 bitop3:0xc8\n"
  "v_cmp_ne_u32 vcc, 0, v52\n"
  "s_cbranch_vccnz Lslow48\n"
  "s_branch Lb48\n"
  "Lslow49:\n"
  "s_add_u32 s76, s76, 1\n"
  "s_cmp_gt_u32 s76, 0x200000\n"
  "s_cbranch_scc1 Lb49\n"
  "buffer_load_dwordx4 v[240:243], v174, s[4:7], s73 offen offset:0 sc1\n"
  "buffer_load_dwordx4 v[244:247], v175, s[4:7], s73 offen offset:0 sc1\n"
  "s_waitcnt vmcnt(0)\n"
  "v_xor_b32 v240, v34, v240\n"
  "v_xor_b32 v242, v35, v242\n"
  "v_xor_b32 v244, v34, v244\n"
  "v_xor_b32 v246, v35, v246\n"
  "v_or3_b32 v52, v240, v242, v244\n"
  "v_bitop3_b32 v52, v52, s33, v246 bitop3:0xc8\n"
  "v_cmp_ne_u32_e64 vcc, 0, v52\n"
  "s_cbranch_vccnz Lslow49\n"
  "buffer_load_dwordx4 v[248:251], v174, s[4:7], s73 offen offset:2048 sc1\n"
  "buffer_load_dwordx4 v[252:255], v175, s[4:7], s73 offen offset:2048 sc1\n"
  "buffer_load_dwordx4 v[192:195], v174, s[4:7], s0 offen offset:0 sc1\n"
  "buffer_load_dwordx4 v[196:199], v175, s[4:7], s0 offen offset:0 sc1\n"
  "buffer_load_dwordx4 v[200:203], v174, s[4:7], s0 offen offset:2048 sc1\n"
  "buffer_load_dwordx4 v[204:207], v175, s[4:7], s0 offen offset:2048 sc1\n"
  "buffer_load_dwordx4 v[208:211], v174, s[4:7], s1 offen offset:0 sc1\n"
  "buffer_load_dwordx4 v[212:215], v175, s[4:7], s1 offen offset:0 sc1\n"
  "buffer_load_dwordx4 v[216:219], v174, s[4:7], s1 offen offset:2048 sc1\n"
  "buffer_load_dwordx4 v[220:223], v175, s[4:7], s1 offen offset:2048 sc1\n"
  "buffer_load_dwordx4 v[224:227], v175, s[4:7], s3 offen offset:0 sc1\n"
  "buffer_load_dwordx4 v[228:231], v175, s[4:7], s3 offen offset:2048 sc1\n"
  "s_waitcnt vmcnt(0)\n"
  "s_branch Lb49\n"
  "Lslow50:\n"
  "s_add_u32 s76, s76, 1\n"
  "s_cmp_gt_u32 s76, 0x200000\n"
  "s_cbranch_scc1 Lb50\n"
  "buffer_load_dwordx4 v[248:251], v174, s[4:7], s73 offen offset:2048 sc1\n"
  "buffer_load_dwordx4 v[252:255], v175, s[4:7], s73 offen offset:2048 sc1\n"
  "s_waitcnt vmcnt(0)\n"
  "v_xor_b32 v248, v34, v248\n"
  "v_xor_b32 v250, v35, v250\n"
  "v_xor_b32 v252, v34, v252\n"
  "v_xor_b32 v254, v35, v254\n"
  "v_or3_b32 v52, v248, v250, v252\n"
  "v_bitop3_b32 v52, v52, s33, v254 bitop3:0xc8\n"
  "v_cmp_ne_u32_e64 vcc, 0, v52\n"
  "s_cbranch_vccnz Lslow50\n"
  "buffer_load_dwordx4 v[192:195], v174, s[4:7], s0 offen offset:0 sc1\n"
  "buffer_load_dwordx4 v[196:199], v175, s[4:7], s0 offen offset:0 sc1\n"
  "buffer_load_dwordx4 v[200:203], v174, s[4:7], s0 offen offset:2048 sc1\n"
  "buffer_load_dwordx4 v[204:207], v175, s[4:7], s0 offen offset:2048 sc1\n"
  "buffer_load_dwordx4 v[208:211], v174, s[4:7], s1 offen offset:0 sc1\n"
  "buffer_load_dwordx4 v[212:215], v175, s[4:7], s1 offen offset:0 sc1\n"
  "buffer_load_dwordx4 v[216:219], v174, s[4:7], s1 offen offset:2048 sc1\n"
  "buffer_load_dwordx4 v[220:223], v175, s[4:7], s1 offen offset:2048 sc1\n"
  "buffer_load_dwordx4 v[224:227], v175, s[4:7], s3 offen offset:0 sc1\n"
  "buffer_load_dwordx4 v[228:231], v175, s[4:7], s3 offen offset:2048 sc1\n"
  "s_waitcnt vmcnt(0)\n"
  "s_branch Lb50\n"
  "Lslow51:\n"
  "s_add_u32 s76, s76, 1\n"
  "s_cmp_gt_u32 s76, 0x200000\n"
  "s_cbranch_scc1 Lb51\n"
  "buffer_load_dwordx4 v[192:195], v174, s[4:7], s0 offen offset:0 sc1\n"
  "buffer_load_dwordx4 v[196:199], v175, s[4:7], s0 offen offset:0 sc1\n"
  "s_waitcnt vmcnt(0)\n"
  "v_xor_b32 v192, v34, v192\n"
  "v_xor_b32 v194, v35, v194\n"
  "v_xor_b32 v196, v34, v196\n"
  "v_xor_b32 v198, v35, v198\n"
  "v_or3_b32 v52, v192, v194, v196\n"
  "v_bitop3_b32 v52, v52, s33, v198 bitop3:0xc8\n"
  "v_cmp_ne_u32_e64 vcc, 0, v52\n"
  "s_cbranch_vccnz Lslow51\n"
  "buffer_load_dwordx4 v[200:203], v174, s[4:7], s0 offen offset:2048 sc1\n"
  "buffer_load_dwordx4 v[204:207], v175, s[4:7], s0 offen offset:2048 sc1\n"
  "buffer_load_dwordx4 v[208:211], v174, s[4:7], s1 offen offset:0 sc1\n"
  "buffer_load_dwordx4 v[212:215], v175, s[4:7], s1 offen offset:0 sc1\n"
  "buffer_load_dwordx4 v[216:219], v174, s[4:7], s1 offen offset:2048 sc1\n"
  "buffer_load_dwordx4 v[220:223], v175, s[4:7], s1 offen offset:2048 sc1\n"
  "buffer_load_dwordx4 v[224:227], v175, s[4:7], s3 offen offset:0 sc1\n"
  "buffer_load_dwordx4 v[228:231], v175, s[4:7], s3 offen offset:2048 sc1\n"
  "buffer_load_dwordx4 v[232:235], v175, s[4:7], s94 offen offset:0 sc1\n"
  "s_waitcnt vmcnt(0)\n"
  "s_branch Lb51\n"
  "Lslow52:\n"
  "s_add_u32 s76, s76, 1\n"
  "s_cmp_gt_u32 s76, 0x200000\n"
  "s_cbranch_scc1 Lb52\n"
  "buffer_load_dwordx4 v[200:203], v174, s[4:7], s0 offen offset:2048 sc1\n"
  "buffer_load_dwordx4 v[204:207], v175, s[4:7], s0 offen offset:2048 sc1\n"
  "s_waitcnt vmcnt(0)\n"
  "v_xor_b32 v200, v34, v200\n"
  "v_xor_b32 v202, v35, v202\n"
  "v_xor_b32 v204, v34, v204\n"
  "v_xor_b32 v206, v35, v206\n"
  "v_or3_b32 v52, v200, v202, v204\n"
  "v_bitop3_b32 v52, v52, s33, v206 bitop3:0xc8\n"
  "v_cmp_ne_u32_e64 vcc, 0, v52\n"
  "s_cbranch_vccnz Lslow52\n"
  "buffer_load_dwordx4 v[208:211], v174, s[4:7], s1 offen offset:0 sc1\n"
  "buffer_load_dwordx4 v[212:215], v175, s[4:7], s1 offen offset:0 sc1\n"
  "buffer_load_dwordx4 v[216:219], v174, s[4:7], s1 offen offset:2048 sc1\n"
  "buffer_load_dwordx4 v[220:223], v175, s[4:7], s1 offen offset:2048 sc1\n"
  "buffer_load_dwordx4 v[224:227], v175, s[4:7], s3 offen offset:0 sc1\n"
  "buffer_load_dwordx4 v[228:231], v175, s[4:7], s3 offen offset:2048 sc1\n"
  "buffer_load_dwordx4 v[232:235], v175, s[4:7], s94 offen offset:0 sc1\n"
  "buffer_load_dwordx4 v[236:239], v175, s[4:7], s94 offen offset:2048 sc1\n"
  "buffer_load_dwordx4 v[240:243], v175, s[4:7], s95 offen offset:0 sc1\n"
  "s_waitcnt vmcnt(0)\n"
  "s_branch Lb52\n"
  "Lslow53:\n"
  "s_add_u32 s76, s76, 1\n"
  "s_cmp_gt_u32 s76, 0x200000\n"
  "s_cbranch_scc1 Lb53\n"
  "buffer_load_dwordx4 v[208:211], v174, s[4:7], s1 offen offset:0 sc1\n"
  "buffer_load_dwordx4 v[212:215], v175, s[4:7], s1 offen offset:0 sc1\n"
  "s_waitcnt vmcnt(0)\n"
  "v_xor_b32 v208, v34, v208\n"
  "v_xor_b32 v210, v35, v210\n"
  "v_xor_b32 v212, v34, v212\n"
  "v_xor_b32 v214, v35, v214\n"
  "v_or3_b32 v52, v208, v210, v212\n"
  "v_bitop3_b32 v52, v52, s33, v214 bitop3:0xc8\n"
  "v_cmp_ne_u32_e64 vcc, 0, v52\n"
  "s_cbranch_vccnz Lslow53\n"
  "buffer_load_dwordx4 v[216:219], v174, s[4:7], s1 offen offset:2048 sc1\n"
  "buffer_load_dwordx4 v[220:223], v175, s[4:7], s1 offen offset:2048 sc1\n"
  "buffer_load_dwordx4 v[224:227], v175, s[4:7], s3 offen offset:0 sc1\n"
  "buffer_load_dwordx4 v[228:231], v175, s[4:7], s3 offen offset:2048 sc1\n"
  "buffer_load_dwordx4 v[232:235], v175, s[4:7], s94 offen offset:0 sc1\n"
  "buffer_load_dwordx4 v[236:239], v175, s[4:7], s94 offen offset:2048 sc1\n"
  "buffer_load_dwordx4 v[240:243], v175, s[4:7], s95 offen offset:0 sc1\n"
  "buffer_load_dwordx4 v[244:247], v175, s[4:7], s95 offen offset:2048 sc1\n"
  "buffer_load_dwordx4 v[248:251], v175, s[4:7], s30 offen offset:0 sc1\n"
  "s_waitcnt vmcnt(0)\n"
  "s_branch Lb53\n"
  "Lslow54:\n"
  "s_add_u32 s76, s76, 1\n"
  "s_cmp_gt_u32 s76, 0x200000\n"
  "s_cbranch_scc1 Lb54\n"
  "buffer_load_dwordx4 v[216:219], v174, s[4:7], s1 offen offset:2048 sc1\n"
  "buffer_load_dwordx4 v[220:223], v175, s[4:7], s1 offen offset:2048 sc1\n"
  "s_waitcnt vmcnt(0)\n"
  "v_xor_b32 v216, v34, v216\n"
  "v_xor_b32 v218, v35, v218\n"
  "v_xor_b32 v220, v34, v220\n"
  "v_xor_b32 v222, v35, v222\n"
  "v_or3_b32 v52, v216, v218, v220\n"
  "v_bitop3_b32 v52, v52, s33, v222 bitop3:0xc8\n"
  "v_cmp_ne_u32_e64 vcc, 0, v52\n"
  "s_cbranch_vccnz Lslow54\n"
  "buffer_load_dwordx4 v[224:227], v175, s[4:7], s3 offen offset:0 sc1\n"
  "buffer_load_dwordx4 v[228:231], v175, s[4:7], s3 offen offset:2048 sc1\n"
  "buffer_load_dwordx4 v[232:235], v175, s[4:7], s94 offen offset:0 sc1\n"
  "buffer_load_dwordx4 v[236:239], v175, s[4:7], s94 offen offset:2048 sc1\n"
  "buffer_load_dwordx4 v[240:243], v175, s[4:7], s95 offen offset:0 sc1\n"
  "buffer_load_dwordx4 v[244:247], v175, s[4:7], s95 offen offset:2048 sc1\n"
  "buffer_load_dwordx4 v[248:251], v175, s[4:7], s30 offen offset:0 sc1\n"
  "buffer_load_dwordx4 v[252:255], v175, s[4:7], s30 offen offset:2048 sc1\n"
  "buffer_load_dwordx4 v[192:195], v174, s[4:7], s3 offen offset:0 sc1\n"
  "s_waitcnt vmcnt(0)\n"
  "s_branch Lb54\n"
  "Lslow55:\n"
  "s_add_u32 s76, s76, 1\n"
  "s_cmp_gt_u32 s76, 0x200000\n"
  "s_cbranch_scc1 Lb55\n"
  "buffer_load_dwordx4 v[224:227], v175, s[4:7], s3 offen offset:0 sc1\n"
  "buffer_load_dwordx4 v[228:231], v175, s[4:7], s3 offen offset:2048 sc1\n"
  "s_waitcnt vmcnt(0)\n"
  "v_xor_b32 v224, v34, v224\n"
  "v_xor_b32 v226, v35, v226\n"
  "v_xor_b32 v228, v34, v228\n"
  "v_xor_b32 v230, v35, v230\n"
  "v_or3_b32 v52, v224, v226, v228\n"
  "v_bitop3_b32 v52, v52, s33, v230 bitop3:0xc8\n"
  "v_cmp_ne_u32_e64 vcc, 0, v52\n"
  "s_cbranch_vccnz Lslow55\n"
  "buffer_load_dwordx4 v[232:235], v175, s[4:7], s94 offen offset:0 sc1\n"
  "buffer_load_dwordx4 v[236:239], v175, s[4:7], s94 offen offset:2048 sc1\n"
  "buffer_load_dwordx4 v[240:243], v175, s[4:7], s95 offen offset:0 sc1\n"
  "buffer_load_dwordx4 v[244:247], v175, s[4:7], s95 offen offset:2048 sc1\n"
  "buffer_load_dwordx4 v[248:251], v175, s[4:7], s30 offen offset:0 sc1\n"
  "buffer_load_dwordx4 v[252:255], v175, s[4:7], s30 offen offset:2048 sc1\n"
  "buffer_load_dwordx4 v[192:195], v174, s[4:7], s3 offen offset:0 sc1\n"
  "buffer_load_dwordx4 v[196:199], v174, s[4:7], s3 offen offset:2048 sc1\n"
  "buffer_load_dwordx4 v[200:203], v174, s[4:7], s94 offen offset:0 sc1\n"
  "s_waitcnt vmcnt(0)\n"
  "s_branch Lb55\n"
  "Lslow56:\n"
  "s_add_u32 s76, s76, 1\n"
  "s_cmp_gt_u32 s76, 0x200000\n"
  "s_cbranch_scc1 Lb56\n"
  "buffer_load_dwordx4 v[232:235], v175, s[4:7], s94 offen offset:0 sc1\n"
  "buffer_load_dwordx4 v[236:239], v175, s[4:7], s94 offen offset:2048 sc1\n"
  "s_waitcnt vmcnt(0)\n"
  "v_xor_b32 v232, v34, v232\n"
  "v_xor_b32 v234, v35, v234\n"
  "v_xor_b32 v236, v34, v236\n"
  "v_xor_b32 v238, v35, v238\n"
  "v_or3_b32 v52, v232, v234, v236\n"
  "v_bitop3_b32 v52, v52, s33, v238 bitop3:0xc8\n"
  "v_cmp_ne_u32_e64 vcc, 0, v52\n"
  "s_cbranch_vccnz Lslow56\n"
  "buffer_load_dwordx4 v[240:243], v175, s[4:7], s95 offen offset:0 sc1\n"
  "buffer_load_dwordx4 v[244:247], v175, s[4:7], s95 offen offset:2048 sc1\n"
  "buffer_load_dwordx4 v[248:251], v175, s[4:7], s30 offen offset:0 sc1\n"
  "buffer_load_dwordx4 v[252:255], v175, s[4:7], s30 offen offset:2048 sc1\n"
  "buffer_load_dwordx4 v[192:195], v174, s[4:7], s3 offen offset:0 sc1\n"
  "buffer_load_dwordx4 v[196:199], v174, s[4:7], s3 offen offset:2048 sc1\n"
  "buffer_load_dwordx4 v[200:203], v174, s[4:7], s94 offen offset:0 sc1\n"
  "buffer_load_dwordx4 v[204:207], v174, s[4:7], s94 offen offset:2048 sc1\n"
  "buffer_load_dwordx4 v[208:211], v174, s[4:7], s95 offen offset:0 sc1\n"
  "s_waitcnt vmcnt(0)\n"
  "s_branch Lb56\n"
  "Lslow57:\n"
  "s_add_u32 s76, s76, 1\n"
  "s_cmp_gt_u32 s76, 0x200000\n"
  "s_cbranch_scc1 Lb57\n"
  "buffer_load_dwordx4 v[240:243], v175, s[4:7], s95 offen offset:0 sc1\n"
  "buffer_load_dwordx4 v[244:247], v175, s[4:7], s95 offen offset:2048 sc1\n"
  "s_waitcnt vmcnt(0)\n"
  "v_xor_b32 v240, v34, v240\n"
  "v_xor_b32 v242, v35, v242\n"
  "v_xor_b32 v244, v34, v244\n"
  "v_xor_b32 v246, v35, v246\n"
  "v_or3_b32 v52, v240, v242, v244\n"
  "v_bitop3_b32 v52, v52, s33, v246 bitop3:0xc8\n"
  "v_cmp_ne_u32_e64 vcc, 0, v52\n"
  "s_cbranch_vccnz Lslow57\n"
  "buffer_load_dwordx4 v[248:251], v175, s[4:7], s30 offen offset:0 sc1\n"
  "buffer_load_dwordx4 v[252:255], v175, s[4:7], s30 offen offset:2048 sc1\n"
  "buffer_load_dwordx4 v[192:195], v174, s[4:7], s3 offen offset:0 sc1\n"
  "buffer_load_dwordx4 v[196:199], v174, s[4:7], s3 offen offset:2048 sc1\n"
  "buffer_load_dwordx4 v[200:203], v174, s[4:7], s94 offen offset:0 sc1\n"
  "buffer_load_dwordx4 v[204:207], v174, s[4:7], s94 offen offset:2048 sc1\n"
  "buffer_load_dwordx4 v[208:211], v174, s[4:7], s95 offen offset:0 sc1\n"
  "buffer_load_dwordx4 v[212:215], v174, s[4:7], s95 offen offset:2048 sc1\n"
  "buffer_load_dwordx4 v[216:219], v174, s[4:7], s30 offen offset:0 sc1\n"
  "s_waitcnt vmcnt(0)\n"
  "s_branch Lb57\n"
  "Lslow58:\n"
  "s_add_u32 s76, s76, 1\n"
  "s_cmp_gt_u32 s76, 0x200000\n"
  "s_cbranch_scc1 Lb58\n"
  "buffer_load_dwordx4 v[248:251], v175, s[4:7], s30 offen offset:0 sc1\n"
  "buffer_load_dwordx4 v[252:255], v175, s[4:7], s30 offen offset:2048 sc1\n"
  "s_waitcnt vmcnt(0)\n"
  "v_xor_b32 v248, v34, v248\n"
  "v_xor_b32 v250, v35, v250\n"
  "v_xor_b32 v252, v34, v252\n"
  "v_xor_b32 v254, v35, v254\n"
  "v_or3_b32 v52, v248, v250, v252\n"
  "v_bitop3_b32 v52, v52, s33, v254 bitop3:0xc8\n"
  "v_cmp_ne_u32 vcc, 0, v52\n"
  "s_cbranch_vccnz Lslow58\n"
  "s_branch Lb58\n"
  "Lslow59:\n"
  "s_add_u32 s76, s76, 1\n"
  "s_cmp_gt_u32 s76, 0x200000\n"
  "s_cbranch_scc1 Lb59\n"
  "buffer_load_dwordx4 v[192:195], v174, s[4:7], s3 offen offset:0 sc1\n"
  "buffer_load_dwordx4 v[196:199], v174, s[4:7], s3 offen offset:2048 sc1\n"
  "s_waitcnt vmcnt(0)\n"
  "v_xor_b32 v192, v34, v192\n"
  "v_xor_b32 v194, v35, v194\n"
  "v_xor_b32 v196, v34, v196\n"
  "v_xor_b32 v198, v35, v198\n"
  "v_or3_b32 v52, v192, v194, v196\n"
  "v_bitop3_b32 v52, v52, s33, v198 bitop3:0xc8\n"
  "v_cmp_ne_u32 vcc, 0, v52\n"
  "s_cbranch_vccnz Lslow59\n"
  "s_branch Lb59\n"
  "Lslow60:\n"
  "s_add_u32 s76, s76, 1\n"
  "s_cmp_gt_u32 s76, 0x200000\n"
  "s_cbranch_scc1 Lb60\n"
  "buffer_load_dwordx4 v[200:203], v174, s[4:7], s94 offen offset:0 sc1\n"
  "buffer_load_dwordx4 v[204:207], v174, s[4:7], s94 offen offset:2048 sc1\n"
  "s_waitcnt vmcnt(0)\n"
  "v_xor_b32 v200, v34, v200\n"
  "v_xor_b32 v202, v35, v202\n"
  "v_xor_b32 v204, v34, v204\n"
  "v_xor_b32 v206, v35, v206\n"
  "v_or3_b32 v52, v200, v202, v204\n"
  "v_bitop3_b32 v52, v52, s33, v206 bitop3:0xc8\n"
  "v_cmp_ne_u32 vcc, 0, v52\n"
  "s_cbranch_vccnz Lslow60\n"
  "s_branch Lb60\n"
  "Lslow61:\n"
  "s_add_u32 s76, s76, 1\n"
  "s_cmp_gt_u32 s76, 0x200000\n"
  "s_cbranch_scc1 Lb61\n"
  "buffer_load_dwordx4 v[208:211], v174, s[4:7], s95 offen offset:0 sc1\n"
  "buffer_load_dwordx4 v[212:215], v174, s[4:7], s95 offen offset:2048 sc1\n"
  "s_waitcnt vmcnt(0)\n"
  "v_xor_b32 v208, v34, v208\n"
  "v_xor_b32 v210, v35, v210\n"
  "v_xor_b32 v212, v34, v212\n"
  "v_xor_b32 v214, v35, v214\n"
  "v_or3_b32 v52, v208, v210, v212\n"
  "v_bitop3_b32 v52, v52, s33, v214 bitop3:0xc8\n"
  "v_cmp_ne_u32 vcc, 0, v52\n"
  "s_cbranch_vccnz Lslow61\n"
  "s_branch Lb61\n"
  "Lslow62:\n"
  "s_add_u32 s76, s76, 1\n"
  "s_cmp_gt_u32 s76, 0x200000\n"
  "s_cbranch_scc1 Lb62\n"
  "buffer_load_dwordx4 v[216:219], v174, s[4:7], s30 offen offset:0 sc1\n"
  "buffer_load_dwordx4 v[220:223], v174, s[4:7], s30 offen offset:2048 sc1\n"
  "s_waitcnt vmcnt(0)\n"
  "v_xor_b32 v216, v34, v216\n"
  "v_xor_b32 v218, v35, v218\n"
  "v_xor_b32 v220, v34, v220\n"
  "v_xor_b32 v222, v35, v222\n"
  "v_or3_b32 v52, v216, v218, v220\n"
  "v_bitop3_b32 v52, v52, s33, v222 bitop3:0xc8\n"
  "v_cmp_ne_u32 vcc, 0, v52\n"
  "s_cbranch_vccnz Lslow62\n"
  "s_branch Lb62\n"
  "Lslow63:\n"
  "s_add_u32 s76, s76, 1\n"
  "s_cmp_gt_u32 s76, 0x200000\n"
  "s_cbranch_scc1 LbackYi\n"
  "buffer_load_dwordx4 v[64:67], v52, s[4:7], 0 offen sc1\n"
  "buffer_load_dwordx4 v[68:71], v52, s[4:7], 0 offen offset:512 sc1\n"
  "s_waitcnt vmcnt(0)\n"
  "v_xor_b32 v68, v4, v68\n"
  "v_xor_b32 v69, v5, v69\n"
  "v_xor_b32 v70, v6, v70\n"
  "v_xor_b32 v71, v7, v71\n"
  "v_xor_b32 v64, v4, v64\n"
  "v_xor_b32 v65, v5, v65\n"
  "v_xor_b32 v66, v6, v66\n"
  "v_xor_b32_e64 v67, v7, v67\n"
  "v_or3_b32 v58, v68, v69, v70\n"
  "v_or3_b32 v58, v58, v71, v64\n"
  "v_or3_b32 v58, v58, v65, v66\n"
  "v_bitop3_b32 v58, v58, s33, v67 bitop3:0xc8\n"
  "v_cmp_ne_u32 vcc, 0, v58\n"
  "s_cbranch_vccnz Lslow63\n"
  "s_branch LbackYi\n"
  "Lslow64:\n"
  "s_add_u32 s76, s76, 1\n"
  "s_cmp_gt_u32 s76, 0x200000\n"
  "s_cbranch_scc1 LbackI\n"
  "s_mov_b64 exec, vcc\n"
  "s_add_i32 s87, s73, 0\n"
  "buffer_load_dword v52, v1, s[4:7], s87 offen sc1\n"
  "s_waitcnt vmcnt(0)\n"
  "v_xor_b32 v52, v4, v52\n"
  "v_and_b32 v52, s33, v52\n"
  "v_cmp_ne_u32 vcc, 0, v52\n"
  "s_mov_b64 exec, -1\n"
  "s_cbranch_vccnz Lslow64\n"
  "s_branch LbackI\n"
  "Lslow65:\n"
  "s_add_u32 s76, s76, 1\n"
  "s_cmp_gt_u32 s76, 0x200000\n"
  "s_cbranch_scc1 LbackHi\n"
  "buffer_load_dwordx4 v[36:39], v168, s[4:7], s74 offen sc1\n"
  "buffer_load_dwordx4 v[40:43], v170, s[4:7], s74 offen sc1\n"
  "buffer_load_dwordx4 v[176:179], v32, s[4:7], s50 offen offset:0 sc1\n"
  "buffer_load_dwordx4 v[180:183], v32, s[4:7], s50 offen offset:256 sc1\n"
  "s_waitcnt vmcnt(0)\n"
  "v_xor_b32 v183, s49, v183\n"
  "v_xor_b32 v181, s48, v181\n"
  "v_xor_b32 v179, s49, v179\n"
  "v_xor_b32 v177, s48, v177\n"
  "v_xor_b32 v40, v34, v40\n"
  "v_xor_b32 v42, v35, v42\n"
  "v_xor_b32 v36, v34, v36\n"
  "v_xor_b32_e64 v38, v35, v38\n"
  "v_or3_b32 v52, v40, v42, v36\n"
  "v_bitop3_b32 v52, v52, s33, v38 bitop3:0xc8\n"
  "v_or3_b32 v52, v52, v177, v179\n"
  "v_or3_b32 v52, v52, v181, v183\n"
  "v_cmp_ne_u32 vcc, 0, v52\n"
  "s_cbranch_vccnz Lslow65\n"
  "s_branch LbackHi\n"
  "Lslow66:\n"
  "s_add_u32 s76, s76, 1\n"
  "s_cmp_gt_u32 s76, 0x200000\n"
  "s_cbranch_scc1 Lb66\n"
  "buffer_load_dwordx4 v[192:195], v172, s[4:7], s73 offen offset:0 sc1\n"
  "buffer_load_dwordx4 v[196:199], v173, s[4:7], s73 offen offset:0 sc1\n"
  "s_waitcnt vmcnt(0)\n"
  "v_xor_b32 v192, v34, v192\n"
  "v_xor_b32 v194, v35, v194\n"
  "v_xor_b32 v196, v34, v196\n"
  "v_xor_b32 v198, v35, v198\n"
  "v_or3_b32 v52, v192, v194, v196\n"
  "v_bitop3_b32 v52, v52, s33, v198 bitop3:0xc8\n"
  "v_cmp_ne_u32 vcc, 0, v52\n"
  "s_cbranch_vccnz Lslow66\n"
  "buffer_load_dwordx4 v[200:203], v172, s[4:7], s73 offen offset:2048 sc1\n"
  "buffer_load_dwordx4 v[204:207], v173, s[4:7], s73 offen offset:2048 sc1\n"
  "buffer_load_dwordx4 v[208:211], v172, s[4:7], s0 offen offset:0 sc1\n"
  "buffer_load_dwordx4 v[212:215], v173, s[4:7], s0 offen offset:0 sc1\n"
  "buffer_load_dwordx4 v[216:219], v172, s[4:7], s0 offen offset:2048 sc1\n"
  "buffer_load_dwordx4 v[220:223], v173, s[4:7], s0 offen offset:2048 sc1\n"
  "s_waitcnt vmcnt(0)\n"
  "s_branch Lb66\n"
  "Lslow67:\n"
  "s_add_u32 s76, s76, 1\n"
  "s_cmp_gt_u32 s76, 0x200000\n"
  "s_cbranch_scc1 Lb67\n"
  "buffer_load_dwordx4 v[200:203], v172, s[4:7], s73 offen offset:2048 sc1\n"
  "buffer_load_dwordx4 v[204:207], v173, s[4:7], s73 offen offset:2048 sc1\n"
  "s_waitcnt vmcnt(0)\n"
  "v_xor_b32 v200, v34, v200\n"
  "v_xor_b32 v202, v35, v202\n"
  "v_xor_b32 v204, v34, v204\n"
  "v_xor_b32 v206, v35, v206\n"
  "v_or3_b32 v52, v200, v202, v204\n"
  "v_bitop3_b32 v52, v52, s33, v206 bitop3:0xc8\n"
  "v_cmp_ne_u32_e64 vcc, 0, v52\n"
  "s_cbranch_vccnz Lslow67\n"
  "buffer_load_dwordx4 v[208:211], v172, s[4:7], s0 offen offset:0 sc1\n"
  "buffer_load_dwordx4 v[212:215], v173, s[4:7], s0 offen offset:0 sc1\n"
  "buffer_load_dwordx4 v[216:219], v172, s[4:7], s0 offen offset:2048 sc1\n"
  "buffer_load_dwordx4 v[220:223], v173, s[4:7], s0 offen offset:2048 sc1\n"
  "buffer_load_dwordx4 v[224:227], v172, s[4:7], s1 offen offset:0 sc1\n"
  "buffer_load_dwordx4 v[228:231], v173, s[4:7], s1 offen offset:0 sc1\n"
  "buffer_load_dwordx4 v[232:235], v172, s[4:7], s1 offen offset:2048 sc1\n"
  "buffer_load_dwordx4 v[236:239], v173, s[4:7], s1 offen offset:2048 sc1\n"
  "s_waitcnt vmcnt(0)\n"
  "s_branch Lb67\n"
  "Lslow68:\n"
  "s_add_u32 s76, s76, 1\n"
  "s_cmp_gt_u32 s76, 0x200000\n"
  "s_cbranch_scc1 Lb68\n"
  "buffer_load_dwordx4 v[208:211], v172, s[4:7], s0 offen offset:0 sc1\n"
  "buffer_load_dwordx4 v[212:215], v173, s[4:7], s0 offen offset:0 sc1\n"
  "s_waitcnt vmcnt(0)\n"
  "v_xor_b32 v208, v34, v208\n"
  "v_xor_b32 v210, v35, v210\n"
  "v_xor_b32 v212, v34, v212\n"
  "v_xor_b32 v214, v35, v214\n"
  "v_or3_b32 v52, v208, v210, v212\n"
  "v_bitop3_b32 v52, v52, s33, v214 bitop3:0xc8\n"
  "v_cmp_ne_u32_e64 vcc, 0, v52\n"
  "s_cbranch_vccnz Lslow68\n"
  "buffer_load_dwordx4 v[216:219], v172, s[4:7], s0 offen offset:2048 sc1\n"
  "buffer_load_dwordx4 v[220:223], v173, s[4:7], s0 offen offset:2048 sc1\n"
  "buffer_load_dwordx4 v[224:227], v172, s[4:7], s1 offen offset:0 sc1\n"
  "buffer_load_dwordx4 v[228:231], v173, s[4:7], s1 offen offset:0 sc1\n"
  "buffer_load_dwordx4 v[232:235], v172, s[4:7], s1 offen offset:2048 sc1\n"
  "buffer_load_dwordx4 v[236:239], v173, s[4:7], s1 offen offset:2048 sc1\n"
  "buffer_load_dwordx4 v[240:243], v172, s[4:7], s3 offen offset:0 sc1\n"
  "buffer_load_dwordx4 v[244:247], v173, s[4:7], s3 offen offset:0 sc1\n"
  "buffer_load_dwordx4 v[248:251], v172, s[4:7], s3 offen offset:2048 sc1\n"
  "s_waitcnt vmcnt(0)\n"
  "s_branch Lb68\n"
  "Lslow69:\n"
  "s_add_u32 s76, s76, 1\n"
  "s_cmp_gt_u32 s76, 0x200000\n"
  "s_cbranch_scc1 Lb69\n"
  "buffer_load_dwordx4 v[216:219], v172, s[4:7], s0 offen offset:2048 sc1\n"
  "buffer_load_dwordx4 v[220:223], v173, s[4:7], s0 offen offset:2048 sc1\n"
  "s_waitcnt vmcnt(0)\n"
  "v_xor_b32 v216, v34, v216\n"
  "v_xor_b32 v218, v35, v218\n"
  "v_xor_b32 v220, v34, v220\n"
  "v_xor_b32 v222, v35, v222\n"
  "v_or3_b32 v52, v216, v218, v220\n"
  "v_bitop3_b32 v52, v52, s33, v222 bitop3:0xc8\n"
  "v_cmp_ne_u32_e64 vcc, 0, v52\n"
  "s_cbranch_vccnz Lslow69\n"
  "buffer_load_dwordx4 v[224:227], v172, s[4:7], s1 offen offset:0 sc1\n"
  "buffer_load_dwordx4 v[228:231], v173, s[4:7], s1 offen offset:0 sc1\n"
  "buffer_load_dwordx4 v[232:235], v172, s[4:7], s1 offen offset:2048 sc1\n"
  "buffer_load_dwordx4 v[236:239], v173, s[4:7], s1 offen offset:2048 sc1\n"
  "buffer_load_dwordx4 v[240:243], v172, s[4:7], s3 offen offset:0 sc1\n"
  "buffer_load_dwordx4 v[244:247], v173, s[4:7], s3 offen offset:0 sc1\n"
  "buffer_load_dwordx4 v[248:251], v172, s[4:7], s3 offen offset:2048 sc1\n"
  "buffer_load_dwordx4 v[252:255], v173, s[4:7], s3 offen offset:2048 sc1\n"
  "s_waitcnt vmcnt(0)\n"
  "s_branch Lb69\n"
  "Lslow70:\n"
  "s_add_u32 s76, s76, 1\n"
  "s_cmp_gt_u32 s76, 0x200000\n"
  "s_cbranch_scc1 Lb70\n"
  "buffer_load_dwordx4 v[224:227], v172, s[4:7], s1 offen offset:0 sc1\n"
  "buffer_load_dwordx4 v[228:231], v173, s[4:7], s1 offen offset:0 sc1\n"
  "s_waitcnt vmcnt(0)\n"
  "v_xor_b32 v224, v34, v224\n"
  "v_xor_b32 v226, v35, v226\n"
  "v_xor_b32 v228, v34, v228\n"
  "v_xor_b32 v230, v35, v230\n"
  "v_or3_b32 v52, v224, v226, v228\n"
  "v_bitop3_b32 v52, v52, s33, v230 bitop3:0xc8\n"
  "v_cmp_ne_u32_e64 vcc, 0, v52\n"
  "s_cbranch_vccnz Lslow70\n"
  "buffer_load_dwordx4 v[232:235], v172, s[4:7], s1 offen offset:2048 sc1\n"
  "buffer_load_dwordx4 v[236:239], v173, s[4:7], s1 offen offset:2048 sc1\n"
  "buffer_load_dwordx4 v[240:243], v172, s[4:7], s3 offen offset:0 sc1\n"
  "buffer_load_dwordx4 v[244:247], v173, s[4:7], s3 offen offset:0 sc1\n"
  "buffer_load_dwordx4 v[248:251], v172, s[4:7], s3 offen offset:2048 sc1\n"
  "buffer_load_dwordx4 v[252:255], v173, s[4:7], s3 offen offset:2048 sc1\n"
  "buffer_load_dwordx4 v[192:195], v172, s[4:7], s94 offen offset:0 sc1\n"
  "buffer_load_dwordx4 v[196:199], v173, s[4:7], s94 offen offset:0 sc1\n"
  "s_waitcnt vmcnt(0)\n"
  "s_branch Lb70\n"
  "Lslow71:\n"
  "s_add_u32 s76, s76, 1\n"
  "s_cmp_gt_u32 s76, 0x200000\n"
  "s_cbranch_scc1 Lb71\n"
  "buffer_load_dwordx4 v[232:235], v172, s[4:7], s1 offen offset:2048 sc1\n"
  "buffer_load_dwordx4 v[236:239], v173, s[4:7], s1 offen offset:2048 sc1\n"
  "s_waitcnt vmcnt(0)\n"
  "v_xor_b32 v232, v34, v232\n"
  "v_xor_b32 v234, v35, v234\n"
  "v_xor_b32 v236, v34, v236\n"
  "v_xor_b32 v238, v35, v238\n"
  "v_or3_b32 v52, v232, v234, v236\n"
  "v_bitop3_b32 v52, v52, s33, v238 bitop3:0xc8\n"
  "v_cmp_ne_u32_e64 vcc, 0, v52\n"
  "s_cbranch_vccnz Lslow71\n"
  "buffer_load_dwordx4 v[240:243], v172, s[4:7], s3 offen offset:0 sc1\n"
  "buffer_load_dwordx4 v[244:247], v173, s[4:7], s3 offen offset:0 sc1\n"
  "buffer_load_dwordx4 v[248:251], v172, s[4:7], s3 offen offset:2048 sc1\n"
  "buffer_load_dwordx4 v[252:255], v173, s[4:7], s3 offen offset:2048 sc1\n"
  "buffer_load_dwordx4 v[192:195], v172, s[4:7], s94 offen offset:0 sc1\n"
  "buffer_load_dwordx4 v[196:199], v173, s[4:7], s94 offen offset:0 sc1\n"
  "buffer_load_dwordx4 v[200:203], v172, s[4:7], s94 offen offset:2048 sc1\n"
  "buffer_load_dwordx4 v[204:207], v173, s[4:7], s94 offen offset:2048 sc1\n"
  "s_waitcnt vmcnt(0)\n"
  "s_branch Lb71\n"
  "Lslow72:\n"
  "s_add_u32 s76, s76, 1\n"
  "s_cmp_gt_u32 s76, 0x200000\n"
  "s_cbranch_scc1 Lb72\n"
  "buffer_load_dwordx4 v[240:243], v172, s[4:7], s3 offen offset:0 sc1\n"
  "buffer_load_dwordx4 v[244:247], v173, s[4:7], s3 offen offset:0 sc1\n"
  "s_waitcnt vmcnt(0)\n"
  "v_xor_b32 v240, v34, v240\n"
  "v_xor_b32 v242, v35, v242\n"
  "v_xor_b32 v244, v34, v244\n"
  "v_xor_b32 v246, v35, v246\n"
  "v_or3_b32 v52, v240, v242, v244\n"
  "v_bitop3_b32 v52, v52, s33, v246 bitop3:0xc8\n"
  "v_cmp_ne_u32_e64 vcc, 0, v52\n"
  "s_cbranch_vccnz Lslow72\n"
  "buffer_load_dwordx4 v[248:251], v172, s[4:7], s3 offen offset:2048 sc1\n"
  "buffer_load_dwordx4 v[252:255], v173, s[4:7], s3 offen offset:2048 sc1\n"
  "buffer_load_dwordx4 v[192:195], v172, s[4:7], s94 offen offset:0 sc1\n"
  "buffer_load_dwordx4 v[196:199], v173, s[4:7], s94 offen offset:0 sc1\n"
  "buffer_load_dwordx4 v[200:203], v172, s[4:7], s94 offen offset:2048 sc1\n"
  "buffer_load_dwordx4 v[204:207], v173, s[4:7], s94 offen offset:2048 sc1\n"
  "buffer_load_dwordx4 v[208:211], v172, s[4:7], s95 offen offset:0 sc1\n"
  "buffer_load_dwordx4 v[212:215], v173, s[4:7], s95 offen offset:0 sc1\n"
  "s_waitcnt vmcnt(0)\n"
  "s_branch Lb72\n"
  "Lslow73:\n"
  "s_add_u32 s76, s76, 1\n"
  "s_cmp_gt_u32 s76, 0x200000\n"
  "s_cbranch_scc1 Lb73\n"
  "buffer_load_dwordx4 v[248:251], v172, s[4:7], s3 offen offset:2048 sc1\n"
  "buffer_load_dwordx4 v[252:255], v173, s[4:7], s3 offen offset:2048 sc1\n"
  "s_waitcnt vmcnt(0)\n"
  "v_xor_b32 v248, v34, v248\n"
  "v_xor_b32 v250, v35, v250\n"
  "v_xor_b32 v252, v34, v252\n"
  "v_xor_b32 v254, v35, v254\n"
  "v_or3_b32 v52, v248, v250, v252\n"
  "v_bitop3_b32 v52, v52, s33, v254 bitop3:0xc8\n"
  "v_cmp_ne_u32_e64 vcc, 0, v52\n"
  "s_cbranch_vccnz Lslow73\n"
  "buffer_load_dwordx4 v[192:195], v172, s[4:7], s94 offen offset:0 sc1\n"
  "buffer_load_dwordx4 v[196:199], v173, s[4:7], s94 offen offset:0 sc1\n"
  "buffer_load_dwordx4 v[200:203], v172, s[4:7], s94 offen offset:2048 sc1\n"
  "buffer_load_dwordx4 v[204:207], v173, s[4:7], s94 offen offset:2048 sc1\n"
  "buffer_load_dwordx4 v[208:211], v172, s[4:7], s95 offen offset:0 sc1\n"
  "buffer_load_dwordx4 v[212:215], v173, s[4:7], s95 offen offset:0 sc1\n"
  "buffer_load_dwordx4 v[216:219], v172, s[4:7], s95 offen offset:2048 sc1\n"
  "buffer_load_dwordx4 v[220:223], v173, s[4:7], s95 offen offset:2048 sc1\n"
  "s_waitcnt vmcnt(0)\n"
  "s_branch Lb73\n"
  "Lslow74:\n"
  "s_add_u32 s76, s76, 1\n"
  "s_cmp_gt_u32 s76, 0x200000\n"
  "s_cbranch_scc1 Lb74\n"
  "buffer_load_dwordx4 v[192:195], v172, s[4:7], s94 offen offset:0 sc1\n"
  "buffer_load_dwordx4 v[196:199], v173, s[4:7], s94 offen offset:0 sc1\n"
  "s_waitcnt vmcnt(0)\n"
  "v_xor_b32 v192, v34, v192\n"
  "v_xor_b32 v194, v35, v194\n"
  "v_xor_b32 v196, v34, v196\n"
  "v_xor_b32 v198, v35, v198\n"
  "v_or3_b32 v52, v192, v194, v196\n"
  "v_bitop3_b32 v52, v52, s33, v198 bitop3:0xc8\n"
  "v_cmp_ne_u32_e64 vcc, 0, v52\n"
  "s_cbranch_vccnz Lslow74\n"
  "buffer_load_dwordx4 v[200:203], v172, s[4:7], s94 offen offset:2048 sc1\n"
  "buffer_load_dwordx4 v[204:207], v173, s[4:7], s94 offen offset:2048 sc1\n"
  "buffer_load_dwordx4 v[208:211], v172, s[4:7], s95 offen offset:0 sc1\n"
  "buffer_load_dwordx4 v[212:215], v173, s[4:7], s95 offen offset:0 sc1\n"
  "buffer_load_dwordx4 v[216:219], v172, s[4:7], s95 offen offset:2048 sc1\n"
  "buffer_load_dwordx4 v[220:223], v173, s[4:7], s95 offen offset:2048 sc1\n"
  "buffer_load_dwordx4 v[224:227], v172, s[4:7], s30 offen offset:0 sc1\n"
  "buffer_load_dwordx4 v[228:231], v173, s[4:7], s30 offen offset:0 sc1\n"
  "s_waitcnt vmcnt(0)\n"
  "s_branch Lb74\n"
  "Lslow75:\n"
  "s_add_u32 s76, s76, 1\n"
  "s_cmp_gt_u32 s76, 0x200000\n"
  "s_cbranch_scc1 Lb75\n"
  "buffer_load_dwordx4 v[200:203], v172, s[4:7], s94 offen offset:2048 sc1\n"
  "buffer_load_dwordx4 v[204:207], v173, s[4:7], s94 offen offset:2048 sc1\n"
  "s_waitcnt vmcnt(0)\n"
  "v_xor_b32 v200, v34, v200\n"
  "v_xor_b32 v202, v35, v202\n"
  "v_xor_b32 v204, v34, v204\n"
  "v_xor_b32 v206, v35, v206\n"
  "v_or3_b32 v52, v200, v202, v204\n"
  "v_bitop3_b32 v52, v52, s33, v206 bitop3:0xc8\n"
  "v_cmp_ne_u32 vcc, 0, v52\n"
  "s_cbranch_vccnz Lslow75\n"
  "s_branch Lb75\n"
  "Lslow76:\n"
  "s_add_u32 s76, s76, 1\n"
  "s_cmp_gt_u32 s76, 0x200000\n"
  "s_cbranch_scc1 Lb76\n"
  "buffer_load_dwordx4 v[208:211], v172, s[4:7], s95 offen offset:0 sc1\n"
  "buffer_load_dwordx4 v[212:215], v173, s[4:7], s95 offen offset:0 sc1\n"
  "s_waitcnt vmcnt(0)\n"
  "v_xor_b32 v208, v34, v208\n"
  "v_xor_b32 v210, v35, v210\n"
  "v_xor_b32 v212, v34, v212\n"
  "v_xor_b32 v214, v35, v214\n"
  "v_or3_b32 v52, v208, v210, v212\n"
  "v_bitop3_b32 v52, v52, s33, v214 bitop3:0xc8\n"
  "v_cmp_ne_u32 vcc, 0, v52\n"
  "s_cbranch_vccnz Lslow76\n"
  "s_branch Lb76\n"
  "Lslow77:\n"
  "s_add_u32 s76, s76, 1\n"
  "s_cmp_gt_u32 s76, 0x200000\n"
  "s_cbranch_scc1 Lb77\n"
  "buffer_load_dwordx4 v[216:219], v172, s[4:7], s95 offen offset:2048 sc1\n"
  "buffer_load_dwordx4 v[220:223], v173, s[4:7], s95 offen offset:2048 sc1\n"
  "s_waitcnt vmcnt(0)\n"
  "v_xor_b32 v216, v34, v216\n"
  "v_xor_b32 v218, v35, v218\n"
  "v_xor_b32 v220, v34, v220\n"
  "v_xor_b32 v222, v35, v222\n"
  "v_or3_b32 v52, v216, v218, v220\n"
  "v_bitop3_b32 v52, v52, s33, v222 bitop3:0xc8\n"
  "v_cmp_ne_u32 vcc, 0, v52\n"
  "s_cbranch_vccnz Lslow77\n"
  "s_branch Lb77\n"
  "Lslow78:\n"
  "s_add_u32 s76, s76, 1\n"
  "s_cmp_gt_u32 s76, 0x200000\n"
  "s_cbranch_scc1 Lb78\n"
  "buffer_load_dwordx4 v[224:227], v172, s[4:7], s30 offen offset:0 sc1\n"
  "buffer_load_dwordx4 v[228:231], v173, s[4:7], s30 offen offset:0 sc1\n"
  "s_waitcnt vmcnt(0)\n"
  "v_xor_b32 v224, v34, v224\n"
  "v_xor_b32 v226, v35, v226\n"
  "v_xor_b32 v228, v34, v228\n"
  "v_xor_b32 v230, v35, v230\n"
  "v_or3_b32 v52, v224, v226, v228\n"
  "v_bitop3_b32 v52, v52, s33, v230 bitop3:0xc8\n"
  "v_cmp_ne_u32 vcc, 0, v52\n"
  "s_cbranch_vccnz Lslow78\n"
  "s_branch Lb78\n"
  "Lslow79:\n"
  "s_add_u32 s76, s76, 1\n"
  "s_cmp_gt_u32 s76, 0x200000\n"
  "s_cbranch_scc1 Lb79\n"
  "buffer_load_dwordx4 v[232:235], v172, s[4:7], s30 offen offset:2048 sc1\n"
  "buffer_load_dwordx4 v[236:239], v173, s[4:7], s30 offen offset:2048 sc1\n"
  "s_waitcnt vmcnt(0)\n"
  "v_xor_b32 v232, v34, v232\n"
  "v_xor_b32 v234, v35, v234\n"
  "v_xor_b32 v236, v34, v236\n"
  "v_xor_b32 v238, v35, v238\n"
  "v_or3_b32 v52, v232, v234, v236\n"
  "v_bitop3_b32 v52, v52, s33, v238 bitop3:0xc8\n"
  "v_cmp_ne_u32 vcc, 0, v52\n"
  "s_cbranch_vccnz Lslow79\n"
  "s_branch Lb79\n"
  "Lslow80:\n"
  "s_add_u32 s76, s76, 1\n"
  "s_cmp_gt_u32 s76, 0x200000\n"
  "s_cbranch_scc1 Lb80\n"
  "buffer_load_dwordx4 v[240:243], v174, s[4:7], s73 offen offset:0 sc1\n"
  "buffer_load_dwordx4 v[244:247], v175, s[4:7], s73 offen offset:0 sc1\n"
  "s_waitcnt vmcnt(0)\n"
  "v_xor_b32 v240, v34, v240\n"
  "v_xor_b32 v242, v35, v242\n"
  "v_xor_b32 v244, v34, v244\n"
  "v_xor_b32 v246, v35, v246\n"
  "v_or3_b32 v52, v240, v242, v244\n"
  "v_bitop3_b32 v52, v52, s33, v246 bitop3:0xc8\n"
  "v_cmp_ne_u32_e64 vcc, 0, v52\n"
  "s_cbranch_vccnz Lslow80\n"
  "buffer_load_dwordx4 v[248:251], v174, s[4:7], s73 offen offset:2048 sc1\n"
  "buffer_load_dwordx4 v[252:255], v175, s[4:7], s73 offen offset:2048 sc1\n"
  "buffer_load_dwordx4 v[192:195], v174, s[4:7], s0 offen offset:0 sc1\n"
  "buffer_load_dwordx4 v[196:199], v175, s[4:7], s0 offen offset:0 sc1\n"
  "buffer_load_dwordx4 v[200:203], v174, s[4:7], s0 offen offset:2048 sc1\n"
  "buffer_load_dwordx4 v[204:207], v175, s[4:7], s0 offen offset:2048 sc1\n"
  "buffer_load_dwordx4 v[208:211], v174, s[4:7], s1 offen offset:0 sc1\n"
  "buffer_load_dwordx4 v[212:215], v175, s[4:7], s1 offen offset:0 sc1\n"
  "buffer_load_dwordx4 v[216:219], v174, s[4:7], s1 offen offset:2048 sc1\n"
  "buffer_load_dwordx4 v[220:223], v175, s[4:7], s1 offen offset:2048 sc1\n"
  "buffer_load_dwordx4 v[224:227], v175, s[4:7], s3 offen offset:0 sc1\n"
  "buffer_load_dwordx4 v[228:231], v175, s[4:7], s3 offen offset:2048 sc1\n"
  "s_waitcnt vmcnt(0)\n"
  "s_branch Lb80\n"
  :
  : "{s[0:1]}"(__builtin_amdgcn_kernarg_segment_ptr()), "{s2}"(blockIdx.x), "{v0}"(threadIdx.x),
    "{s3}"((unsigned)(size_t)(__attribute__((address_space(3))) char*)lds)
  : "memory", "vcc", "s99", "v255", "a255");
#endif
}

extern "C" void kernel_launch(void* const* d_in, const int* in_sizes, int n_in, void* d_out, int out_size, void* d_ws, size_t ws_size,
                              hipStream_t stream) {
  const unsigned F = 0x00020000u;
  s4_mixer_kernel<<<256, 256, 0, stream>>>(d_ws, 0xc90000u, F, d_in[10], 2048u * 1792u * 2u, F, d_in[12], 2048u * 1792u * 2u, F, d_in[0], d_in[1],
                                           d_in[2], d_in[3], d_in[4], d_in[5], d_in[6], d_in[7], d_in[8], d_in[13], d_in[11], 1792u * 2u, F, d_out,
                                           2048u * 512u * 2u, F);
}
```
